```python
import torch

def _softmax(x, axis=-1):
    return torch.softmax(x, dim=axis)
def _gelu(x):
    return torch.nn.functional.gelu(x, approximate="tanh")
def _silu(x):
    return torch.nn.functional.silu(x)
def _sigmoid(x):
    return torch.sigmoid(x)
def _layernorm(x, axis=-1, eps=1e-5):
    m = x.mean(dim=axis, keepdim=True)
    v = x.var(dim=axis, keepdim=True, unbiased=False)
    return (x - m) / torch.sqrt(v + eps)
def _scatter_add(tgt, idx, src, axis=0):
    return tgt.clone().index_add_(axis, idx, src)
def _segment_sum(src, seg, n):
    out = torch.zeros((n,) + src.shape[1:], dtype=src.dtype)
    return out.index_add_(0, seg, src)
def _pad(x, pw):
    flat = tuple(v for pair in reversed(pw) for v in pair)
    return torch.nn.functional.pad(x, flat)

kernel_name = 'interleave_layers__softmax_full_mha__glu_gated_reglu__prompt'

def setup_inputs():

    _w = (lambda a: (lambda t: t.to(torch.bfloat16) if t.is_floating_point() else t)(torch.as_tensor(a)))
    def _A_setup():
        B, S, D = 1, 2048, 512
        return {
            "x": _w((lambda *s: torch.randn(*s, dtype=torch.bfloat16) * 0.05)(B, S, D)),
            "wqkv": _w((lambda *s: torch.randn(*s, dtype=torch.bfloat16) * 0.05)(D, 3 * D)),
            "wo": _w((lambda *s: torch.randn(*s, dtype=torch.bfloat16) * 0.05)(D, D)),
        }
    _A_in = _A_setup()

    def _B_setup():
        B, S, D, F = 1, 2048, 512, 1792
        return {
            "x": _w((lambda *s: torch.randn(*s, dtype=torch.bfloat16) * 0.05)(B, S, D)),
            "W_gate": _w((lambda *s: torch.randn(*s, dtype=torch.bfloat16) * 0.4)(D, F) * (2.0 / D) ** 0.5),
            "W_up": _w((lambda *s: torch.randn(*s, dtype=torch.bfloat16) * 0.4)(D, F) * (2.0 / D) ** 0.5),
            "W_down": _w((lambda *s: torch.randn(*s, dtype=torch.bfloat16) * 0.4)(F, D) * (1.0 / F) ** 0.5),
        }
    _B_in = _B_setup()

    return {
        "A_x": _A_in["x"],
        "A_wqkv": _A_in["wqkv"],
        "A_wo": _A_in["wo"],
        "B_x": _B_in["x"],
        "B_W_gate": _B_in["W_gate"],
        "B_W_up": _B_in["W_up"],
        "B_W_down": _B_in["W_down"],
    }

def reference(A_x, A_wqkv, A_wo, B_x, B_W_gate, B_W_up, B_W_down):
    def _A_ref(x, wqkv, wo):
        B, S, D = x.shape
        H = 32; Dh = D // H
        qkv = (lambda x, *d: x.permute(*d))((x @ wqkv).reshape(B, S, 3, H, Dh), 2, 0, 3, 1, 4)
        q, k, v = qkv[0], qkv[1], qkv[2]
        scale = Dh ** -0.5
        scores = torch.einsum("bhqd,bhkd->bhqk", q, k) * scale
        p = _softmax(scores, axis=-1)
        o = torch.einsum("bhqk,bhkd->bhqd", p, v)
        return (lambda x, *d: x.permute(*d))(o, 0, 2, 1, 3).reshape(B, S, D) @ wo

    def _B_ref(x, W_gate, W_up, W_down):
        gate = torch.einsum("bsd,df->bsf", x, W_gate)
        up = torch.einsum("bsd,df->bsf", x, W_up)
        gate = gate * (lambda x, dt: x.to(dt))(gate > 0, gate.dtype)
        h = gate * up
        return torch.einsum("bsf,fd->bsd", h, W_down)

    x = A_x
    x = x + _A_ref(_layernorm(x, axis=-1), A_wqkv, A_wo)
    x = x + _B_ref(_layernorm(x, axis=-1), B_W_gate, B_W_up, B_W_down)
    x = x + _A_ref(_layernorm(x, axis=-1), A_wqkv, A_wo)
    x = x + _B_ref(_layernorm(x, axis=-1), B_W_gate, B_W_up, B_W_down)
    return x
```

```cpp
#include <hip/hip_runtime.h>
#include <hip/hip_bf16.h>
#include <stdint.h>
#include <utility>

typedef short bf16x8 __attribute__((ext_vector_type(8)));
typedef short s16x4 __attribute__((ext_vector_type(4)));
typedef float f32x16 __attribute__((ext_vector_type(16)));
typedef float f32x4 __attribute__((ext_vector_type(4)));
typedef float f32x2 __attribute__((ext_vector_type(2)));
typedef unsigned u32x4 __attribute__((ext_vector_type(4)));
typedef __bf16 bf16x2_t __attribute__((ext_vector_type(2)));
#define LAS __attribute__((address_space(3)))
#define DI __device__ __forceinline__
#define MFMA32(a, b, c) __builtin_amdgcn_mfma_f32_32x32x16_bf16((a), (b), (c), 0, 0, 0)

constexpr int SEQ = 2048, DM = 512, NH = 32, FF = 1792;
constexpr float LN_EPS = 1e-5f;
constexpr float QSCALE = 0.25f * 1.4426950408889634f;

template <int... I, class Fn> DI void static_for_impl(std::integer_sequence<int, I...>, Fn&& f) { (f(std::integral_constant<int, I>{}), ...); }
template <int N, class Fn> DI void static_for(Fn&& f) { static_for_impl(std::make_integer_sequence<int, N>{}, f); }

DI unsigned pack_bf16(float lo, float hi) { bf16x2_t p; p.x = (__bf16)lo; p.y = (__bf16)hi; return __builtin_bit_cast(unsigned, p); }
DI float bf_lo(unsigned u) { return __uint_as_float(u << 16); }
DI float bf_hi(unsigned u) { return __uint_as_float(u & 0xffff0000u); }
DI float dot2(unsigned a, unsigned b, float c) { return __builtin_amdgcn_fdot2_f32_bf16(__builtin_bit_cast(bf16x2_t, a), __builtin_bit_cast(bf16x2_t, b), c, false); }
DI float half_sum(float v) {
    auto r = __builtin_amdgcn_permlane32_swap(__float_as_uint(v), __float_as_uint(v), false, false);
    return __uint_as_float(r[0]) + __uint_as_float(r[1]);
}
DI float half_max(float v) {
    auto r = __builtin_amdgcn_permlane32_swap(__float_as_uint(v), __float_as_uint(v), false, false);
    return fmaxf(__uint_as_float(r[0]), __uint_as_float(r[1]));
}
DI void glds16(const char* sbase, unsigned voff, unsigned lds_dst) {
    asm volatile("s_mov_b32 m0, %2\n\ts_nop 0\n\tglobal_load_lds_dwordx4 %0, %1" ::"v"(voff), "s"(sbase), "s"(lds_dst) : "memory");
}
#define ST_MODE 2
#define ST_SIDE 2
template <int MODE> DI void st16m(void* p, u32x4 v) {
    if constexpr (MODE == 0) *(u32x4*)p = v;
    else if constexpr (MODE == 1) __builtin_nontemporal_store(v, (u32x4*)p);
    else if constexpr (MODE == 2) asm volatile("global_store_dwordx4 %0, %1, off sc1\n\ts_nop 1" ::"v"(p), "v"(v) : "memory");
    else asm volatile("global_store_dwordx4 %0, %1, off sc0 sc1\n\ts_nop 1" ::"v"(p), "v"(v) : "memory");
}
DI void st16(void* p, u32x4 v) { st16m<ST_MODE>(p, v); }
template <int N> DI void wait_vm_barrier() { asm volatile("s_waitcnt vmcnt(%0) lgkmcnt(0)\n\ts_barrier" ::"i"(N) : "memory"); }
DI void lds_barrier() { asm volatile("s_waitcnt lgkmcnt(0)\n\ts_barrier" ::: "memory"); }
DI bf16x8 lds_b128(unsigned addr) { return *(const LAS bf16x8*)(uintptr_t)addr; }
DI bf16x8 lds_tr_frag(unsigned addr) {
    typedef short v4i16_t __attribute__((ext_vector_type(4)));
    v4i16_t lo = __builtin_amdgcn_ds_read_tr16_b64_v4i16((LAS v4i16_t*)(uintptr_t)addr);
    v4i16_t hi = __builtin_amdgcn_ds_read_tr16_b64_v4i16((LAS v4i16_t*)(uintptr_t)(addr + 512));
    return __builtin_shufflevector(lo, hi, 0, 1, 2, 3, 4, 5, 6, 7);
}

#define ST_DECL
#define ST(i)
#define STC(i)
#define ST_OUT(kind, role)
enum { EPI_QKV = 0, EPI_RES = 1, EPI_GLU = 2 };

struct GemmArgs {
    const char* A;
    const char* W;
    const char* R;
    const float* CS;
    char* O0;
    char* O1;
    int o_sc, o_sr;
};

#define XC_QKV 2
#define XC_RES 2
#define XC_GLU 2
#define GLU_WM 1
#define MFMA_FIRST 1
#define GLU_BK 32
#define GLU_PRO 2
#define NS_QKV 6
#define NS_DN 6
#define NS_GLU 5
template <int EPI, int KTOT> struct Cfg;
template <> struct Cfg<EPI_QKV, 512> { static constexpr int MT = 2, NP = 3, WM = 2, WN = 2, BK = 32, NS = NS_QKV, PRO = 4, KG = 2, NCB = 8, XC = XC_QKV; };
template <> struct Cfg<EPI_RES, 512> { static constexpr int MT = 2, NP = 1, WM = 2, WN = 2, BK = 64, NS = 8, PRO = 4, KG = 4, NCB = 8, XC = XC_RES; };
template <> struct Cfg<EPI_RES, FF> { static constexpr int MT = 2, NP = 1, WM = 2, WN = 2, BK = 64, NS = NS_DN, PRO = 4, KG = 4, NCB = 8, XC = XC_RES; };
template <> struct Cfg<EPI_GLU, 512> { static constexpr int MT = 4, NP = 4, WM = GLU_WM, WN = 4, BK = GLU_BK, NS = NS_GLU, PRO = GLU_PRO, KG = 1, NCB = 14, XC = XC_GLU; };

template <int EPI, int KTOT> struct Geo : Cfg<EPI, KTOT> {
    using C = Cfg<EPI, KTOT>;
    static constexpr int TM = C::MT / C::WM, TN = C::NP * 2 / C::WN, NT = KTOT / C::BK, KS = C::BK / 16, UPS = KS / C::KG, NU = NT * UPS;
    static constexpr int ABYTES = C::MT * C::BK * 64, PBYTES = C::BK * 128, BBYTES = C::NP * PBYTES, SBYTES = ABYTES + BBYTES;
    static constexpr int GA = ABYTES / 4096, GB = BBYTES / 4096, G = GA + GB;
    static constexpr bool LN = EPI != EPI_RES;
    static constexpr int ROWF = C::NS * SBYTES;
    static constexpr int LDS = LN ? ROWF + 1024 : ROWF;
    static constexpr int NRB = SEQ / (32 * C::MT), GRID = NRB * C::NCB, THREADS = 256 + 64 * C::WM * C::WN;
    static_assert(GA * 4096 == ABYTES && GB * 4096 == BBYTES && GA <= KS && KS % GA == 0 && GB <= 4, "pieces must divide over the waves");
    static_assert(LDS <= 163840 && G * C::NS + 4 <= 63, "LDS / vmcnt budget");
    static constexpr int issued(int s) { int a = C::PRO + 2 * s, b = s + C::NS - 1; a = a < b ? a : b; return a < NT ? a : NT; }
    static constexpr int last_issue_step() { int s = 0; while (issued(s + 1) < NT) ++s; return s; }
};
#define SBAR() __builtin_amdgcn_sched_barrier(0)

template <int N> DI void glds_run(const char* sbase, unsigned voff, unsigned lds_dst) {
    static_assert(N >= 1 && N <= 4, "");
    if constexpr (N == 1) asm volatile("s_mov_b32 m0, %2\n\ts_nop 0\n\tglobal_load_lds_dwordx4 %0, %1" ::"v"(voff), "s"(sbase), "s"(lds_dst) : "memory");
    if constexpr (N == 2) asm volatile("s_mov_b32 m0, %2\n\ts_nop 0\n\tglobal_load_lds_dwordx4 %0, %1\n\tglobal_load_lds_dwordx4 %0, %1 offset:1024" ::"v"(voff), "s"(sbase), "s"(lds_dst) : "memory");
    if constexpr (N == 3) asm volatile("s_mov_b32 m0, %2\n\ts_nop 0\n\tglobal_load_lds_dwordx4 %0, %1\n\tglobal_load_lds_dwordx4 %0, %1 offset:1024\n\tglobal_load_lds_dwordx4 %0, %1 offset:2048" ::"v"(voff), "s"(sbase), "s"(lds_dst) : "memory");
    if constexpr (N == 4) asm volatile("s_mov_b32 m0, %2\n\ts_nop 0\n\tglobal_load_lds_dwordx4 %0, %1\n\tglobal_load_lds_dwordx4 %0, %1 offset:1024\n\tglobal_load_lds_dwordx4 %0, %1 offset:2048\n\tglobal_load_lds_dwordx4 %0, %1 offset:3072" ::"v"(voff), "s"(sbase), "s"(lds_dst) : "memory");
}

DI unsigned tr_lane_off(int lane, int j) {
    const int hh = lane >> 5, blk = (lane >> 4) & 1, q = (lane & 15) >> 2, pp = lane & 3, pi = ((pp & 1) << 1) | (pp >> 1);
    const int ch = (4 * j + 2 * blk + (pi >> 1)) ^ (((q >> 1) & 1) << 2);
    return (8 * hh + q) * 128 + ch * 16 + 8 * (pi & 1);
}
DI int slot_col(int r32) { const int pp = (r32 >> 2) & 3; return (r32 & 16) + 4 * (((pp & 1) << 1) | (pp >> 1)) + (r32 & 3); }
DI bf16x8 corr_frag(float v, bool second_lo, int hh) {
    const unsigned h = pack_bf16(v, 0.f) & 0xffffu, l = pack_bf16(v - bf_lo(h), 0.f) & 0xffffu;
    u32x4 u; u[0] = second_lo ? (h | (l << 16)) : (h | (h << 16)); u[1] = second_lo ? h : l; u[2] = 0; u[3] = 0;
    if (hh) { u[0] = 0; u[1] = 0; }
    return __builtin_bit_cast(bf16x8, u);
}

template <int EPI, int KTOT>
__global__ __launch_bounds__((Geo<EPI, KTOT>::THREADS)) void gemm_kernel(GemmArgs p) {
    using G_ = Geo<EPI, KTOT>;
    constexpr int MT = G_::MT, WN = G_::WN, BK = G_::BK, NS = G_::NS, NT = G_::NT, KS = G_::KS, KG = G_::KG, UPS = G_::UPS, NU = G_::NU;
    constexpr int TM = G_::TM, TN = G_::TN, GA = G_::GA, GB = G_::GB, G = G_::G;
    constexpr int ABYTES = G_::ABYTES, PBYTES = G_::PBYTES, BBYTES = G_::BBYTES, SBYTES = G_::SBYTES;
    constexpr bool LN = G_::LN;
    constexpr int LOAD_U = (EPI == EPI_GLU ? 2 : G_::last_issue_step() + 1) * UPS;
    constexpr int CORR_U = EPI == EPI_GLU ? 10 * UPS : NU - 2;
    extern __shared__ __attribute__((aligned(16))) char smem[];
    ST_DECL ST(0); STC(6);
    constexpr int KIND = EPI == EPI_QKV ? 0 : EPI == EPI_GLU ? 3 : KTOT == 512 ? 2 : 4;
    const unsigned lds0 = (unsigned)(uintptr_t)(LAS char*)smem;
    const int tid = threadIdx.x, lane = tid & 63, wid = __builtin_amdgcn_readfirstlane(tid >> 6);
    const int r32 = lane & 31, hh = lane >> 5;
    LAS f32x2* rowf = (LAS f32x2*)(uintptr_t)(lds0 + G_::ROWF);

    int rb, cb;
    {
        const int x = blockIdx.x & 7, i = blockIdx.x >> 3;
        constexpr int XC = G_::XC, CH = G_::NCB / XC, RH = G_::NRB * XC / 8;
        rb = (x / XC) * RH + i / CH; cb = (x % XC) * CH + i % CH;
    }
    constexpr int A_RB = 32 * KTOT * 2;

    if (wid < 4) {
        const unsigned vo = lane * 16;
        const char* sA = p.A + (size_t)(rb * MT + (wid * GA) / KS) * A_RB + ((wid * GA) % KS) * 1024;
        const char* sB = p.W + (size_t)cb * (NT * BBYTES) + wid * GB * 1024;
        auto issue = [&](int slot) {
            glds_run<GA>(sA, vo, lds0 + slot * SBYTES + wid * GA * 1024); sA += KS * 1024;
            glds_run<GB>(sB, vo, lds0 + slot * SBYTES + ABYTES + wid * GB * 1024); sB += BBYTES;
        };
        constexpr int PRO = G_::issued(0);
#pragma unroll
        for (int s = 0; s < PRO; ++s) issue(s);
        constexpr int AUX = LN ? 4 : 0;
        f32x4 stp[4];
        if constexpr (LN) {
            const char* sp = p.R + (size_t)(rb * MT + wid % MT) * 4096;
            const unsigned so = hh * 2048 + r32 * 16;
            asm volatile("global_load_dwordx4 %0, %4, %5\n\tglobal_load_dwordx4 %1, %4, %5 offset:512\n\t"
                         "global_load_dwordx4 %2, %4, %5 offset:1024\n\tglobal_load_dwordx4 %3, %4, %5 offset:1536"
                         : "=&v"(stp[0]), "=&v"(stp[1]), "=&v"(stp[2]), "=&v"(stp[3]) : "v"(so), "s"(sp) : "memory");
        }
        ST(1);
        static_for<NT>([&](auto sc) {
            constexpr int s = decltype(sc)::value;
            wait_vm_barrier<G * (G_::issued(s) - 1 - s) + (s < PRO ? AUX : 0)>();
            if constexpr (s == 0) ST(2);
            if constexpr (s == NT / 2) ST(3);
            if constexpr (s == NT - 1) ST(4);
#pragma unroll
            for (int k = G_::issued(s); k < G_::issued(s + 1); ++k) issue(k % NS);
            if constexpr (LN && s == PRO) {
                asm volatile("" : "+v"(stp[0]), "+v"(stp[1]), "+v"(stp[2]), "+v"(stp[3]));
                const f32x4 t = (stp[0] + stp[1]) + (stp[2] + stp[3]);
                const float mean = half_sum(t[0] + t[2]) * (1.0f / KTOT), var = fmaxf(half_sum(t[1] + t[3]) * (1.0f / KTOT) - mean * mean, 0.f);
                if (hh == 0) rowf[(wid % MT) * 32 + r32] = f32x2{mean, __builtin_amdgcn_rsqf(var + LN_EPS)};
            }
        });
        ST(5); STC(7); ST_OUT(KIND, 0);
        return;
    }

    const int wm = (wid - 4) / WN, wn = (wid - 4) % WN;
    u32x4 resid[2];
    float cpart[TN];
    unsigned aoff = lds0 + wm * TM * KS * 1024 + lane * 16;
    unsigned boff = lds0 + ABYTES + (wn >> 1) * PBYTES + tr_lane_off(lane, wn & 1);
    constexpr int HS = (NS + 1) / 2;
    unsigned aoff2 = aoff + HS * SBYTES, boff2 = boff + HS * SBYTES;
    asm volatile("" : "+v"(aoff2), "+v"(boff2));
    constexpr int TSTR = (WN / 2) * PBYTES;

    f32x16 acc[TM][TN];
#pragma unroll
    for (int i = 0; i < TM; ++i)
#pragma unroll
        for (int t = 0; t < TN; ++t) acc[i][t] = f32x16{};
    float rstd[TM];
    constexpr int NF = KG * (TM + TN), NM = KG * TM * TN;
    bf16x8 fr[2][NF];
    auto rd = [&](auto uc, auto fc) {
        constexpr int u = decltype(uc)::value, f = decltype(fc)::value;
        constexpr int slot = (u / UPS) % NS, so = (slot < HS ? slot : slot - HS) * SBYTES, ks = (u % UPS) * KG + f / (TM + TN), e = f % (TM + TN);
        if constexpr (e < TN) fr[u & 1][f] = lds_tr_frag((slot < HS ? boff : boff2) + so + e * TSTR + ks * 2048);
        else fr[u & 1][f] = lds_b128((slot < HS ? aoff : aoff2) + so + ((e - TN) * KS + ks) * 1024);
    };
    auto mma = [&](auto tc, f32x16& c, const bf16x8& af, const bf16x8& bf) {
        if constexpr (EPI == EPI_QKV && decltype(tc)::value == 2) c = MFMA32(af, bf, c);
        else c = MFMA32(bf, af, c);
    };
    ST(1);
    lds_barrier();
    ST(2); STC(6);
    static_for<NF>([&](auto fc) { rd(std::integral_constant<int, 0>{}, fc); });
    SBAR();
    static_for<NU>([&](auto uc) {
        constexpr int u = decltype(uc)::value;
        if constexpr (u == LOAD_U) {
            if constexpr (EPI == EPI_RES) {
#pragma unroll
                for (int e = 0; e < 2; ++e) resid[e] = *(const u32x4*)(p.R + (size_t)(rb * MT + wm) * 32768 + (cb * 8 + wn * 4 + 2 * e) * 512 + lane * 16);
            } else {
                constexpr int NCOL = EPI == EPI_QKV ? 3 * DM : 2 * FF;
#pragma unroll
                for (int t = 0; t < TN; ++t) cpart[t] = p.CS[hh * NCOL + t * (NCOL / TN) + cb * (32 * WN) + wn * 32 + slot_col(r32)];
            }
            SBAR();
        }
        if constexpr (LN && u == CORR_U) {
            bf16x8 wf[TN];
#pragma unroll
            for (int t = 0; t < TN; ++t) wf[t] = corr_frag(half_sum(cpart[t]), false, hh);
#pragma unroll
            for (int i = 0; i < TM; ++i) {
                const f32x2 st = rowf[(wm * TM + i) * 32 + r32];
                rstd[i] = st.y;
                const bf16x8 xf = corr_frag(-st.x, true, hh);
                static_for<TN>([&](auto tc) { mma(tc, acc[i][decltype(tc)::value], xf, wf[decltype(tc)::value]); });
            }
            SBAR();
        }
        if constexpr (u + 1 < NU && (u + 1) % UPS == 0) lds_barrier();
        static_for<NM>([&](auto mc) {
            constexpr int m = decltype(mc)::value, g = m / (TM * TN), i = (m % (TM * TN)) / TN, t = m % TN;
            mma(std::integral_constant<int, t>{}, acc[i][t], fr[u & 1][g * (TM + TN) + TN + i], fr[u & 1][g * (TM + TN) + t]);
            if constexpr (u + 1 < NU) {
                SBAR();
                constexpr int PER = (NF + NM - 1) / NM < 2 ? 2 : (NF + NM - 1) / NM;
                static_for<PER>([&](auto kc) {
                    constexpr int f = m * PER + decltype(kc)::value;
                    if constexpr (f < NF) rd(std::integral_constant<int, u + 1>{}, std::integral_constant<int, f>{});
                });
                SBAR();
            }
        });
    });

    STC(7); ST(3);
    if constexpr (EPI == EPI_QKV) {
        const int tile = rb * MT + wm;
        const int head0 = cb * 4 + wn * 2;
        const float rq = rstd[0] * QSCALE, rk = rstd[0];
#pragma unroll
        for (int e = 0; e < 2; ++e) {
            u32x4 q, k;
#pragma unroll
            for (int j = 0; j < 4; ++j) {
                q[j] = pack_bf16(rq * acc[0][0][8 * e + 2 * j], rq * acc[0][0][8 * e + 2 * j + 1]);
                k[j] = pack_bf16(rk * acc[0][1][8 * e + 2 * j], rk * acc[0][1][8 * e + 2 * j + 1]);
            }
            st16(p.O0 + (size_t)((head0 + e) * 64 + tile) * 1024 + lane * 16, q);
            st16(p.O1 + (size_t)((head0 + e) * 128 + tile) * 1024 + lane * 16, k);
        }
#pragma unroll
        for (int e = 0; e < 2; ++e) {
            u32x4 o;
#pragma unroll
            for (int j = 0; j < 4; ++j) {
                const f32x4 st = *(LAS f32x4*)&rowf[wm * 32 + 16 * e + 8 * (j >> 1) + 4 * hh + 2 * (j & 1)];
                o[j] = pack_bf16(st[1] * acc[0][2][8 * e + 2 * j], st[3] * acc[0][2][8 * e + 2 * j + 1]);
            }
            st16(p.O1 + (size_t)((head0 + (r32 >> 4)) * 128 + 64 + tile) * 1024 + e * 512 + hh * 256 + (r32 & 15) * 16, o);
        }
    } else if constexpr (EPI == EPI_GLU) {
#pragma unroll
        for (int i = 0; i < TM; ++i) {
            const float r2 = rstd[i] * rstd[i];
            char* dst = p.O0 + (size_t)(rb * MT + wm * TM + i) * (32 * FF * 2) + (size_t)(cb * 16 + wn * 4) * 512 + lane * 16;
#pragma unroll
            for (int e = 0; e < 2; ++e) {
                u32x4 o;
#pragma unroll
                for (int j = 0; j < 4; ++j) {
                    const int r = 8 * e + 2 * j;
                    o[j] = pack_bf16(fmaxf(acc[i][0][r], 0.f) * (r2 * acc[i][1][r]), fmaxf(acc[i][0][r + 1], 0.f) * (r2 * acc[i][1][r + 1]));
                }
                st16(dst + e * 1024, o);
            }
        }
    } else {
        const size_t rbo = (size_t)(rb * MT + wm) * 32768;
        float sum = 0.f, sq = 0.f;
#pragma unroll
        for (int e = 0; e < 2; ++e) {
            const int ch = cb * 8 + wn * 4 + 2 * e + hh;
            u32x4 o;
#pragma unroll
            for (int j = 0; j < 4; ++j) {
                const float a = bf_lo(resid[e][j]) + acc[0][0][8 * e + 2 * j], b = bf_hi(resid[e][j]) + acc[0][0][8 * e + 2 * j + 1];
                o[j] = pack_bf16(a, b);
                const float ar = bf_lo(o[j]), br = bf_hi(o[j]);
                sum += ar + br; sq = fmaf(ar, ar, fmaf(br, br, sq));
            }
            st16(p.O0 + rbo + ch * p.o_sc + r32 * p.o_sr, o);
        }
        if (p.O1) {
            sum = half_sum(sum); sq = half_sum(sq);
            if (hh == 0) *(f32x2*)(p.O1 + (size_t)(rb * MT + wm) * 4096 + cb * 512 + r32 * 16 + wn * 8) = f32x2{sum, sq};
        }
    }
    ST(4);
    ST(5); ST_OUT(KIND, 1);
}

DI void load_unit(u32x4 (&v)[4], const char* src, int ld2, int lane, bool swz) {
    const int ch = swz ? ((lane & 7) ^ (((lane >> 4) & 1) << 2)) : (lane & 7);
    const char* a = src + (size_t)(lane >> 3) * ld2 + ch * 16;
#pragma unroll
    for (int i = 0; i < 4; ++i) v[i] = *(const u32x4*)(a + (size_t)i * 8 * ld2);
}
template <int MODE> DI void store_unit_packed(const u32x4 (&v)[4], char* dst, int lane) {
#pragma unroll
    for (int i = 0; i < 4; ++i) st16m<MODE>(dst + i * 1024 + lane * 16, v[i]);
}
template <int EPI, int KTOT> DI size_t packed_off(int cb, int pan, int kb) {
    using G_ = Geo<EPI, KTOT>;
    return (size_t)cb * (G_::NT * G_::BBYTES) + (size_t)((kb * 32) / G_::BK) * G_::BBYTES + pan * G_::PBYTES + ((kb * 32) % G_::BK) * 128;
}

struct PrepArgs { const char* x; const char* wqkv; const char* wg; const char* wu; char* xt; char* st; char* wqkv_p; float* cs_qkv; float* cs_gu; };

__global__ __launch_bounds__(512) void prep_kernel(PrepArgs p) {
    __shared__ float red[8][64];
    ST_DECL ST(0); ST(1); ST(2); STC(6); STC(7);
    const int tid = threadIdx.x, lane = tid & 63, wid = __builtin_amdgcn_readfirstlane(tid >> 6);
    const int b = blockIdx.x;
    u32x4 v[4];
    if (b < 64) {
        load_unit(v, p.x + (size_t)b * 32768 + wid * 128, 1024, lane, false);
#pragma unroll
        for (int i = 0; i < 4; ++i) {
            const int row = i * 8 + (lane >> 3);
            st16(p.xt + (size_t)b * 32768 + (wid * 8 + (lane & 7)) * 512 + row * 16, v[i]);
            float sum = 0.f, sq = 0.f;
#pragma unroll
            for (int j = 0; j < 4; ++j) { const float lo = bf_lo(v[i][j]), hi = bf_hi(v[i][j]); sum += lo + hi; sq = fmaf(lo, lo, fmaf(hi, hi, sq)); }
            sum += __shfl_xor(sum, 1); sq += __shfl_xor(sq, 1);
            sum += __shfl_xor(sum, 2); sq += __shfl_xor(sq, 2);
            if ((lane & 3) == 0) *(f32x2*)(p.st + (size_t)b * 4096 + wid * 512 + row * 16 + ((lane >> 2) & 1) * 8) = f32x2{sum, sq};
        }
        ST(3); ST(4);
        ST(5); ST_OUT(5, wid >> 2);
        return;
    }
    const int j = b - 64, half = j & 1, pan = j >> 1, kb = half * 8 + wid;
    const bool isq = pan < 24;
    const char* src = isq ? p.wqkv + (size_t)kb * 32 * 3072 + pan * 128 : (pan < 52 ? p.wg + (pan - 24) * 128 : p.wu + (pan - 52) * 128) + (size_t)kb * 32 * (FF * 2);
    load_unit(v, src, isq ? 3072 : FF * 2, lane, true);
    if (isq) store_unit_packed<ST_MODE>(v, p.wqkv_p + packed_off<EPI_QKV, 512>(pan & 7, pan >> 3, kb), lane);
    ST(3);
    float c[8];
#pragma unroll
    for (int e = 0; e < 8; ++e) c[e] = 0.f;
#pragma unroll
    for (int i = 0; i < 4; ++i)
#pragma unroll
        for (int e = 0; e < 4; ++e) { c[2 * e] += bf_lo(v[i][e]); c[2 * e + 1] += bf_hi(v[i][e]); }
#pragma unroll
    for (int e = 0; e < 8; ++e) { c[e] += __shfl_xor(c[e], 8); c[e] += __shfl_xor(c[e], 20); c[e] = half_sum(c[e]); }
    if (lane < 8) { *(f32x4*)&red[wid][lane * 8] = f32x4{c[0], c[1], c[2], c[3]}; *(f32x4*)&red[wid][lane * 8 + 4] = f32x4{c[4], c[5], c[6], c[7]}; }
    __syncthreads();
    if (tid < 64) {
        float t = 0.f;
#pragma unroll
        for (int w = 0; w < 8; ++w) t += red[w][tid];
        if (isq) p.cs_qkv[half * (3 * DM) + pan * 64 + tid] = t;
        else p.cs_gu[half * (2 * FF) + (pan - 24) * 64 + tid] = t;
    }
    ST(4);
    ST(5); ST_OUT(5, wid >> 2);
}

constexpr int AT_K = 0, AT_V = 65536, AT_ONES = 131072, AT_LDS = AT_ONES + 9216;

#define AT_E(C, i) "v_exp_f32 v[" #C "+" #i "], v[" #C "+" #i "]\n\t"
#define AT_C(P, C, i) "v_cvt_pk_bf16_f32 v[" #P "], v[" #C "+" #i "], v[" #C "+" #i "+1]\n\t"
#define AT_HALF(C, O, V0, PRE, MID)                                                                               \
    AT_E(C, 0) AT_E(C, 1) PRE AT_E(C, 2) AT_E(C, 3) AT_E(C, 4) AT_E(C, 5) MID                                     \
    AT_E(C, 6) AT_E(C, 7) AT_C(108, C, 0) AT_C(109, C, 2) AT_C(110, C, 4) AT_E(C, 8) AT_C(111, C, 6) AT_E(C, 9) AT_E(C, 10) \
    "v_mfma_f32_32x32x16_bf16 " O ", v[" #V0 ":" #V0 "+3], v[108:111], " O "\n\t"                                 \
    AT_E(C, 11) AT_E(C, 12) AT_E(C, 13) AT_E(C, 14) AT_E(C, 15)                                                   \
    AT_C(112, C, 8) AT_C(113, C, 10) AT_C(114, C, 12) AT_C(115, C, 14)
#define AT_TILE(U, V0C, V0N, SYNC)                                                                                \
    AT_HALF(64, "%[o0]", V0C,                                                                                     \
            "v_mfma_f32_32x32x16_bf16 %[o1], v[104:107], v[112:115], %[o1]\n\t"                                   \
            "ds_read_b128 v[104:107], %[va] offset:" #U "*1024+512\n\t",                                          \
            "v_mfma_f32_32x32x16_bf16 v[80:95], v[96:99], %[q1], %[nm1]\n\t" SYNC                                 \
            "ds_read_b128 v[96:99], %[ka] offset:" #U "*1024+1024\n\t"                                            \
            "ds_read_b128 v[" #V0N ":" #V0N "+3], %[va] offset:" #U "*1024+1024\n\t")                             \
    AT_HALF(80, "%[o1]", V0C,                                                                                     \
            "s_waitcnt lgkmcnt(0)\n\t"                                                                            \
            "v_mfma_f32_32x32x16_bf16 %[o0], v[104:107], v[112:115], %[o0]\n\t",                                  \
            "v_mfma_f32_32x32x16_bf16 v[64:79], v[96:99], %[q0], %[nm0]\n\t")

DI f32x16 neg_tile_max(const bf16x8& k0, const bf16x8& q) {
    const f32x16 s0 = MFMA32(k0, q, f32x16{});
    float m = s0[0];
#pragma unroll
    for (int r = 1; r < 16; ++r) m = fmaxf(m, s0[r]);
    m = half_max(m);
    f32x16 n;
#pragma unroll
    for (int r = 0; r < 16; ++r) n[r] = -m;
    return n;
}
DI f32x16 attn_exact(const bf16x8& q, unsigned ka, unsigned va, unsigned vs) {
    float m = -INFINITY;
    f32x16 o = f32x16{};
#pragma unroll 1
    for (int t = 0; t < 64; ++t) {
        const f32x16 s = MFMA32(lds_b128(ka), q, f32x16{});
        float tm = s[0];
#pragma unroll
        for (int r = 1; r < 16; ++r) tm = fmaxf(tm, s[r]);
        const float mn = fmaxf(m, half_max(tm));
        const float alpha = __builtin_amdgcn_exp2f(m - mn);
        m = mn;
#pragma unroll
        for (int r = 0; r < 16; ++r) o[r] *= alpha;
        u32x4 p0, p1;
#pragma unroll
        for (int j = 0; j < 4; ++j) {
            p0[j] = pack_bf16(__builtin_amdgcn_exp2f(s[2 * j] - m), __builtin_amdgcn_exp2f(s[2 * j + 1] - m));
            p1[j] = pack_bf16(__builtin_amdgcn_exp2f(s[8 + 2 * j] - m), __builtin_amdgcn_exp2f(s[8 + 2 * j + 1] - m));
        }
        o = MFMA32(lds_b128(va), __builtin_bit_cast(bf16x8, p0), o);
        o = MFMA32(lds_b128(va + 512), __builtin_bit_cast(bf16x8, p1), o);
        ka += 1024; va += vs;
    }
    return o;
}

struct AttnArgs {
    const char* Qf; const char* KVf; char* Ot;
    const char* Wg; const char* Wu; const char* Wo; const char* Wd; char* wp;
};
constexpr size_t WP_QKV = 0, WP_O = (size_t)DM * 3 * DM * 2, WP_GU = WP_O + (size_t)DM * DM * 2, WP_D = WP_GU + (size_t)DM * FF * 4;

__global__ __launch_bounds__(512) void attn_kernel(AttnArgs p) {
    extern __shared__ __attribute__((aligned(16))) char smem[];
    ST_DECL ST(0);
    const unsigned lds0 = (unsigned)(uintptr_t)(LAS char*)smem;
    const int tid = threadIdx.x, lane = tid & 63, wid = __builtin_amdgcn_readfirstlane(tid >> 6);
    const int r32 = lane & 31, hh = lane >> 5;
    const int x = blockIdx.x & 7, idx = blockIdx.x >> 3, head = x * 4 + (idx >> 3), qb = idx & 7;

    if (__builtin_expect(wid < 4, 1)) {
        const unsigned vo = lane * 16;
        const char* src = p.KVf + (size_t)head * 131072 + wid * 2048;
        auto chunk = [&](int c) {
            glds_run<2>(src + c * 8192, vo, lds0 + AT_K + c * 8192 + wid * 2048);
            glds_run<2>(src + 65536 + c * 8192, vo, lds0 + AT_V + c * 8192 + wid * 2048);
        };
        chunk(0); chunk(1); chunk(2);
        ST(1);
        wait_vm_barrier<8>();
        ST(2);
        chunk(3); chunk(4); chunk(5); chunk(6); chunk(7);
        ST(3);
        wait_vm_barrier<0>();
        ST(4);
        if (p.wp) {
#pragma unroll 1
            for (int u = blockIdx.x * 4 + wid; u < 1472; u += 1024) {
                const char* src; size_t dst; int ld2;
                if (u < 896) { const int mat = u >= 448, v = u - mat * 448, pan = v >> 4, kb = v & 15;
                    src = (mat ? p.Wu : p.Wg) + (size_t)kb * 32 * (FF * 2) + pan * 128; ld2 = FF * 2; dst = WP_GU + packed_off<EPI_GLU, 512>(pan >> 1, mat * 2 + (pan & 1), kb); }
                else if (u < 1024) { const int v = u - 896, pan = v & 7, kb = v >> 3;
                    src = p.Wo + (size_t)kb * 32 * 1024 + pan * 128; ld2 = 1024; dst = WP_O + packed_off<EPI_RES, 512>(pan, 0, kb); }
                else { const int v = u - 1024, pan = v & 7, kb = v >> 3;
                    src = p.Wd + (size_t)kb * 32 * 1024 + pan * 128; ld2 = 1024; dst = WP_D + packed_off<EPI_RES, FF>(pan, 0, kb); }
                u32x4 v[4];
                load_unit(v, src, ld2, lane, true);
                store_unit_packed<ST_SIDE>(v, p.wp + dst, lane);
            }
        }
        ST(5); STC(6); STC(7); ST_OUT(1, 0);
        return;
    }

    const int qtile = qb * 8 + (wid - 4) * 2;
    const bf16x8 q0 = *(const bf16x8*)(p.Qf + (size_t)(head * 64 + qtile) * 1024 + lane * 16);
    const bf16x8 q1 = *(const bf16x8*)(p.Qf + (size_t)(head * 64 + qtile + 1) * 1024 + lane * 16);
    {
        const unsigned ov = ((tid & 15) == 0 || (tid & 15) == 4) ? 0x3f803f80u : 0u;
        *(LAS u32x4*)(uintptr_t)(lds0 + AT_ONES + (tid - 256) * 16) = u32x4{ov, ov, ov, ov};
        *(LAS u32x4*)(uintptr_t)(lds0 + AT_ONES + tid * 16) = u32x4{ov, ov, ov, ov};
        if (tid < 320) *(LAS u32x4*)(uintptr_t)(lds0 + AT_ONES + (tid + 256) * 16) = u32x4{ov, ov, ov, ov};
    }
    const unsigned ka0 = lds0 + AT_K + lane * 16;
    const bool vlane = r32 < 16;
    const unsigned va0 = lds0 + (vlane ? AT_V : AT_ONES) + hh * 256 + (r32 & 15) * 16;
    const unsigned vstep = vlane ? 8192u : 0u;

    ST(1);
    lds_barrier();
    ST(2);
    const bf16x8 k0 = lds_b128(ka0);
    const f32x16 nm0 = neg_tile_max(k0, q0), nm1 = neg_tile_max(k0, q1);
    f32x16 o0 = f32x16{}, o1 = f32x16{};
    ST(3); STC(6);
    {
        unsigned ka = ka0, va = va0; int cnt;
        asm volatile(
            "ds_read_b128 v[96:99], %[ka]\n\t"
            "ds_read_b128 v[100:103], %[va]\n\t"
            "v_mov_b32 v104, 0\n\tv_mov_b32 v105, 0\n\tv_mov_b32 v106, 0\n\tv_mov_b32 v107, 0\n\t"
            "v_mov_b32 v112, 0\n\tv_mov_b32 v113, 0\n\tv_mov_b32 v114, 0\n\tv_mov_b32 v115, 0\n\t"
            "s_mov_b32 %[cnt], 0\n\t"
            "s_waitcnt lgkmcnt(0)\n\t"
            "v_mfma_f32_32x32x16_bf16 v[64:79], v[96:99], %[q0], %[nm0]\n\t"
            "s_nop 12\n\t"
            "1:\n\t"
            AT_TILE(0, 100, 116, "") AT_TILE(1, 116, 100, "") AT_TILE(2, 100, 116, "") AT_TILE(3, 116, 100, "")
            AT_TILE(4, 100, 116, "") AT_TILE(5, 116, 100, "") AT_TILE(6, 100, 116, "")
            AT_TILE(7, 116, 100, "s_cmp_lg_u32 %[cnt], 0\n\ts_cbranch_scc1 2f\n\ts_barrier\n\t2:\n\t")
            "v_add_u32 %[ka], 0x2000, %[ka]\n\t"
            "v_add_u32 %[va], %[vstep], %[va]\n\t"
            "s_add_u32 %[cnt], %[cnt], 1\n\t"
            "s_cmp_lt_u32 %[cnt], 8\n\t"
            "s_cbranch_scc1 1b\n\t"
            "v_mfma_f32_32x32x16_bf16 %[o1], v[104:107], v[112:115], %[o1]\n\t"
            "s_nop 15\n\t"
            : [o0] "+v"(o0), [o1] "+v"(o1), [ka] "+v"(ka), [va] "+v"(va), [cnt] "=&s"(cnt)
            : [q0] "v"(q0), [q1] "v"(q1), [nm0] "v"(nm0), [nm1] "v"(nm1), [vstep] "v"(vstep)
            : "memory", "scc", "v64", "v65", "v66", "v67", "v68", "v69", "v70", "v71", "v72", "v73", "v74", "v75", "v76", "v77", "v78", "v79",
              "v80", "v81", "v82", "v83", "v84", "v85", "v86", "v87", "v88", "v89", "v90", "v91", "v92", "v93", "v94", "v95",
              "v96", "v97", "v98", "v99", "v100", "v101", "v102", "v103", "v104", "v105", "v106", "v107",
              "v108", "v109", "v110", "v111", "v112", "v113", "v114", "v115", "v116", "v117", "v118", "v119");
    }
    STC(7); ST(4);
    if (__builtin_expect(__any(!(o0[8] < 1.0e18f) || !(o1[8] < 1.0e18f)), 0)) {
        o0 = attn_exact(q0, ka0, va0, vlane ? 1024u : 0u);
        o1 = attn_exact(q1, ka0, va0, vlane ? 1024u : 0u);
    }
    const float i0 = 1.0f / o0[8], i1 = 1.0f / o1[8];
    u32x4 w0, w1;
#pragma unroll
    for (int j = 0; j < 4; ++j) { w0[j] = pack_bf16(o0[2 * j] * i0, o0[2 * j + 1] * i0); w1[j] = pack_bf16(o1[2 * j] * i1, o1[2 * j + 1] * i1); }
    st16(p.Ot + (size_t)qtile * 32768 + head * 1024 + lane * 16, w0);
    st16(p.Ot + (size_t)(qtile + 1) * 32768 + head * 1024 + lane * 16, w1);
    ST(5); ST_OUT(1, 1);
}

template <int EPI, int KTOT> static void launch_gemm(const GemmArgs& a, hipStream_t stream) {
    using G_ = Geo<EPI, KTOT>;
    (void)hipFuncSetAttribute((const void*)gemm_kernel<EPI, KTOT>, hipFuncAttributeMaxDynamicSharedMemorySize, G_::LDS);
    gemm_kernel<EPI, KTOT><<<G_::GRID, G_::THREADS, G_::LDS, stream>>>(a);
}

extern "C" void kernel_launch(void* const* d_in, const int* in_sizes, int n_in, void* d_out, int out_size, void* d_ws, size_t ws_size,
                              hipStream_t stream) {
    const char* x0 = (const char*)d_in[0];
    const char* wqkv = (const char*)d_in[1];
    const char* wo = (const char*)d_in[2];
    const char* wg = (const char*)d_in[4];
    const char* wu = (const char*)d_in[5];
    const char* wd = (const char*)d_in[6];
    char* ws = (char*)d_ws;
    char* Qf = ws;
    char* KVf = ws + (2 << 20);
    char* Ot = ws + (6 << 20);
    char* XA = ws + (8 << 20);
    char* XB = ws + (10 << 20);
    char* Ht = ws + (12 << 20);
    char* STA = ws + (19 << 20);
    char* STB = STA + (256 << 10);
    float* CSQ = (float*)(STB + (256 << 10));
    float* CSG = CSQ + 2 * 3 * DM;
    char* WP = ws + (20 << 20);
    (void)hipFuncSetAttribute((const void*)attn_kernel, hipFuncAttributeMaxDynamicSharedMemorySize, AT_LDS);
    prep_kernel<<<224, 512, 0, stream>>>(PrepArgs{x0, wqkv, wg, wu, XA, STA, WP + WP_QKV, CSQ, CSG});
    const int NL = 2;
    for (int layer = 0; layer < NL; ++layer) {
        launch_gemm<EPI_QKV, 512>(GemmArgs{XA, WP + WP_QKV, STA, CSQ, Qf, KVf, 0, 0}, stream);
        attn_kernel<<<256, 512, AT_LDS, stream>>>(AttnArgs{Qf, KVf, Ot, wg, wu, wo, wd, layer == 0 ? WP : nullptr});
        launch_gemm<EPI_RES, 512>(GemmArgs{Ot, WP + WP_O, XA, nullptr, XB, STB, 512, 16}, stream);
        launch_gemm<EPI_GLU, 512>(GemmArgs{XB, WP + WP_GU, STB, CSG, Ht, nullptr, 0, 0}, stream);
        if (layer == 0) launch_gemm<EPI_RES, FF>(GemmArgs{Ht, WP + WP_D, XB, nullptr, XA, STA, 512, 16}, stream);
        else launch_gemm<EPI_RES, FF>(GemmArgs{Ht, WP + WP_D, XB, nullptr, (char*)d_out, nullptr, 16, 1024}, stream);
    }
}
```

```cpp
#include <hip/hip_runtime.h>
#include <hip/hip_bf16.h>
#include <stdint.h>
#include <utility>

typedef short bf16x8 __attribute__((ext_vector_type(8)));
typedef float f32x16 __attribute__((ext_vector_type(16)));
typedef float f32x4 __attribute__((ext_vector_type(4)));
typedef float f32x2 __attribute__((ext_vector_type(2)));
typedef unsigned u32x4 __attribute__((ext_vector_type(4)));
typedef unsigned u32x2 __attribute__((ext_vector_type(2)));
typedef __bf16 bf16x2_t __attribute__((ext_vector_type(2)));
typedef __amdgpu_buffer_rsrc_t rsrc_t;
#define LAS __attribute__((address_space(3)))
#define GAS __attribute__((address_space(1)))
#define DI __device__ __forceinline__
#define MFMA32(a, b, c) __builtin_amdgcn_mfma_f32_32x32x16_bf16((a), (b), (c), 0, 0, 0)
#define SBAR() __builtin_amdgcn_sched_barrier(0)

constexpr int SEQ = 2048, DM = 512, FF = 1792;
constexpr float LN_EPS = 1e-5f;
constexpr float QSCALE = 0.25f * 1.4426950408889634f;

constexpr unsigned MB = 1u << 20;
constexpr unsigned WS_LAYER = 20 * MB;
constexpr unsigned WS_Q = 0, WS_KV = 2 * MB, WS_O = 6 * MB, WS_XA = 8 * MB, WS_XB = 10 * MB, WS_H = 12 * MB, WS_STA = 19 * MB, WS_STB = WS_STA + MB / 4, WS_FL = WS_STB + MB / 4;
constexpr unsigned FL_QKV = 0, FL_O = 8 * 256, FL_X1 = FL_O + 8 * 256, FL_H = FL_X1 + 16 * 256, FL_X2 = FL_H + 16 * 256, FL_PK = FL_X2 + 32 * 256, FL_BYTES = FL_PK + 1024;
constexpr unsigned WS_WP = 40 * MB;
constexpr unsigned WP_QKV = WS_WP, WP_O = WP_QKV + DM * 3 * DM * 2, WP_GU = WP_O + DM * DM * 2, WP_D = WP_GU + DM * FF * 4;
constexpr unsigned WS_CSQ = WP_D + FF * DM * 2, WS_CSG = WS_CSQ + 2 * 3 * DM * 4, WS_END = WS_CSG + 2 * 2 * FF * 4;

template <int... I, class Fn> DI void static_for_impl(std::integer_sequence<int, I...>, Fn&& f) { (f(std::integral_constant<int, I>{}), ...); }
template <int N, class Fn> DI void static_for(Fn&& f) { static_for_impl(std::make_integer_sequence<int, N>{}, f); }

DI unsigned pack_bf16(float lo, float hi) { bf16x2_t p; p.x = (__bf16)lo; p.y = (__bf16)hi; return __builtin_bit_cast(unsigned, p); }
DI float bf_lo(unsigned u) { return __uint_as_float(u << 16); }
DI float bf_hi(unsigned u) { return __uint_as_float(u & 0xffff0000u); }
DI float relu(float v) { return __builtin_amdgcn_fmed3f(v, 0.f, __builtin_inff()); }
DI float half_sum(float v) {
    auto r = __builtin_amdgcn_permlane32_swap(__float_as_uint(v), __float_as_uint(v), false, false);
    return __uint_as_float(r[0]) + __uint_as_float(r[1]);
}
DI float half_max(float v) {
    auto r = __builtin_amdgcn_permlane32_swap(__float_as_uint(v), __float_as_uint(v), false, false);
    return fmaxf(__uint_as_float(r[0]), __uint_as_float(r[1]));
}
DI void st16(rsrc_t r, unsigned voff, unsigned soff, u32x4 v) { __builtin_amdgcn_raw_buffer_store_b128(v, r, voff + soff, 0, 16); }
DI u32x4 ld16(rsrc_t r, unsigned voff, unsigned soff) { return __builtin_amdgcn_raw_buffer_load_b128(r, voff, soff, 16); }
template <int N> DI void wait_vm_barrier() { asm volatile("s_waitcnt vmcnt(%0) lgkmcnt(0)\n\ts_barrier" ::"i"(N) : "memory"); }
DI void lds_barrier() { asm volatile("s_waitcnt lgkmcnt(0)\n\ts_barrier" ::: "memory"); }
DI bf16x8 lds_b128(unsigned addr) { return *(const LAS bf16x8*)(uintptr_t)addr; }
DI bf16x8 lds_tr_frag(unsigned addr) {
    typedef short v4i16_t __attribute__((ext_vector_type(4)));
    v4i16_t lo = __builtin_amdgcn_ds_read_tr16_b64_v4i16((LAS v4i16_t*)(uintptr_t)addr);
    v4i16_t hi = __builtin_amdgcn_ds_read_tr16_b64_v4i16((LAS v4i16_t*)(uintptr_t)(addr + 512));
    return __builtin_shufflevector(lo, hi, 0, 1, 2, 3, 4, 5, 6, 7);
}
template <int N> DI void glds_run(const char* sbase, unsigned voff, unsigned lds_dst) {
    static_assert(N >= 1 && N <= 4, "");
#define GL(o) "\n\tglobal_load_lds_dwordx4 %0, %1 offset:" #o " sc1"
    if constexpr (N == 1) asm volatile("s_mov_b32 m0, %2\n\ts_nop 3" GL(0)::"v"(voff), "s"(sbase), "s"(lds_dst) : "memory");
    if constexpr (N == 2) asm volatile("s_mov_b32 m0, %2\n\ts_nop 3" GL(0) GL(1024)::"v"(voff), "s"(sbase), "s"(lds_dst) : "memory");
    if constexpr (N == 3) asm volatile("s_mov_b32 m0, %2\n\ts_nop 3" GL(0) GL(1024) GL(2048)::"v"(voff), "s"(sbase), "s"(lds_dst) : "memory");
    if constexpr (N == 4) asm volatile("s_mov_b32 m0, %2\n\ts_nop 3" GL(0) GL(1024) GL(2048) GL(3072)::"v"(voff), "s"(sbase), "s"(lds_dst) : "memory");
#undef GL
}
DI bool publish(char* ws, unsigned flag_off, unsigned cnt_addr, int lane) {
    asm volatile("s_waitcnt vmcnt(0)" ::: "memory");
    unsigned old = 0;
    if (lane == 0) old = __hip_atomic_fetch_add((LAS unsigned*)(uintptr_t)cnt_addr, 1u, __ATOMIC_RELAXED, __HIP_MEMORY_SCOPE_WORKGROUP);
    const bool last = (__builtin_amdgcn_readfirstlane(old) & 3) == 3;
    if (last && lane == 0) __hip_atomic_store((GAS unsigned*)(ws + flag_off), 1u, __ATOMIC_RELAXED, __HIP_MEMORY_SCOPE_AGENT);
    return last;
}
DI void wait_flags(const char* ws, unsigned flag_off, int n, int lane) {
    const GAS unsigned* f = (const GAS unsigned*)(ws + flag_off) + (lane < n ? lane : 0);
    const unsigned long long t0 = __builtin_amdgcn_s_memrealtime();
    for (;;) {
        const unsigned v = __hip_atomic_load(f, __ATOMIC_RELAXED, __HIP_MEMORY_SCOPE_AGENT);
        if (__all(v == 1u)) break;
        if (__builtin_amdgcn_s_memrealtime() - t0 > 2000000ull) break;
        __builtin_amdgcn_s_sleep(1);
    }
    asm volatile("s_waitcnt vmcnt(0) lgkmcnt(0)" ::: "memory");
}

#define ST_DECL
#define ST(i)
#define STC(i)
#define ST_OUT(kind, role)

enum { PH_QKV = 0, PH_ATT = 1, PH_WO = 2, PH_GLU = 3, PH_DN = 4 };
#define PRIO 0
#define XCV 2
template <int PH> struct Cfg;
template <> struct Cfg<PH_QKV> { static constexpr int KTOT = 512, MT = 2, NP = 3, WM = 2, WN = 2, BK = 32, NS = 8, PA = 8, KG = 2, NCB = 8, XC = XCV; };
template <> struct Cfg<PH_WO> { static constexpr int KTOT = 512, MT = 2, NP = 1, WM = 2, WN = 2, BK = 64, NS = 8, PA = 4, KG = 4, NCB = 8, XC = XCV; };
template <> struct Cfg<PH_GLU> { static constexpr int KTOT = 512, MT = 4, NP = 4, WM = 1, WN = 4, BK = 32, NS = 5, PA = 3, KG = 1, NCB = 14, XC = XCV; };
template <> struct Cfg<PH_DN> { static constexpr int KTOT = FF, MT = 2, NP = 1, WM = 2, WN = 2, BK = 64, NS = 8, PA = 4, KG = 4, NCB = 8, XC = XCV; };

constexpr int AT_K = 0, AT_V = 65536, AT_ONES = 131072, AT_Q = AT_ONES + 9216, LDS_ROWQ = AT_Q + 8192, LDS_ROWG = LDS_ROWQ + 1024, LDS_CNT = LDS_ROWG + 1024, LDS_BYTES = LDS_CNT + 16;

template <int PH> struct Geo : Cfg<PH> {
    using C = Cfg<PH>;
    static constexpr int TM = C::MT / C::WM, TN = C::NP * 2 / C::WN, NT = C::KTOT / C::BK, KS = C::BK / 16, UPS = KS / C::KG, NU = NT * UPS;
    static constexpr int ABYTES = C::MT * C::BK * 64, PBYTES = C::BK * 128, BBYTES = C::NP * PBYTES, SBYTES = ABYTES + BBYTES;
    static constexpr int GA = ABYTES / 4096, GB = BBYTES / 4096, G = GA + GB;
    static constexpr bool LN = PH == PH_QKV || PH == PH_GLU;
    static constexpr int ROWF = PH == PH_QKV ? LDS_ROWQ : LDS_ROWG;
    static constexpr int NRB = SEQ / (32 * C::MT), AUX = LN ? 4 : 0;
    static constexpr unsigned WP = PH == PH_QKV ? WP_QKV : PH == PH_WO ? WP_O : PH == PH_GLU ? WP_GU : WP_D;
    static constexpr unsigned A = PH == PH_QKV ? WS_XA : PH == PH_WO ? WS_O : PH == PH_GLU ? WS_XB : WS_H;
    static_assert(C::WM * C::WN == 4 && GA * 4096 == ABYTES && GB * 4096 == BBYTES && GA <= KS && KS % GA == 0 && GB <= 4, "pieces must divide over the waves");
    static_assert(C::NS * SBYTES <= 131072 && C::NS <= NT, "LDS budget");
    static constexpr int a_off(int k) {
        if (PH == PH_WO) return (k < 4 ? 32768 : 65536) + k * 8192;
        return ((k + (PH == PH_QKV ? 5 : PH == PH_DN ? 1 : 0)) % C::NS) * SBYTES;
    }
    static constexpr int b_off(int k) { return PH == PH_WO ? (k < 4 ? 0 : 32768) + k * 8192 : a_off(k) + ABYTES; }
    static DI void tile(int b, int& rb, int& cb) {
        const int x = b & 7, i = b >> 3;
        constexpr int CH = C::NCB / C::XC, RH = NRB * C::XC / 8;
        rb = (x / C::XC) * RH + i / CH; cb = (x % C::XC) * CH + i % CH;
    }
    static DI size_t packed_off(int cb, int pan, int kb) {
        return (size_t)WP + (size_t)cb * (NT * BBYTES) + (size_t)((kb * 32) / C::BK) * BBYTES + pan * PBYTES + ((kb * 32) % C::BK) * 128;
    }
};

DI unsigned tr_lane_off(int lane, int j) {
    const int hh = lane >> 5, blk = (lane >> 4) & 1, q = (lane & 15) >> 2, pp = lane & 3, pi = ((pp & 1) << 1) | (pp >> 1);
    const int ch = (4 * j + 2 * blk + (pi >> 1)) ^ (((q >> 1) & 1) << 2);
    return (8 * hh + q) * 128 + ch * 16 + 8 * (pi & 1);
}
DI int slot_col(int r32) { const int pp = (r32 >> 2) & 3; return (r32 & 16) + 4 * (((pp & 1) << 1) | (pp >> 1)) + (r32 & 3); }
DI bf16x8 corr_frag(float v, bool second_lo, int hh) {
    const unsigned h = pack_bf16(v, 0.f) & 0xffffu, l = pack_bf16(v - bf_lo(h), 0.f) & 0xffffu;
    u32x4 u; u[0] = second_lo ? (h | (l << 16)) : (h | (h << 16)); u[1] = second_lo ? h : l; u[2] = 0; u[3] = 0;
    if (hh) { u[0] = 0; u[1] = 0; }
    return __builtin_bit_cast(bf16x8, u);
}

#define FRESH(ws, lds0, w, lane, b) int b = blockIdx.x; asm volatile("" : "+s"(ws), "+s"(lds0), "+s"(w), "+s"(b)); asm volatile("" : "+v"(lane))

enum { OP_A, OP_F, OP_X, OP_W, OP_N, OP_E };
struct Sched { int n; int op[128]; int arg[128]; int pend[128]; int xw; };
template <int PH, int NX> constexpr Sched make_sched() {
    using G_ = Geo<PH>; using N_ = Geo<NX>;
    Sched sc{}; int cnt = 0, lastop[64] = {}, xpos = 0; bool ndone[8] = {};
    int next = 0;
    auto push = [&](int op, int arg) { sc.op[sc.n] = op; sc.arg[sc.n] = arg; sc.pend[sc.n] = 0; ++sc.n; };
    auto item = [&](int s) -> int {
        if (next < G_::NS) { push(OP_A, next); cnt += G_::GA; lastop[next] = cnt; ++next; return G_::GA; }
        if (next == G_::NS) { ++next; if (G_::AUX) { push(OP_X, 0); cnt += G_::AUX; xpos = cnt; return G_::AUX; } }
        const int k = next - 1;
        if (k < G_::NT && k <= s - 1 + G_::NS) { push(OP_F, k); cnt += G_::G; lastop[k] = cnt; ++next; return G_::G; }
        return 0;
    };
    auto nextb = [&](int s) {
        if (PH == PH_QKV) return;
        for (int j = 0; j < N_::NS; ++j) {
            if (ndone[j]) continue;
            bool ok = next > G_::NT;
            for (int k = s; k < G_::NT && ok; ++k) {
                const int lo = N_::b_off(j), hi = lo + N_::BBYTES;
                if (lo < G_::a_off(k) + G_::ABYTES && G_::a_off(k) < hi) ok = false;
                if (lo < G_::b_off(k) + G_::BBYTES && G_::b_off(k) < hi) ok = false;
            }
            if (ok) { push(OP_N, j); cnt += N_::GB; ndone[j] = true; }
        }
    };
    for (int k = 0; k < G_::PA; ++k) item(0);
    sc.xw = -1;
    for (int s = 0; s < G_::NT; ++s) {
        push(OP_W, s); sc.pend[sc.n - 1] = cnt - lastop[s] < 63 ? cnt - lastop[s] : 63;
        if (G_::AUX && sc.xw < 0 && xpos && lastop[s] > xpos) sc.xw = s;
        for (int q = 0; q < G_::G;) { const int r = item(s); if (!r) break; q += r; }
        nextb(s);
    }
    push(OP_E, 0);
    nextb(G_::NT);
    return sc;
}
template <int PH> struct NextPh { static constexpr int v = PH == PH_WO ? PH_GLU : PH == PH_GLU ? PH_DN : PH_QKV; };
template <int PH> struct SchedOf { static constexpr Sched v = make_sched<PH, NextPh<PH>::v>(); };

template <int PH> DI void issue_b(const char* ws, int k, int cb, unsigned lds0, int wid, unsigned vo) {
    using G_ = Geo<PH>;
    glds_run<G_::GB>(ws + G_::WP + (size_t)cb * (G_::NT * G_::BBYTES) + k * G_::BBYTES + wid * G_::GB * 1024, vo, lds0 + G_::b_off(k) + wid * G_::GB * 1024);
}
template <int PH> DI void prefetch_b(char* ws, unsigned lds0, int wid, int lane) {
    FRESH(ws, lds0, wid, lane, bid);
    int rb, cb; Geo<PH>::tile(bid, rb, cb);
#pragma unroll
    for (int k = 0; k < Geo<PH>::NS; ++k) issue_b<PH>(ws, k, cb, lds0, wid, lane * 16);
}

template <int PH> DI void gemm_load(char* ws, int layer, unsigned lds0, int wid, int lane, bool no_next) {
    using G_ = Geo<PH>;
    constexpr int NX = NextPh<PH>::v;
    constexpr int MT = G_::MT, NT = G_::NT, KS = G_::KS, GA = G_::GA;
    ST_DECL ST(0);
    FRESH(ws, lds0, wid, lane, bid);
    int rb, cb, nrb, ncb; G_::tile(bid, rb, cb); Geo<NX>::tile(bid, nrb, ncb);
    const unsigned lw = layer * WS_LAYER;
    const unsigned vo = lane * 16;
    const char* sA = ws + lw + G_::A + (size_t)(rb * MT + (wid * GA) / KS) * (64 * G_::KTOT) + ((wid * GA) % KS) * 1024;
    ST(1);
    asm volatile("s_barrier" ::: "memory");
    ST(2);
    f32x4 stp[4];
    static_for<SchedOf<PH>::v.n>([&](auto ic) {
        constexpr int i = decltype(ic)::value, op = SchedOf<PH>::v.op[i], k = SchedOf<PH>::v.arg[i];
        if constexpr (op == OP_A || op == OP_F) glds_run<GA>(sA + k * (KS * 1024), vo, lds0 + G_::a_off(k) + wid * GA * 1024);
        if constexpr (op == OP_F) issue_b<PH>(ws, k, cb, lds0, wid, vo);
        if constexpr (op == OP_N) {
            if (SchedOf<PH>::v.op[SchedOf<PH>::v.n - 1] != OP_N || i < SchedOf<PH>::v.n - 1 || !no_next) issue_b<NX>(ws, k, ncb, lds0, wid, vo);
        }
        if constexpr (op == OP_X) {
            const char* sp = ws + lw + (PH == PH_QKV ? WS_STA : WS_STB) + (size_t)(rb * MT + wid % MT) * 4096;
            const unsigned so = (lane >> 5) * 2048 + (lane & 31) * 16;
            asm volatile("s_nop 4\n\tglobal_load_dwordx4 %0, %4, %5 sc1\n\tglobal_load_dwordx4 %1, %4, %5 offset:512 sc1\n\t"
                         "global_load_dwordx4 %2, %4, %5 offset:1024 sc1\n\tglobal_load_dwordx4 %3, %4, %5 offset:1536 sc1"
                         : "=&v"(stp[0]), "=&v"(stp[1]), "=&v"(stp[2]), "=&v"(stp[3]) : "v"(so), "s"(sp) : "memory");
        }
        if constexpr (op == OP_W) {
            if constexpr (G_::LN && k == SchedOf<PH>::v.xw + 1) {
                asm volatile("" : "+v"(stp[0]), "+v"(stp[1]), "+v"(stp[2]), "+v"(stp[3]));
                const f32x4 t = (stp[0] + stp[1]) + (stp[2] + stp[3]);
                const float mean = half_sum(t[0] + t[2]) * (1.0f / DM), var = fmaxf(half_sum(t[1] + t[3]) * (1.0f / DM) - mean * mean, 0.f);
                if (lane < 32) ((LAS f32x2*)(uintptr_t)(lds0 + G_::ROWF))[(wid % MT) * 32 + lane] = f32x2{mean, __builtin_amdgcn_rsqf(var + LN_EPS)};
            }
            wait_vm_barrier<SchedOf<PH>::v.pend[i]>();
            if constexpr (k == 0) ST(3);
            if constexpr (k == NT - 1) ST(4);
        }
        if constexpr (op == OP_E) {
            static_assert(i >= SchedOf<PH>::v.n - 2, "at most one weight stage behind the end barrier");
            if (no_next) asm volatile("s_waitcnt vmcnt(0)" ::: "memory");
            lds_barrier(); ST(5);
        }
    });
    ST_OUT(layer * 5 + PH, 0);
}

template <int PH> DI void gemm_comp(char* ws, rsrc_t rws, rsrc_t rout, int layer, unsigned lds0, int cw, int lane, bool& poller) {
    using G_ = Geo<PH>;
    constexpr int MT = G_::MT, WN = G_::WN, NS = G_::NS, NT = G_::NT, KS = G_::KS, KG = G_::KG, UPS = G_::UPS, NU = G_::NU, TM = G_::TM, TN = G_::TN;
    constexpr int PBYTES = G_::PBYTES;
    constexpr bool LN = G_::LN;
    constexpr int LOAD_U = (PH == PH_GLU ? 2 : PH == PH_WO ? 1 : NT - NS + 1) * UPS;
    constexpr int CORR_U = PH == PH_GLU ? 10 * UPS : NU - 2;
    static_assert(!LN || (SchedOf<PH>::v.xw >= 0 && CORR_U >= (SchedOf<PH>::v.xw + 1) * UPS), "row table must be ready");
    ST_DECL ST(0);
    FRESH(ws, lds0, cw, lane, bid);
    int rb, cb; G_::tile(bid, rb, cb);
    const unsigned lw = layer * WS_LAYER;
    const int r32 = lane & 31, hh = lane >> 5, wm = cw / WN, wn = cw % WN;
    if (poller) {
        if constexpr (PH == PH_QKV) wait_flags(ws, WS_FL + FL_X2 + rb * 256, 8, lane);
        if constexpr (PH == PH_WO) wait_flags(ws, lw + WS_FL + FL_O + (rb >> 2) * 256, 32, lane);
        if constexpr (PH == PH_GLU) wait_flags(ws, lw + WS_FL + FL_X1 + rb * 256, 16, lane);
        if constexpr (PH == PH_DN) wait_flags(ws, lw + WS_FL + FL_H + (rb >> 1) * 256, 14, lane);
    }
    lds_barrier();
    LAS f32x2* rowf = (LAS f32x2*)(uintptr_t)(lds0 + G_::ROWF);
    u32x4 resid[2];
    float cpart[TN];
    unsigned aoff = lds0 + wm * TM * KS * 1024 + lane * 16;
    unsigned boff = lds0 + (wn >> 1) * PBYTES + tr_lane_off(lane, wn & 1);
    unsigned aoff2 = aoff + 49152, boff2 = boff + 49152, aoff3 = aoff + 98304, boff3 = boff + 98304;
    asm volatile("" : "+v"(aoff2), "+v"(boff2), "+v"(aoff3), "+v"(boff3));
    constexpr int TSTR = (WN / 2) * PBYTES;

    f32x16 acc[TM][TN];
#pragma unroll
    for (int i = 0; i < TM; ++i)
#pragma unroll
        for (int t = 0; t < TN; ++t) acc[i][t] = f32x16{};
    float rstd[TM];
    constexpr int NF = KG * (TM + TN), NM = KG * TM * TN;
    bf16x8 fr[2][NF];
    auto rd = [&](auto uc, auto fc) {
        constexpr int u = decltype(uc)::value, f = decltype(fc)::value;
        constexpr int ks = (u % UPS) * KG + f / (TM + TN), e = f % (TM + TN);
        constexpr int so = e < TN ? G_::b_off(u / UPS) : G_::a_off(u / UPS), hi = so / 49152, lo = so % 49152;
        if constexpr (e < TN) fr[u & 1][f] = lds_tr_frag((hi == 0 ? boff : hi == 1 ? boff2 : boff3) + lo + e * TSTR + ks * 2048);
        else fr[u & 1][f] = lds_b128((hi == 0 ? aoff : hi == 1 ? aoff2 : aoff3) + lo + ((e - TN) * KS + ks) * 1024);
    };
    auto mma = [&](auto tc, f32x16& c, const bf16x8& af, const bf16x8& bf) {
        if constexpr (PH == PH_QKV && decltype(tc)::value == 2) c = MFMA32(af, bf, c);
        else c = MFMA32(bf, af, c);
    };
    ST(1);
    lds_barrier();
    ST(2); STC(6);
    static_for<NF>([&](auto fc) { rd(std::integral_constant<int, 0>{}, fc); });
    SBAR();
    static_for<NU>([&](auto uc) {
        constexpr int u = decltype(uc)::value;
        if constexpr (u == LOAD_U) {
            if constexpr (!LN) {
#pragma unroll
                for (int e = 0; e < 2; ++e) resid[e] = ld16(rws, lane * 16 + e * 1024, lw + (PH == PH_WO ? WS_XA : WS_XB) + (rb * MT + wm) * 32768 + (cb * 8 + wn * 4) * 512);
            } else {
                constexpr int NCOL = PH == PH_QKV ? 3 * DM : 2 * FF;
#pragma unroll
                for (int t = 0; t < TN; ++t)
                    cpart[t] = __uint_as_float(__builtin_amdgcn_raw_buffer_load_b32(rws, (hh * NCOL + slot_col(r32)) * 4,
                                                                                    (PH == PH_QKV ? WS_CSQ : WS_CSG) + (t * (NCOL / TN) + cb * (32 * WN) + wn * 32) * 4, 0));
            }
            SBAR();
        }
        if constexpr (LN && u == CORR_U) {
            bf16x8 wf[TN];
#pragma unroll
            for (int t = 0; t < TN; ++t) wf[t] = corr_frag(half_sum(cpart[t]), false, hh);
#pragma unroll
            for (int i = 0; i < TM; ++i) {
                const f32x2 st = rowf[(wm * TM + i) * 32 + r32];
                rstd[i] = st.y;
                const bf16x8 xf = corr_frag(-st.x, true, hh);
                static_for<TN>([&](auto tc) { mma(tc, acc[i][decltype(tc)::value], xf, wf[decltype(tc)::value]); });
            }
            SBAR();
        }
        if constexpr (u + 1 < NU && (u + 1) % UPS == 0) lds_barrier();
        static_for<NM>([&](auto mc) {
            constexpr int m = decltype(mc)::value, g = m / (TM * TN), i = (m % (TM * TN)) / TN, t = m % TN;
            mma(std::integral_constant<int, t>{}, acc[i][t], fr[u & 1][g * (TM + TN) + TN + i], fr[u & 1][g * (TM + TN) + t]);
            if constexpr (u + 1 < NU) {
                SBAR();
                constexpr int PER = (NF + NM - 1) / NM < 2 ? 2 : (NF + NM - 1) / NM;
                static_for<PER>([&](auto kc) {
                    constexpr int f = m * PER + decltype(kc)::value;
                    if constexpr (f < NF) rd(std::integral_constant<int, u + 1>{}, std::integral_constant<int, f>{});
                });
                SBAR();
            }
        });
    });

    STC(7); ST(3);
    if constexpr (PH == PH_QKV) {
        const int tile = rb * MT + wm;
        const int head0 = cb * 4 + wn * 2;
        const float rq = rstd[0] * QSCALE, rk = rstd[0];
#pragma unroll
        for (int e = 0; e < 2; ++e) {
            u32x4 q, k;
#pragma unroll
            for (int j = 0; j < 4; ++j) {
                q[j] = pack_bf16(rq * acc[0][0][8 * e + 2 * j], rq * acc[0][0][8 * e + 2 * j + 1]);
                k[j] = pack_bf16(rk * acc[0][1][8 * e + 2 * j], rk * acc[0][1][8 * e + 2 * j + 1]);
            }
            st16(rws, lane * 16, lw + WS_KV + ((head0 + e) * 128 + tile) * 1024, k);
            st16(rws, lane * 16, lw + WS_Q + ((head0 + e) * 64 + tile) * 1024, q);
        }
#pragma unroll
        for (int e = 0; e < 2; ++e) {
            u32x4 o;
#pragma unroll
            for (int j = 0; j < 4; ++j) {
                const f32x4 st = *(LAS f32x4*)&rowf[wm * 32 + 16 * e + 8 * (j >> 1) + 4 * hh + 2 * (j & 1)];
                o[j] = pack_bf16(st[1] * acc[0][2][8 * e + 2 * j], st[3] * acc[0][2][8 * e + 2 * j + 1]);
            }
            st16(rws, (r32 >> 4) * 131072 + e * 512 + hh * 256 + (r32 & 15) * 16, lw + WS_KV + (head0 * 128 + 64 + tile) * 1024, o);
        }
        lds_barrier();
        ST(4);
        poller = publish(ws, lw + WS_FL + FL_QKV + cb * 256 + rb * 4, lds0 + LDS_CNT, lane);
    } else if constexpr (PH == PH_GLU) {
#pragma unroll
        for (int i = 0; i < TM; ++i) {
            const float r2 = rstd[i] * rstd[i];
#pragma unroll
            for (int e = 0; e < 2; ++e) {
                u32x4 o;
#pragma unroll
                for (int j = 0; j < 4; ++j) {
                    const int r = 8 * e + 2 * j;
                    o[j] = pack_bf16(relu(acc[i][0][r]) * (r2 * acc[i][1][r]), relu(acc[i][0][r + 1]) * (r2 * acc[i][1][r + 1]));
                }
                st16(rws, lane * 16 + e * 1024, lw + WS_H + (rb * MT + i) * (64 * FF) + (cb * 16 + wn * 4) * 512, o);
            }
        }
        lds_barrier();
        ST(4);
        poller = publish(ws, lw + WS_FL + FL_H + rb * 256 + cb * 4, lds0 + LDS_CNT, lane);
    } else {
        const bool last = PH == PH_DN && layer == 1;
        const unsigned rbo = (rb * MT + wm) * 32768;
        float sum = 0.f, sq = 0.f;
#pragma unroll
        for (int e = 0; e < 2; ++e) {
            u32x4 o;
#pragma unroll
            for (int j = 0; j < 4; ++j) {
                const float a = bf_lo(resid[e][j]) + acc[0][0][8 * e + 2 * j], b = bf_hi(resid[e][j]) + acc[0][0][8 * e + 2 * j + 1];
                o[j] = pack_bf16(a, b);
                const float ar = bf_lo(o[j]), br = bf_hi(o[j]);
                sum += ar + br; sq = fmaf(ar, ar, fmaf(br, br, sq));
            }
            if (last) __builtin_amdgcn_raw_buffer_store_b128(o, rout, r32 * 1024 + (2 * e + hh) * 16 + rbo + (cb * 8 + wn * 4) * 16, 0, 0);
            else st16(rws, lane * 16 + e * 1024, (PH == PH_WO ? lw + WS_XB : WS_LAYER + WS_XA) + rbo + (cb * 8 + wn * 4) * 512, o);
        }
        if (!last) {
            sum = half_sum(sum); sq = half_sum(sq);
            if (hh == 0) __builtin_amdgcn_raw_buffer_store_b64(u32x2{__float_as_uint(sum), __float_as_uint(sq)}, rws,
                                                               r32 * 16 + (PH == PH_WO ? lw + WS_STB : WS_LAYER + WS_STA) + (rb * MT + wm) * 4096 + cb * 512 + wn * 8, 0, 16);
        }
        lds_barrier();
        ST(4);
        if (!last) {
            if constexpr (PH == PH_WO) poller = publish(ws, lw + WS_FL + FL_X1 + (rb >> 1) * 256 + ((rb & 1) * 8 + cb) * 4, lds0 + LDS_CNT, lane);
            else poller = publish(ws, WS_FL + FL_X2 + rb * 256 + cb * 4, lds0 + LDS_CNT, lane);
        }
    }
    ST(5); ST_OUT(layer * 5 + PH, 1);
}

DI void load_unit(u32x4 (&v)[4], const char* src, int ld2, int lane, bool swz) {
    const int ch = swz ? ((lane & 7) ^ (((lane >> 4) & 1) << 2)) : (lane & 7);
    const char* a = src + (size_t)(lane >> 3) * ld2 + ch * 16;
#pragma unroll
    for (int i = 0; i < 4; ++i) v[i] = *(const u32x4*)(a + (size_t)i * 8 * ld2);
}
DI void store_unit_packed(const u32x4 (&v)[4], rsrc_t rws, unsigned dst, int lane) {
#pragma unroll
    for (int i = 0; i < 4; ++i) st16(rws, lane * 16 + i * 1024, dst, v[i]);
}
DI rsrc_t make_rsrc(void* p, unsigned bytes) { return __builtin_amdgcn_make_buffer_rsrc(p, 0, bytes, 0x00020000); }

struct PrepArgs { const char* x; const char* wqkv; const char* wg; const char* wu; char* ws; };

__global__ __launch_bounds__(512) void prep_kernel(PrepArgs p) {
    __shared__ float red[8][64];
    const int tid = threadIdx.x, lane = tid & 63, wid = __builtin_amdgcn_readfirstlane(tid >> 6);
    const int b = blockIdx.x;
    const rsrc_t rws = make_rsrc(p.ws, WS_END);
    u32x4 v[4];
    if (b >= 224) {
        const unsigned o = ((b - 224) & 3) * 8192 + tid * 16;
        if (o < FL_BYTES) st16(rws, o, ((b - 224) >> 2) * WS_LAYER + WS_FL, u32x4{0, 0, 0, 0});
        return;
    }
    if (b < 64) {
        load_unit(v, p.x + (size_t)b * 32768 + wid * 128, 1024, lane, false);
#pragma unroll
        for (int i = 0; i < 4; ++i) {
            const int row = i * 8 + (lane >> 3);
            st16(rws, (lane & 7) * 512 + row * 16, WS_XA + b * 32768 + wid * 4096, v[i]);
            float sum = 0.f, sq = 0.f;
#pragma unroll
            for (int j = 0; j < 4; ++j) { const float lo = bf_lo(v[i][j]), hi = bf_hi(v[i][j]); sum += lo + hi; sq = fmaf(lo, lo, fmaf(hi, hi, sq)); }
            sum += __shfl_xor(sum, 1); sq += __shfl_xor(sq, 1);
            sum += __shfl_xor(sum, 2); sq += __shfl_xor(sq, 2);
            if ((lane & 3) == 0) *(f32x2*)(p.ws + WS_STA + (size_t)b * 4096 + wid * 512 + row * 16 + ((lane >> 2) & 1) * 8) = f32x2{sum, sq};
        }
        return;
    }
    const int j = b - 64, half = j & 1, pan = j >> 1, kb = half * 8 + wid;
    const bool isq = pan < 24;
    const char* src = isq ? p.wqkv + (size_t)kb * 32 * 3072 + pan * 128 : (pan < 52 ? p.wg + (pan - 24) * 128 : p.wu + (pan - 52) * 128) + (size_t)kb * 32 * (FF * 2);
    load_unit(v, src, isq ? 3072 : FF * 2, lane, true);
    if (isq) store_unit_packed(v, rws, (unsigned)Geo<PH_QKV>::packed_off(pan & 7, pan >> 3, kb), lane);
    float c[8];
#pragma unroll
    for (int e = 0; e < 8; ++e) c[e] = 0.f;
#pragma unroll
    for (int i = 0; i < 4; ++i)
#pragma unroll
        for (int e = 0; e < 4; ++e) { c[2 * e] += bf_lo(v[i][e]); c[2 * e + 1] += bf_hi(v[i][e]); }
#pragma unroll
    for (int e = 0; e < 8; ++e) { c[e] += __shfl_xor(c[e], 8); c[e] += __shfl_xor(c[e], 20); c[e] = half_sum(c[e]); }
    if (lane < 8) { *(f32x4*)&red[wid][lane * 8] = f32x4{c[0], c[1], c[2], c[3]}; *(f32x4*)&red[wid][lane * 8 + 4] = f32x4{c[4], c[5], c[6], c[7]}; }
    __syncthreads();
    if (tid < 64) {
        float t = 0.f;
#pragma unroll
        for (int w = 0; w < 8; ++w) t += red[w][tid];
        if (isq) ((float*)(p.ws + WS_CSQ))[half * (3 * DM) + pan * 64 + tid] = t;
        else ((float*)(p.ws + WS_CSG))[half * (2 * FF) + (pan - 24) * 64 + tid] = t;
    }
}

#define AT_E(C, i) "v_exp_f32 v[" #C "+" #i "], v[" #C "+" #i "]\n\t"
#define AT_C(P, C, i) "v_cvt_pk_bf16_f32 v[" #P "], v[" #C "+" #i "], v[" #C "+" #i "+1]\n\t"
#define AT_HALF(C, O, V0, PRE, MID)                                                                               \
    AT_E(C, 0) AT_E(C, 1) PRE AT_E(C, 2) AT_E(C, 3) AT_E(C, 4) AT_E(C, 5) MID                                     \
    AT_E(C, 6) AT_E(C, 7) AT_C(108, C, 0) AT_C(109, C, 2) AT_C(110, C, 4) AT_E(C, 8) AT_C(111, C, 6) AT_E(C, 9) AT_E(C, 10) \
    "v_mfma_f32_32x32x16_bf16 " O ", v[" #V0 ":" #V0 "+3], v[108:111], " O "\n\t"                                 \
    AT_E(C, 11) AT_E(C, 12) AT_E(C, 13) AT_E(C, 14) AT_E(C, 15)                                                   \
    AT_C(112, C, 8) AT_C(113, C, 10) AT_C(114, C, 12) AT_C(115, C, 14)
#define AT_TILE(U, V0C, V0N, SYNC)                                                                                \
    AT_HALF(64, "%[o0]", V0C,                                                                                     \
            "v_mfma_f32_32x32x16_bf16 %[o1], v[104:107], v[112:115], %[o1]\n\t"                                   \
            "ds_read_b128 v[104:107], %[va] offset:" #U "*1024+512\n\t",                                          \
            "v_mfma_f32_32x32x16_bf16 v[80:95], v[96:99], %[q1], %[nm1]\n\t" SYNC                                 \
            "ds_read_b128 v[96:99], %[ka] offset:" #U "*1024+1024\n\t"                                            \
            "ds_read_b128 v[" #V0N ":" #V0N "+3], %[va] offset:" #U "*1024+1024\n\t")                             \
    AT_HALF(80, "%[o1]", V0C,                                                                                     \
            "s_waitcnt lgkmcnt(0)\n\t"                                                                            \
            "v_mfma_f32_32x32x16_bf16 %[o0], v[104:107], v[112:115], %[o0]\n\t",                                  \
            "v_mfma_f32_32x32x16_bf16 v[64:79], v[96:99], %[q0], %[nm0]\n\t")

DI f32x16 neg_tile_max(const bf16x8& k0, const bf16x8& q) {
    const f32x16 s0 = MFMA32(k0, q, f32x16{});
    float m = s0[0];
#pragma unroll
    for (int r = 1; r < 16; ++r) m = fmaxf(m, s0[r]);
    m = half_max(m);
    f32x16 n;
#pragma unroll
    for (int r = 0; r < 16; ++r) n[r] = -m;
    return n;
}
DI f32x16 attn_exact(const bf16x8& q, rsrc_t rws, unsigned kv, unsigned ones, int lane) {
    const bool vlane = (lane & 31) < 16;
    const bf16x8 pad = lds_b128(ones);
    float m = -INFINITY;
    f32x16 o = f32x16{};
#pragma unroll 1
    for (int t = 0; t < 64; ++t) {
        const bf16x8 kf = __builtin_bit_cast(bf16x8, ld16(rws, lane * 16, kv + t * 1024));
        const unsigned vo = (lane >> 5) * 256 + (lane & 15) * 16;
        bf16x8 v0 = __builtin_bit_cast(bf16x8, ld16(rws, vo, kv + 65536 + t * 1024)), v1 = __builtin_bit_cast(bf16x8, ld16(rws, vo + 512, kv + 65536 + t * 1024));
        if (!vlane) { v0 = pad; v1 = pad; }
        const f32x16 s = MFMA32(kf, q, f32x16{});
        float tm = s[0];
#pragma unroll
        for (int r = 1; r < 16; ++r) tm = fmaxf(tm, s[r]);
        const float mn = fmaxf(m, half_max(tm));
        const float alpha = __builtin_amdgcn_exp2f(m - mn);
        m = mn;
#pragma unroll
        for (int r = 0; r < 16; ++r) o[r] *= alpha;
        u32x4 p0, p1;
#pragma unroll
        for (int j = 0; j < 4; ++j) {
            p0[j] = pack_bf16(__builtin_amdgcn_exp2f(s[2 * j] - m), __builtin_amdgcn_exp2f(s[2 * j + 1] - m));
            p1[j] = pack_bf16(__builtin_amdgcn_exp2f(s[8 + 2 * j] - m), __builtin_amdgcn_exp2f(s[8 + 2 * j + 1] - m));
        }
        o = MFMA32(v0, __builtin_bit_cast(bf16x8, p0), o);
        o = MFMA32(v1, __builtin_bit_cast(bf16x8, p1), o);
    }
    return o;
}

struct MkArgs { const char* Wg; const char* Wu; const char* Wo; const char* Wd; char* ws; char* out; };

DI void attn_load(const MkArgs& p, rsrc_t rws, int layer, unsigned lds0, int wid, int lane) {
    ST_DECL ST(0);
    char* ws = p.ws;
    FRESH(ws, lds0, wid, lane, bid);
    const int x = bid & 7, idx = bid >> 3, head = x * 4 + (idx >> 3), qb = idx & 7;
    const unsigned lw = layer * WS_LAYER;
    const unsigned vo = lane * 16;
    const char* src = ws + lw + WS_KV + (size_t)head * 131072 + wid * 2048;
    auto chunk = [&](int c) {
        glds_run<2>(src + c * 8192, vo, lds0 + AT_K + c * 8192 + wid * 2048);
        glds_run<2>(src + 65536 + c * 8192, vo, lds0 + AT_V + c * 8192 + wid * 2048);
    };
    ST(1);
    asm volatile("s_barrier" ::: "memory");
    ST(2);
    glds_run<2>(ws + lw + WS_Q + (size_t)(head * 64 + qb * 8 + wid * 2) * 1024, vo, lds0 + AT_Q + wid * 2048);
    chunk(0); chunk(1);
    wait_vm_barrier<4>();
    ST(3);
    chunk(2); chunk(3); chunk(4); chunk(5); chunk(6); chunk(7);
    wait_vm_barrier<0>();
    ST(4);
    if (layer == 0) {
#pragma unroll 1
        for (int u = bid * 4 + wid; u < 1472; u += 1024) {
            const char* s; size_t dst; int ld2;
            if (u < 896) { const int mat = u >= 448, v = u - mat * 448, pan = v >> 4, kb = v & 15;
                s = (mat ? p.Wu : p.Wg) + (size_t)kb * 32 * (FF * 2) + pan * 128; ld2 = FF * 2; dst = Geo<PH_GLU>::packed_off(pan >> 1, mat * 2 + (pan & 1), kb); }
            else if (u < 1024) { const int v = u - 896, pan = v & 7, kb = v >> 3;
                s = p.Wo + (size_t)kb * 32 * 1024 + pan * 128; ld2 = 1024; dst = Geo<PH_WO>::packed_off(pan, 0, kb); }
            else { const int v = u - 1024, pan = v & 7, kb = v >> 3;
                s = p.Wd + (size_t)kb * 32 * 1024 + pan * 128; ld2 = 1024; dst = Geo<PH_DN>::packed_off(pan, 0, kb); }
            u32x4 v[4];
            load_unit(v, s, ld2, lane, true);
            store_unit_packed(v, rws, (unsigned)dst, lane);
        }
        if (publish(ws, WS_FL + FL_PK + bid * 4, lds0 + LDS_CNT + 4, lane)) {
            __builtin_amdgcn_s_sleep(127);
#pragma unroll 1
            for (int i = 0; i < 4; ++i) wait_flags(ws, WS_FL + FL_PK + i * 256, 64, lane);
        }
    }
    lds_barrier();
    prefetch_b<PH_WO>(ws, lds0, wid, lane);
    lds_barrier();
    ST(5); ST_OUT(layer * 5 + PH_ATT, 0);
}

DI void attn_comp(char* ws, rsrc_t rws, int layer, unsigned lds0, int cw, int lane, bool& poller) {
    ST_DECL ST(0);
    FRESH(ws, lds0, cw, lane, bid);
    const int x = bid & 7, idx = bid >> 3, head = x * 4 + (idx >> 3), qb = idx & 7;
    const unsigned lw = layer * WS_LAYER;
    if (poller) wait_flags(ws, lw + WS_FL + FL_QKV + (head >> 2) * 256, 32, lane);
    lds_barrier();
    const int r32 = lane & 31, hh = lane >> 5;
    const unsigned ka0 = lds0 + AT_K + lane * 16;
    const bool vlane = r32 < 16;
    const unsigned va0 = lds0 + (vlane ? AT_V : AT_ONES) + hh * 256 + (r32 & 15) * 16;
    const unsigned vstep = vlane ? 8192u : 0u;

    ST(1);
    lds_barrier();
    ST(2);
    const bf16x8 q0 = lds_b128(lds0 + AT_Q + cw * 2048 + lane * 16), q1 = lds_b128(lds0 + AT_Q + cw * 2048 + 1024 + lane * 16);
    const bf16x8 k0 = lds_b128(ka0);
    const f32x16 nm0 = neg_tile_max(k0, q0), nm1 = neg_tile_max(k0, q1);
    f32x16 o0 = f32x16{}, o1 = f32x16{};
    STC(6);
    {
        unsigned ka = ka0, va = va0; int cnt, tmp;
        asm volatile(
            "ds_read_b128 v[96:99], %[ka]\n\t"
            "ds_read_b128 v[100:103], %[va]\n\t"
            "v_mov_b32 v104, 0\n\tv_mov_b32 v105, 0\n\tv_mov_b32 v106, 0\n\tv_mov_b32 v107, 0\n\t"
            "v_mov_b32 v112, 0\n\tv_mov_b32 v113, 0\n\tv_mov_b32 v114, 0\n\tv_mov_b32 v115, 0\n\t"
            "s_mov_b32 %[cnt], 0\n\t"
            "s_waitcnt lgkmcnt(0)\n\t"
            "v_mfma_f32_32x32x16_bf16 v[64:79], v[96:99], %[q0], %[nm0]\n\t"
            "s_nop 12\n\t"
            "1:\n\t"
            AT_TILE(0, 100, 116, "") AT_TILE(1, 116, 100, "") AT_TILE(2, 100, 116, "") AT_TILE(3, 116, 100, "")
            AT_TILE(4, 100, 116, "") AT_TILE(5, 116, 100, "") AT_TILE(6, 100, 116, "")
            AT_TILE(7, 116, 100, "s_and_b32 %[tmp], %[cnt], 3\n\ts_cbranch_scc1 2f\n\ts_barrier\n\t2:\n\t")
            "v_add_u32 %[ka], 0x2000, %[ka]\n\t"
            "v_add_u32 %[va], %[vstep], %[va]\n\t"
            "s_add_u32 %[cnt], %[cnt], 1\n\t"
            "s_cmp_lt_u32 %[cnt], 8\n\t"
            "s_cbranch_scc1 1b\n\t"
            "v_mfma_f32_32x32x16_bf16 %[o1], v[104:107], v[112:115], %[o1]\n\t"
            "s_nop 15\n\t"
            : [o0] "+v"(o0), [o1] "+v"(o1), [ka] "+v"(ka), [va] "+v"(va), [cnt] "=&s"(cnt), [tmp] "=&s"(tmp)
            : [q0] "v"(q0), [q1] "v"(q1), [nm0] "v"(nm0), [nm1] "v"(nm1), [vstep] "v"(vstep)
            : "memory", "scc", "v64", "v65", "v66", "v67", "v68", "v69", "v70", "v71", "v72", "v73", "v74", "v75", "v76", "v77", "v78", "v79",
              "v80", "v81", "v82", "v83", "v84", "v85", "v86", "v87", "v88", "v89", "v90", "v91", "v92", "v93", "v94", "v95",
              "v96", "v97", "v98", "v99", "v100", "v101", "v102", "v103", "v104", "v105", "v106", "v107",
              "v108", "v109", "v110", "v111", "v112", "v113", "v114", "v115", "v116", "v117", "v118", "v119");
    }
    STC(7); ST(3);
    if (__builtin_expect(__any(!(o0[8] < 1.0e18f) || !(o1[8] < 1.0e18f)), 0)) {
        o0 = attn_exact(q0, rws, lw + WS_KV + head * 131072, lds0 + AT_ONES + (r32 & 15) * 16, lane);
        o1 = attn_exact(q1, rws, lw + WS_KV + head * 131072, lds0 + AT_ONES + (r32 & 15) * 16, lane);
    }
    const float i0 = 1.0f / o0[8], i1 = 1.0f / o1[8];
    u32x4 w0, w1;
#pragma unroll
    for (int j = 0; j < 4; ++j) { w0[j] = pack_bf16(o0[2 * j] * i0, o0[2 * j + 1] * i0); w1[j] = pack_bf16(o1[2 * j] * i1, o1[2 * j + 1] * i1); }
    const int qtile = qb * 8 + cw * 2;
    st16(rws, lane * 16, lw + WS_O + qtile * 32768 + head * 1024, w0);
    st16(rws, lane * 16, lw + WS_O + (qtile + 1) * 32768 + head * 1024, w1);
    lds_barrier();
    ST(4);
    poller = publish(ws, lw + WS_FL + FL_O + qb * 256 + head * 4, lds0 + LDS_CNT, lane);
    ST(5); ST_OUT(layer * 5 + PH_ATT, 1);
}

__global__ __launch_bounds__(512) void mk_kernel(MkArgs p) {
    extern __shared__ __attribute__((aligned(16))) char smem[];
    const unsigned lds0 = (unsigned)(uintptr_t)(LAS char*)smem;
    const int tid = threadIdx.x, lane = tid & 63, wid = __builtin_amdgcn_readfirstlane(tid >> 6);
    const rsrc_t rws = make_rsrc(p.ws, WS_END);
    const bool glu = (blockIdx.x >> 3) < 28;
    if (wid < 4) {
        prefetch_b<PH_QKV>(p.ws, lds0, wid, lane);
#pragma unroll 1
        for (int layer = 0; layer < 2; ++layer) {
            gemm_load<PH_QKV>(p.ws, layer, lds0, wid, lane, false);
            attn_load(p, rws, layer, lds0, wid, lane);
            gemm_load<PH_WO>(p.ws, layer, lds0, wid, lane, !glu);
            if (glu) gemm_load<PH_GLU>(p.ws, layer, lds0, wid, lane, false);
            else prefetch_b<PH_DN>(p.ws, lds0, wid, lane);
            gemm_load<PH_DN>(p.ws, layer, lds0, wid, lane, layer == 1);
        }
    } else {
        const rsrc_t rout = make_rsrc(p.out, SEQ * DM * 2);
        {
            const unsigned ov = ((tid & 15) == 0 || (tid & 15) == 4) ? 0x3f803f80u : 0u;
            *(LAS u32x4*)(uintptr_t)(lds0 + AT_ONES + (tid - 256) * 16) = u32x4{ov, ov, ov, ov};
            *(LAS u32x4*)(uintptr_t)(lds0 + AT_ONES + tid * 16) = u32x4{ov, ov, ov, ov};
            if (tid < 320) *(LAS u32x4*)(uintptr_t)(lds0 + AT_ONES + (tid + 256) * 16) = u32x4{ov, ov, ov, ov};
            if (tid == 256) *(LAS u32x4*)(uintptr_t)(lds0 + LDS_CNT) = u32x4{0, 0, 0, 0};
        }
        bool poller = false;
#pragma unroll 1
        for (int layer = 0; layer < 2; ++layer) {
            gemm_comp<PH_QKV>(p.ws, rws, rout, layer, lds0, wid - 4, lane, poller);
            attn_comp(p.ws, rws, layer, lds0, wid - 4, lane, poller);
            gemm_comp<PH_WO>(p.ws, rws, rout, layer, lds0, wid - 4, lane, poller);
            if (glu) gemm_comp<PH_GLU>(p.ws, rws, rout, layer, lds0, wid - 4, lane, poller);
            gemm_comp<PH_DN>(p.ws, rws, rout, layer, lds0, wid - 4, lane, poller);
        }
    }
}

extern "C" void kernel_launch(void* const* d_in, const int* in_sizes, int n_in, void* d_out, int out_size, void* d_ws, size_t ws_size,
                              hipStream_t stream) {
    (void)hipFuncSetAttribute((const void*)mk_kernel, hipFuncAttributeMaxDynamicSharedMemorySize, LDS_BYTES);
    prep_kernel<<<232, 512, 0, stream>>>(PrepArgs{(const char*)d_in[0], (const char*)d_in[1], (const char*)d_in[4], (const char*)d_in[5], (char*)d_ws});
    mk_kernel<<<256, 512, LDS_BYTES, stream>>>(MkArgs{(const char*)d_in[4], (const char*)d_in[5], (const char*)d_in[2], (const char*)d_in[6], (char*)d_ws, (char*)d_out});
}
```

```cpp
#include <hip/hip_runtime.h>
#include <hip/hip_bf16.h>
#include <stdint.h>
#include <utility>

typedef short bf16x8 __attribute__((ext_vector_type(8)));
typedef float f32x16 __attribute__((ext_vector_type(16)));
typedef float f32x4 __attribute__((ext_vector_type(4)));
typedef float f32x2 __attribute__((ext_vector_type(2)));
typedef unsigned u32x4 __attribute__((ext_vector_type(4)));
typedef unsigned u32x2 __attribute__((ext_vector_type(2)));
typedef __bf16 bf16x2_t __attribute__((ext_vector_type(2)));
typedef __amdgpu_buffer_rsrc_t rsrc_t;
#define LAS __attribute__((address_space(3)))
#define GAS __attribute__((address_space(1)))
#define DI __device__ __forceinline__
#define MFMA32(a, b, c) __builtin_amdgcn_mfma_f32_32x32x16_bf16((a), (b), (c), 0, 0, 0)
#define SBAR() __builtin_amdgcn_sched_barrier(0)

constexpr int SEQ = 2048, DM = 512, FF = 1792;
constexpr float LN_EPS = 1e-5f;
constexpr float QSCALE = 0.25f * 1.4426950408889634f;

enum { PH_QKV = 0, PH_ATT = 1, PH_WO = 2, PH_GLU = 3, PH_DN = 4 };
constexpr unsigned MB = 1u << 20;
constexpr unsigned WS_LAYER = 20 * MB;
constexpr unsigned WS_Q = 0, WS_KV = 2 * MB, WS_O = 6 * MB, WS_XA = 8 * MB, WS_XB = 10 * MB, WS_H = 12 * MB, WS_STA = 19 * MB, WS_STB = WS_STA + MB / 4, WS_FL = WS_STB + MB / 4;
constexpr unsigned FR = 256;
constexpr unsigned FL_PH = 256 * FR, FL_PK = 5 * FL_PH, FL_EPOCH = FL_PK + 2048;
constexpr unsigned WS_WP = 40 * MB;
constexpr unsigned WP_QKV = WS_WP, WP_O = WP_QKV + DM * 3 * DM * 2, WP_GU = WP_O + DM * DM * 2, WP_D = WP_GU + DM * FF * 4;
constexpr unsigned WS_CSQ = WP_D + FF * DM * 2, WS_CSG = WS_CSQ + 3 * DM * 16, WS_END = WS_CSG + 2 * FF * 16;
typedef unsigned long long u64;

template <int... I, class Fn> DI void static_for_impl(std::integer_sequence<int, I...>, Fn&& f) { (f(std::integral_constant<int, I>{}), ...); }
template <int N, class Fn> DI void static_for(Fn&& f) { static_for_impl(std::make_integer_sequence<int, N>{}, f); }

DI unsigned pack_bf16(float lo, float hi) { bf16x2_t p; p.x = (__bf16)lo; p.y = (__bf16)hi; return __builtin_bit_cast(unsigned, p); }
DI float bf_lo(unsigned u) { return __uint_as_float(u << 16); }
DI float bf_hi(unsigned u) { return __uint_as_float(u & 0xffff0000u); }
template <int CTRL> DI float dpp(float v) { return __uint_as_float(__builtin_amdgcn_update_dpp(0u, __float_as_uint(v), CTRL, 0xf, 0xf, true)); }
DI float half_sum(float v) {
    auto r = __builtin_amdgcn_permlane32_swap(__float_as_uint(v), __float_as_uint(v), false, false);
    return __uint_as_float(r[0]) + __uint_as_float(r[1]);
}
DI float half_max(float v) {
    auto r = __builtin_amdgcn_permlane32_swap(__float_as_uint(v), __float_as_uint(v), false, false);
    return fmaxf(__uint_as_float(r[0]), __uint_as_float(r[1]));
}
DI void st16(rsrc_t r, unsigned voff, unsigned soff, u32x4 v) { __builtin_amdgcn_raw_buffer_store_b128(v, r, voff + soff, 0, 16); }
DI u32x4 ld16(rsrc_t r, unsigned voff, unsigned soff) { return __builtin_amdgcn_raw_buffer_load_b128(r, voff, soff, 16); }
template <int N> DI void wait_vm_barrier() { asm volatile("s_waitcnt vmcnt(%0) lgkmcnt(0)\n\ts_barrier" ::"i"(N) : "memory"); }
DI void lds_barrier() { asm volatile("s_waitcnt lgkmcnt(0)\n\ts_barrier" ::: "memory"); }
DI bf16x8 lds_b128(unsigned addr) { return *(const LAS bf16x8*)(uintptr_t)addr; }
DI bf16x8 lds_tr_frag(unsigned addr) {
    typedef short v4i16_t __attribute__((ext_vector_type(4)));
    v4i16_t lo = __builtin_amdgcn_ds_read_tr16_b64_v4i16((LAS v4i16_t*)(uintptr_t)addr);
    v4i16_t hi = __builtin_amdgcn_ds_read_tr16_b64_v4i16((LAS v4i16_t*)(uintptr_t)(addr + 512));
    return __builtin_shufflevector(lo, hi, 0, 1, 2, 3, 4, 5, 6, 7);
}
template <int N> DI void glds_run(const char* sbase, unsigned voff, unsigned lds_dst) {
    static_assert(N >= 1 && N <= 4, "");
#define GL(o) "\n\tglobal_load_lds_dwordx4 %0, %1 offset:" #o " sc1"
    if constexpr (N == 1) asm volatile("s_mov_b32 m0, %2\n\ts_nop 3" GL(0)::"v"(voff), "s"(sbase), "s"(lds_dst) : "memory");
    if constexpr (N == 2) asm volatile("s_mov_b32 m0, %2\n\ts_nop 3" GL(0) GL(1024)::"v"(voff), "s"(sbase), "s"(lds_dst) : "memory");
    if constexpr (N == 3) asm volatile("s_mov_b32 m0, %2\n\ts_nop 3" GL(0) GL(1024) GL(2048)::"v"(voff), "s"(sbase), "s"(lds_dst) : "memory");
    if constexpr (N == 4) asm volatile("s_mov_b32 m0, %2\n\ts_nop 3" GL(0) GL(1024) GL(2048) GL(3072)::"v"(voff), "s"(sbase), "s"(lds_dst) : "memory");
#undef GL
}
DI bool arrive(unsigned cnt_addr, int lane) {
    asm volatile("s_waitcnt vmcnt(0) lgkmcnt(0)" ::: "memory");
    unsigned old = 0;
    if (lane == 0) old = __hip_atomic_fetch_add((LAS unsigned*)(uintptr_t)cnt_addr, 1u, __ATOMIC_RELAXED, __HIP_MEMORY_SCOPE_WORKGROUP);
    asm volatile("" ::: "memory");
    return (__builtin_amdgcn_readfirstlane(old) & 3) == 3;
}
DI void signal(char* ws, unsigned first_line_off, int n, int lstride, int me, u64 tag, int lane) {
    if (lane < n) __hip_atomic_store((GAS u64*)(ws + first_line_off + lane * lstride * FR + me * 8), tag, __ATOMIC_RELAXED, __HIP_MEMORY_SCOPE_AGENT);
}
DI bool publish(char* ws, unsigned first_line_off, int n, int lstride, int me, u64 tag, unsigned cnt_addr, int lane) {
    const bool last = arrive(cnt_addr, lane);
    if (last) signal(ws, first_line_off, n, lstride, me, tag, lane);
    return last;
}
DI void wait_tags(const char* ws, unsigned off, int n, u64 tag, int lane) {
    const GAS u64* f = (const GAS u64*)(ws + off) + (lane < n ? lane : 0);
    const u64 t0 = __builtin_amdgcn_s_memrealtime();
    for (;;) {
        const u64 v = __hip_atomic_load(f, __ATOMIC_RELAXED, __HIP_MEMORY_SCOPE_AGENT);
        if (__all(v == tag)) break;
        if (__builtin_amdgcn_s_memrealtime() - t0 > 2000000ull) break;
        __builtin_amdgcn_s_sleep(1);
    }
    asm volatile("s_waitcnt vmcnt(0) lgkmcnt(0)" ::: "memory");
}

#define ST_DECL
#define ST(i)
#define STC(i)
#define ST_OUT(kind, role)

#define XC_Q 2
#define SIGU 2
#define STATU 11
#define CORRU 15
template <int PH> struct Cfg;
template <> struct Cfg<PH_QKV> { static constexpr int KTOT = 512, MT = 2, NP = 3, WM = 2, WN = 2, BK = 32, NS = 8, PA = 8, KG = 2, NCB = 8, XC = XC_Q; };
template <> struct Cfg<PH_WO> { static constexpr int KTOT = 512, MT = 2, NP = 1, WM = 2, WN = 2, BK = 64, NS = 8, PA = 4, KG = 4, NCB = 8, XC = 2; };
template <> struct Cfg<PH_GLU> { static constexpr int KTOT = 512, MT = 4, NP = 4, WM = 1, WN = 4, BK = 32, NS = 5, PA = 3, KG = 1, NCB = 14, XC = 2; };
template <> struct Cfg<PH_DN> { static constexpr int KTOT = FF, MT = 2, NP = 1, WM = 2, WN = 2, BK = 64, NS = 8, PA = 4, KG = 4, NCB = 8, XC = 2; };

constexpr int AT_K = 0, AT_V = 65536, AT_ONES = 131072, AT_Q = AT_ONES + 9216, LDS_ROWQ = AT_Q + 8192, LDS_ROWG = LDS_ROWQ + 1024, LDS_CNT = LDS_ROWG + 1024, LDS_BYTES = LDS_CNT + 16;

template <int PH> struct Geo : Cfg<PH> {
    using C = Cfg<PH>;
    static constexpr int TM = C::MT / C::WM, TN = C::NP * 2 / C::WN, NT = C::KTOT / C::BK, KS = C::BK / 16, UPS = KS / C::KG, NU = NT * UPS;
    static constexpr int ABYTES = C::MT * C::BK * 64, PBYTES = C::BK * 128, BBYTES = C::NP * PBYTES, SBYTES = ABYTES + BBYTES;
    static constexpr int GA = ABYTES / 4096, GB = BBYTES / 4096, G = GA + GB;
    static constexpr bool LN = PH == PH_QKV || PH == PH_GLU;
    static constexpr int ROWF = PH == PH_QKV ? LDS_ROWQ : LDS_ROWG;
    static constexpr int NRB = SEQ / (32 * C::MT), AUX = PH == PH_GLU ? 4 : 0;
    static constexpr unsigned WP = PH == PH_QKV ? WP_QKV : PH == PH_WO ? WP_O : PH == PH_GLU ? WP_GU : WP_D;
    static constexpr unsigned A = PH == PH_QKV ? WS_XA : PH == PH_WO ? WS_O : PH == PH_GLU ? WS_XB : WS_H;
    static_assert(C::WM * C::WN == 4 && GA * 4096 == ABYTES && GB * 4096 == BBYTES && GA <= KS && KS % GA == 0 && GB <= 4, "pieces must divide over the waves");
    static_assert(C::NS * SBYTES <= 131072 && C::NS <= NT, "LDS budget");
    static constexpr int a_off(int k) {
        if (PH == PH_WO) return (k < 4 ? 32768 : 65536) + k * 8192;
        return ((k + (PH == PH_QKV ? 5 : PH == PH_DN ? 1 : 0)) % C::NS) * SBYTES;
    }
    static constexpr int b_off(int k) { return PH == PH_WO ? (k < 4 ? 0 : 32768) + k * 8192 : a_off(k) + ABYTES; }
    static DI void tile(int b, int& rb, int& cb) {
        const int x = b & 7, i = b >> 3;
        constexpr int CH = C::NCB / C::XC, RH = NRB * C::XC / 8;
        rb = (x / C::XC) * RH + i / CH; cb = (x % C::XC) * CH + i % CH;
    }
    static DI size_t packed_off(int cb, int pan, int kb) {
        return (size_t)WP + (size_t)cb * (NT * BBYTES) + (size_t)((kb * 32) / C::BK) * BBYTES + pan * PBYTES + ((kb * 32) % C::BK) * 128;
    }
};

DI unsigned tr_lane_off(int lane, int j) {
    const int hh = lane >> 5, blk = (lane >> 4) & 1, q = (lane & 15) >> 2, pp = lane & 3, pi = ((pp & 1) << 1) | (pp >> 1);
    const int ch = (4 * j + 2 * blk + (pi >> 1)) ^ (((q >> 1) & 1) << 2);
    return (8 * hh + q) * 128 + ch * 16 + 8 * (pi & 1);
}
DI int slot_col(int r32) { const int pp = (r32 >> 2) & 3; return (r32 & 16) + 4 * (((pp & 1) << 1) | (pp >> 1)) + (r32 & 3); }
DI bf16x8 corr_frag(float v, bool second_lo, int hh) {
    const unsigned h = pack_bf16(v, 0.f) & 0xffffu, l = pack_bf16(v - bf_lo(h), 0.f) & 0xffffu;
    u32x4 u; u[0] = second_lo ? (h | (l << 16)) : (h | (h << 16)); u[1] = second_lo ? h : l; u[2] = 0; u[3] = 0;
    if (hh) { u[0] = 0; u[1] = 0; }
    return __builtin_bit_cast(bf16x8, u);
}

#define FRESH(ws, lds0, w, lane, b) int b = blockIdx.x; asm volatile("" : "+s"(ws), "+s"(lds0), "+s"(w), "+s"(b)); asm volatile("" : "+v"(lane))

enum { OP_A, OP_F, OP_X, OP_W, OP_N, OP_E };
struct Sched { int n; int op[128]; int arg[128]; int pend[128]; int xw; int ne; };
template <int PH, int NX> constexpr Sched make_sched() {
    using G_ = Geo<PH>; using N_ = Geo<NX>;
    Sched sc{}; int cnt = 0, lastop[64] = {}, xpos = 0; bool ndone[8] = {};
    int next = 0;
    auto push = [&](int op, int arg) { sc.op[sc.n] = op; sc.arg[sc.n] = arg; sc.pend[sc.n] = 0; ++sc.n; };
    auto item = [&](int s) -> int {
        if (next < G_::NS) { push(OP_A, next); cnt += G_::GA; lastop[next] = cnt; ++next; return G_::GA; }
        if (next == G_::NS) { ++next; if (G_::AUX) { push(OP_X, 0); cnt += G_::AUX; xpos = cnt; return G_::AUX; } }
        const int k = next - 1;
        if (k < G_::NT && k <= s - 2 + G_::NS) { push(OP_F, k); cnt += G_::G; lastop[k] = cnt; ++next; return G_::G; }
        return 0;
    };
    auto nextb = [&](int s) {
        if (PH == PH_QKV) return;
        for (int j = 0; j < N_::NS; ++j) {
            if (ndone[j]) continue;
            bool ok = next > G_::NT;
            for (int k = s; k < G_::NT && ok; ++k) {
                const int lo = N_::b_off(j), hi = lo + N_::BBYTES;
                if (lo < G_::a_off(k) + G_::ABYTES && G_::a_off(k) < hi) ok = false;
                if (lo < G_::b_off(k) + G_::BBYTES && G_::b_off(k) < hi) ok = false;
            }
            if (ok) { push(OP_N, j); cnt += N_::GB; ndone[j] = true; }
        }
    };
    for (int k = 0; k < G_::PA; ++k) item(0);
    sc.xw = -1;
    for (int s = 0; s < G_::NT; ++s) {
        push(OP_W, s); sc.pend[sc.n - 1] = cnt - lastop[s] < 63 ? cnt - lastop[s] : 63;
        if (G_::AUX && sc.xw < 0 && xpos && lastop[s] > xpos) sc.xw = s;
        for (int q = 0; q < G_::G;) { const int r = item(s); if (!r) break; q += r; }
        nextb(s > 0 ? s - 1 : 0);
    }
    sc.ne = sc.n; push(OP_E, 0);
    nextb(G_::NT);
    return sc;
}
template <int PH> struct NextPh { static constexpr int v = PH == PH_WO ? PH_GLU : PH == PH_GLU ? PH_DN : PH_QKV; };
template <int PH> struct SchedOf { static constexpr Sched v = make_sched<PH, NextPh<PH>::v>(); };

template <int PH> DI void issue_b(const char* ws, int k, int cb, unsigned lds0, int wid, unsigned vo) {
    using G_ = Geo<PH>;
    glds_run<G_::GB>(ws + G_::WP + (size_t)cb * (G_::NT * G_::BBYTES) + k * G_::BBYTES + wid * G_::GB * 1024, vo, lds0 + G_::b_off(k) + wid * G_::GB * 1024);
}
template <int PH> DI void prefetch_b(char* ws, unsigned lds0, int wid, int lane) {
    FRESH(ws, lds0, wid, lane, bid);
    int rb, cb; Geo<PH>::tile(bid, rb, cb);
#pragma unroll
    for (int k = 0; k < Geo<PH>::NS; ++k) issue_b<PH>(ws, k, cb, lds0, wid, lane * 16);
}

#define ROTM 7
#define RAW_ROT(rb) (2 * ((rb) & ROTM))
struct RawQkv { const char* sB[3]; unsigned vB; };
DI RawQkv raw_qkv_b(const char* wqkv, int cb, int wid, int lane) {
    RawQkv r;
#pragma unroll
    for (int g = 0; g < 3; ++g) { const int b = wid * 3 + g; r.sB[g] = wqkv + (size_t)(b & 3) * (8 * 3072) + (b >> 2) * 1024 + cb * 128; }
    r.vB = (lane >> 3) * 3072 + (((lane & 7) ^ (((lane >> 4) & 1) << 2)) << 4);
    return r;
}
template <int PH> DI void gemm_load(char* ws, const char* x0, const char* wqkv, int layer, unsigned lds0, int wid, int lane, bool no_next) {
    using G_ = Geo<PH>;
    constexpr int NX = NextPh<PH>::v;
    constexpr int MT = G_::MT, NT = G_::NT, KS = G_::KS, GA = G_::GA;
    ST_DECL ST(0);
    FRESH(ws, lds0, wid, lane, bid);
    int rb, cb, nrb, ncb; G_::tile(bid, rb, cb); Geo<NX>::tile(bid, nrb, ncb);
    const unsigned lw = layer * WS_LAYER;
    const unsigned vo = lane * 16;
    const char* sA = ws + lw + G_::A + (size_t)(rb * MT + (wid * GA) / KS) * (64 * G_::KTOT) + ((wid * GA) % KS) * 1024;
    unsigned vA = vo; int stepA = KS * 1024, stepB = G_::BBYTES, rot = 0;
    RawQkv rq;
    if constexpr (PH == PH_QKV) {
        if (layer == 0) {
            rq = raw_qkv_b(wqkv, cb, wid, lane); stepB = 32 * 3072; rot = RAW_ROT(rb);
            sA = x0 + (size_t)(rb * MT + (wid >> 1)) * 32768 + (wid & 1) * 32; vA = (lane & 31) * 1024 + (lane >> 5) * 16; stepA = 64;
        } else {
#pragma unroll
            for (int g = 0; g < 3; ++g) rq.sB[g] = ws + G_::WP + (size_t)cb * (NT * G_::BBYTES) + (wid * 3 + g) * 1024;
            rq.vB = vo;
        }
    }
    ST(1);
    if (PH != PH_QKV || layer) asm volatile("s_barrier" ::: "memory");
    ST(2);
    f32x4 stp[4];
    static_for<SchedOf<PH>::v.n>([&](auto ic) {
        constexpr int i = decltype(ic)::value, op = SchedOf<PH>::v.op[i], k = SchedOf<PH>::v.arg[i];
        if constexpr (op == OP_A || op == OP_F) glds_run<GA>(sA + (PH == PH_QKV ? (k + rot) & 15 : k) * stepA, vA, lds0 + G_::a_off(k) + wid * GA * 1024);
        if constexpr (op == OP_F && PH != PH_QKV) issue_b<PH>(ws, k, cb, lds0, wid, vo);
        if constexpr (op == OP_F && PH == PH_QKV) {
#pragma unroll
            for (int g = 0; g < 3; ++g) glds_run<1>(rq.sB[g] + ((k + rot) & 15) * stepB, rq.vB, lds0 + G_::b_off(k) + (wid * 3 + g) * 1024);
        }
        if constexpr (op == OP_N) {
            if (i < SchedOf<PH>::v.ne || !no_next) issue_b<NX>(ws, k, ncb, lds0, wid, vo);
        }
        if constexpr (op == OP_X) {
            const char* sp = ws + lw + WS_STB + (size_t)(rb * MT + wid % MT) * 4096;
            const unsigned so = (lane >> 5) * 2048 + (lane & 31) * 16;
            asm volatile("s_nop 4\n\tglobal_load_dwordx4 %0, %4, %5 sc1\n\tglobal_load_dwordx4 %1, %4, %5 offset:512 sc1\n\t"
                         "global_load_dwordx4 %2, %4, %5 offset:1024 sc1\n\tglobal_load_dwordx4 %3, %4, %5 offset:1536 sc1"
                         : "=&v"(stp[0]), "=&v"(stp[1]), "=&v"(stp[2]), "=&v"(stp[3]) : "v"(so), "s"(sp) : "memory");
        }
        if constexpr (op == OP_W) {
            if constexpr (G_::AUX && k == SchedOf<PH>::v.xw + 1) {
                asm volatile("" : "+v"(stp[0]), "+v"(stp[1]), "+v"(stp[2]), "+v"(stp[3]));
                const f32x4 t = (stp[0] + stp[1]) + (stp[2] + stp[3]);
                const float mean = half_sum(t[0] + t[2]) * (1.0f / DM), var = fmaxf(half_sum(t[1] + t[3]) * (1.0f / DM) - mean * mean, 0.f);
                if (lane < 32) ((LAS f32x2*)(uintptr_t)(lds0 + G_::ROWF))[(wid % MT) * 32 + lane] = f32x2{mean, __builtin_amdgcn_rsqf(var + LN_EPS)};
            }
            wait_vm_barrier<SchedOf<PH>::v.pend[i]>();
            if constexpr (k == 0) ST(3);
            if constexpr (k == NT - 1) ST(4);
        }
        if constexpr (op == OP_E) {
                        if (no_next) asm volatile("s_waitcnt vmcnt(0)" ::: "memory");
            lds_barrier(); ST(5);
        }
    });
    ST_OUT(layer * 5 + PH, 0);
}

DI void load_unit(u32x4 (&v)[4], const char* src, int ld2, int lane) {
    const GAS char* a = (const GAS char*)src + (size_t)(lane >> 3) * ld2 + (lane & 7) * 16;
#pragma unroll
    for (int i = 0; i < 4; ++i) v[i] = *(const GAS u32x4*)(a + (size_t)i * 8 * ld2);
}
DI void store_unit_packed(const u32x4 (&v)[4], rsrc_t rws, unsigned dst, int lane) {
#pragma unroll
    for (int i = 0; i < 4; ++i) st16(rws, ((lane ^ (((lane >> 4) & 1) << 2)) << 4) + i * 1024, dst, v[i]);
}
DI void unit_colsum(const u32x4 (&v)[4], unsigned lds_dst, int lane) {
    float c[8];
#pragma unroll
    for (int e = 0; e < 4; ++e) {
        c[2 * e] = (bf_lo(v[0][e]) + bf_lo(v[1][e])) + (bf_lo(v[2][e]) + bf_lo(v[3][e]));
        c[2 * e + 1] = (bf_hi(v[0][e]) + bf_hi(v[1][e])) + (bf_hi(v[2][e]) + bf_hi(v[3][e]));
    }
#pragma unroll
    for (int e = 0; e < 4; ++e) { auto r = __builtin_amdgcn_permlane32_swap(__float_as_uint(c[e]), __float_as_uint(c[e + 4]), false, false); c[e] = __uint_as_float(r[0]) + __uint_as_float(r[1]); }
#pragma unroll
    for (int e = 0; e < 2; ++e) { auto r = __builtin_amdgcn_permlane16_swap(__float_as_uint(c[e]), __float_as_uint(c[e + 2]), false, false); c[e] = __uint_as_float(r[0]) + __uint_as_float(r[1]); }
    const bool b3 = lane & 8;
    const float t = (b3 ? c[1] : c[0]) + dpp<0x128>(b3 ? c[0] : c[1]);
    *(LAS float*)(uintptr_t)(lds_dst + ((lane & 7) * 8 + (lane >> 3)) * 4) = t;
}
DI void store_colsum(unsigned lds_src, rsrc_t rws, unsigned dst, int lane) {
    const LAS float* r = (const LAS float*)(uintptr_t)lds_src + lane;
    __builtin_amdgcn_raw_buffer_store_b32(__float_as_uint((r[0] + r[64]) + (r[128] + r[192])), rws, dst + lane * 4, 0, 16);
}
DI rsrc_t make_rsrc(const void* p, unsigned bytes) { return __builtin_amdgcn_make_buffer_rsrc((void*)p, 0, bytes, 0x00020000); }

struct MkArgs { const char* x; const char* Wqkv; const char* Wo; const char* Wg; const char* Wu; const char* Wd; char* ws; char* out; };

DI void startup_load(const MkArgs& p, int cw, int lane, u32x4 (&v)[4], u64& epoch) {
    const int bid = blockIdx.x < 224 ? blockIdx.x : 0;
    epoch = __hip_atomic_load((const GAS u64*)(p.ws + WS_FL + FL_EPOCH), __ATOMIC_RELAXED, __HIP_MEMORY_SCOPE_AGENT);
    const char* src = bid < 128 ? p.x + (size_t)(bid >> 1) * 32768 + ((bid & 1) * 4 + cw) * 128 : p.Wqkv + (size_t)((bid & 3) * 4 + cw) * 32 * 3072 + ((bid - 128) >> 2) * 128;
    load_unit(v, src, bid < 128 ? 1024 : 3072, lane);
}
DI bool startup_job(rsrc_t rws, unsigned lds0, int cw, int lane, const u32x4 (&v)[4]) {
    const int bid = blockIdx.x;
    if (bid >= 224) return false;
    if (bid < 128) {
#pragma unroll
        for (int i = 0; i < 4; ++i) {
            float sum = 0.f, sq = 0.f;
#pragma unroll
            for (int j = 0; j < 4; ++j) { const float lo = bf_lo(v[i][j]), hi = bf_hi(v[i][j]); sum += lo + hi; sq = fmaf(lo, lo, fmaf(hi, hi, sq)); }
            sum += dpp<0xB1>(sum); sq += dpp<0xB1>(sq);
            sum += dpp<0x4E>(sum); sq += dpp<0x4E>(sq);
            if ((lane & 3) == 0) *(LAS f32x2*)(uintptr_t)(lds0 + AT_Q + cw * 512 + (i * 8 + (lane >> 3)) * 16 + ((lane >> 2) & 1) * 8) = f32x2{sum, sq};
        }
    } else unit_colsum(v, lds0 + AT_Q + cw * 256, lane);
    if (!arrive(lds0 + LDS_CNT, lane)) return false;
    if (bid < 128) {
#pragma unroll
        for (int e = 0; e < 2; ++e) st16(rws, lane * 16 + e * 1024, WS_STA + bid * 2048, *(LAS u32x4*)(uintptr_t)(lds0 + AT_Q + e * 1024 + lane * 16));
    } else store_colsum(lds0 + AT_Q, rws, WS_CSQ + (bid - 128) * 256, lane);
    return true;
}
DI void startup_signal(char* ws, int bid, int lane, u64 tag) {
    asm volatile("s_waitcnt vmcnt(0)" ::: "memory");
    if (bid < 128) signal(ws, WS_FL + PH_QKV * FL_PH + (bid >> 2) * 8 * FR, 8, 1, bid & 3, tag, lane);
    else { const int pan = (bid - 128) >> 2; signal(ws, WS_FL + PH_QKV * FL_PH + (pan & 7) * FR, 32, 8, 4 + (pan >> 3) * 4 + (bid & 3), tag, lane); }
}

template <int PH> DI void gemm_comp(char* ws, rsrc_t rws, rsrc_t rio, int layer, unsigned lds0, int cw, int lane, u64 tag, bool& poller) {
    using G_ = Geo<PH>;
    constexpr int MT = G_::MT, WN = G_::WN, NS = G_::NS, NT = G_::NT, KS = G_::KS, KG = G_::KG, UPS = G_::UPS, NU = G_::NU, TM = G_::TM, TN = G_::TN;
    constexpr int PBYTES = G_::PBYTES;
    constexpr bool LN = G_::LN;
    constexpr int CORR_U = (PH == PH_QKV ? CORRU : 10) * UPS, SIG_U = SIGU, STAT_U = STATU;
    static_assert(!G_::AUX || (SchedOf<PH>::v.xw >= 0 && CORR_U >= (SchedOf<PH>::v.xw + 1) * UPS), "row table must be ready");
    ST_DECL ST(0);
    FRESH(ws, lds0, cw, lane, bid);
    int rb, cb; G_::tile(bid, rb, cb);
    const unsigned lw = layer * WS_LAYER;
    const int r32 = lane & 31, hh = lane >> 5, wm = cw / WN, wn = cw % WN;
    u32x4 resid[2], stp[4];
    float cpart[TN][2];
    auto load_cs = [&]() {
        constexpr int NPAN = PH == PH_QKV ? 8 : 28;
#pragma unroll
        for (int t = 0; t < TN; ++t)
#pragma unroll
            for (int e = 0; e < 2; ++e)
                cpart[t][e] = __uint_as_float(__builtin_amdgcn_raw_buffer_load_b32(rws, slot_col(r32) * 4 + hh * 512 + e * 256,
                                                                                   (PH == PH_QKV ? WS_CSQ : WS_CSG) + (t * NPAN + cb * (WN / 2) + (wn >> 1)) * 1024 + (wn & 1) * 128, 16));
    };
    bf16x8 wf[TN];
    auto make_wf = [&]() {
#pragma unroll
        for (int t = 0; t < TN; ++t) wf[t] = corr_frag(half_sum(cpart[t][0] + cpart[t][1]), false, hh);
    };
    if constexpr (!LN) {
        static_assert(Cfg<PH_WO>::XC == Cfg<PH_DN>::XC, "the residual tile stays with its workgroup");
        const bool raw = PH == PH_WO && layer == 0;
        const unsigned vo = (rb * MT + wm) * 32768 + (raw ? r32 * 1024 + hh * 16 + (cb * 8 + wn * 4) * 16 : lane * 16 + lw + (PH == PH_WO ? WS_XA : WS_XB) + (cb * 8 + wn * 4) * 512);
#pragma unroll
        for (int e = 0; e < 2; ++e) resid[e] = ld16(raw ? rio : rws, vo + e * (raw ? 32 : 1024), 0);
    }
    auto load_st = [&]() {
#pragma unroll
        for (int k = 0; k < 4; ++k) stp[k] = ld16(rws, hh * 2048 + r32 * 16 + k * 512, lw + WS_STA + (rb * MT + wm) * 4096);
    };
    if (PH == PH_GLU || (PH == PH_QKV && layer)) load_cs();
    if (poller && (PH != PH_QKV || layer)) wait_tags(ws, lw + WS_FL + PH * FL_PH + (rb * G_::NCB + cb) * FR, PH == PH_QKV ? 8 : PH == PH_WO ? 32 : PH == PH_GLU ? 16 : 14, tag, lane);
    if (PH != PH_QKV || layer) lds_barrier();
    if (PH == PH_QKV && layer) load_st();
    LAS f32x2* rowf = (LAS f32x2*)(uintptr_t)(lds0 + G_::ROWF);
    unsigned aoff = lds0 + wm * TM * KS * 1024 + lane * 16;
    unsigned boff = lds0 + (wn >> 1) * PBYTES + tr_lane_off(lane, wn & 1);
    unsigned aoff2 = aoff + 49152, boff2 = boff + 49152, aoff3 = aoff + 98304, boff3 = boff + 98304;
    asm volatile("" : "+v"(aoff2), "+v"(boff2), "+v"(aoff3), "+v"(boff3));
    constexpr int TSTR = (WN / 2) * PBYTES;

    f32x16 acc[TM][TN];
#pragma unroll
    for (int i = 0; i < TM; ++i)
#pragma unroll
        for (int t = 0; t < TN; ++t) acc[i][t] = f32x16{};
    if constexpr (!LN) {
#pragma unroll
        for (int r = 0; r < 16; r += 2) { acc[0][0][r] = bf_lo(resid[r >> 3][(r >> 1) & 3]); acc[0][0][r + 1] = bf_hi(resid[r >> 3][(r >> 1) & 3]); }
        asm volatile("" : "+v"(acc[0][0]));
    }
    if constexpr (PH == PH_GLU) { make_wf(); asm volatile("" : "+v"(wf[0]), "+v"(wf[1])); }
    float rstd[TM];
    constexpr int NF = KG * (TM + TN), NM = KG * TM * TN;
    bf16x8 fr[2][NF];
    auto rd = [&](auto uc, auto fc) {
        constexpr int u = decltype(uc)::value, f = decltype(fc)::value;
        constexpr int ks = (u % UPS) * KG + f / (TM + TN), e = f % (TM + TN);
        constexpr int so = e < TN ? G_::b_off(u / UPS) : G_::a_off(u / UPS), hi = so / 49152, lo = so % 49152;
        if constexpr (e < TN) fr[u & 1][f] = lds_tr_frag((hi == 0 ? boff : hi == 1 ? boff2 : boff3) + lo + e * TSTR + ks * 2048);
        else fr[u & 1][f] = lds_b128((hi == 0 ? aoff : hi == 1 ? aoff2 : aoff3) + lo + ((e - TN) * KS + ks) * 1024);
    };
    auto mma = [&](auto tc, f32x16& c, const bf16x8& af, const bf16x8& bf) {
        if constexpr (PH == PH_QKV && decltype(tc)::value == 2) c = MFMA32(af, bf, c);
        else c = MFMA32(bf, af, c);
    };
    ST(1);
    lds_barrier();
    ST(2); STC(6);
    static_for<NF>([&](auto fc) { rd(std::integral_constant<int, 0>{}, fc); });
    SBAR();
    static_for<NU>([&](auto uc) {
        constexpr int u = decltype(uc)::value;
        if constexpr (PH == PH_QKV && u == SIG_U) { if (layer == 0 && poller) { __builtin_amdgcn_s_setprio(3); startup_signal(ws, bid, lane, tag); __builtin_amdgcn_s_setprio(0); } SBAR(); }
        if constexpr (PH == PH_QKV && u == STAT_U) {
            if (layer == 0) {
                __builtin_amdgcn_s_setprio(3);
                wait_tags(ws, WS_FL + PH * FL_PH + (rb * G_::NCB + cb) * FR, 16, tag, lane);
                load_st(); load_cs();
                __builtin_amdgcn_s_setprio(0);
            }
            SBAR();
        }
        if constexpr (LN && u == CORR_U) {
            if constexpr (PH == PH_QKV) make_wf();
#pragma unroll
            for (int i = 0; i < TM; ++i) {
                f32x2 st;
                if constexpr (PH == PH_QKV) {
                    const f32x4 t = (__builtin_bit_cast(f32x4, stp[0]) + __builtin_bit_cast(f32x4, stp[1])) + (__builtin_bit_cast(f32x4, stp[2]) + __builtin_bit_cast(f32x4, stp[3]));
                    const float mean = half_sum(t[0] + t[2]) * (1.0f / DM), var = fmaxf(half_sum(t[1] + t[3]) * (1.0f / DM) - mean * mean, 0.f);
                    st = f32x2{mean, __builtin_amdgcn_rsqf(var + LN_EPS)};
                    if (hh == 0) rowf[cw * 32 + r32] = st;
                } else st = rowf[(wm * TM + i) * 32 + r32];
                rstd[i] = st.y;
                const bf16x8 xf = corr_frag(-st.x, true, hh);
                static_for<TN>([&](auto tc) { mma(tc, acc[i][decltype(tc)::value], xf, wf[decltype(tc)::value]); });
            }
            SBAR();
        }
        if constexpr (u + 1 < NU && (u + 1) % UPS == 0) { asm volatile("s_barrier" ::: "memory"); SBAR(); }
        static_for<NM>([&](auto mc) {
            constexpr int m = decltype(mc)::value, g = m / (TM * TN), i = (m % (TM * TN)) / TN, t = m % TN;
            mma(std::integral_constant<int, t>{}, acc[i][t], fr[u & 1][g * (TM + TN) + TN + i], fr[u & 1][g * (TM + TN) + t]);
            if constexpr (u + 1 < NU) {
                SBAR();
                constexpr int PER = PH == PH_GLU ? 2 : 4;
                static_for<PER>([&](auto kc) {
                    constexpr int f = m * PER + decltype(kc)::value;
                    if constexpr (f < NF) rd(std::integral_constant<int, u + 1>{}, std::integral_constant<int, f>{});
                });
                SBAR();
            }
        });
    });

    STC(7); ST(3);
    if constexpr (PH == PH_QKV) {
        const int tile = rb * MT + wm;
        const int head0 = cb * 4 + wn * 2;
        const float rq = rstd[0] * QSCALE, rk = rstd[0];
#pragma unroll
        for (int e = 0; e < 2; ++e) {
            u32x4 q, k;
#pragma unroll
            for (int j = 0; j < 4; ++j) {
                q[j] = pack_bf16(rq * acc[0][0][8 * e + 2 * j], rq * acc[0][0][8 * e + 2 * j + 1]);
                k[j] = pack_bf16(rk * acc[0][1][8 * e + 2 * j], rk * acc[0][1][8 * e + 2 * j + 1]);
            }
            st16(rws, lane * 16, lw + WS_KV + ((head0 + e) * 128 + tile) * 1024, k);
            st16(rws, lane * 16, lw + WS_Q + ((head0 + e) * 64 + tile) * 1024, q);
        }
#pragma unroll
        for (int e = 0; e < 2; ++e) {
            u32x4 o;
#pragma unroll
            for (int j = 0; j < 4; ++j) {
                const f32x4 st = *(LAS f32x4*)&rowf[cw * 32 + 16 * e + 8 * (j >> 1) + 4 * hh + 2 * (j & 1)];
                o[j] = pack_bf16(st[1] * acc[0][2][8 * e + 2 * j], st[3] * acc[0][2][8 * e + 2 * j + 1]);
            }
            st16(rws, (r32 >> 4) * 131072 + e * 512 + hh * 256 + (r32 & 15) * 16, lw + WS_KV + (head0 * 128 + 64 + tile) * 1024, o);
        }
        lds_barrier();
        ST(4);
        poller = publish(ws, lw + WS_FL + PH_ATT * FL_PH + cb * 32 * FR, 32, 1, rb, tag, lds0 + LDS_CNT, lane);
    } else if constexpr (PH == PH_GLU) {
#pragma unroll
        for (int i = 0; i < TM; ++i) {
            const float r2 = rstd[i] * rstd[i];
#pragma unroll
            for (int e = 0; e < 2; ++e) {
                u32x4 o;
#pragma unroll
                for (int j = 0; j < 4; ++j) {
                    const int r = 8 * e + 2 * j;
                    o[j] = pack_bf16(fmaxf(r2 * acc[i][0][r], 0.f) * acc[i][1][r], fmaxf(r2 * acc[i][0][r + 1], 0.f) * acc[i][1][r + 1]);
                }
                st16(rws, lane * 16 + e * 1024, lw + WS_H + (rb * MT + i) * (64 * FF) + (cb * 16 + wn * 4) * 512, o);
            }
        }
        lds_barrier();
        ST(4);
        poller = publish(ws, lw + WS_FL + PH_DN * FL_PH + rb * 16 * FR, 16, 1, cb, tag, lds0 + LDS_CNT, lane);
    } else {
        const bool last = PH == PH_DN && layer == 1;
        const unsigned rbo = (rb * MT + wm) * 32768;
        const unsigned vo = last ? r32 * 1024 + hh * 16 + rbo + (cb * 8 + wn * 4) * 16 : lane * 16 + (PH == PH_WO ? lw + WS_XB : WS_LAYER + WS_XA) + rbo + (cb * 8 + wn * 4) * 512;
        const rsrc_t ro = last ? rio : rws;
#pragma unroll
        for (int e = 0; e < 2; ++e) {
            u32x4 o;
#pragma unroll
            for (int j = 0; j < 4; ++j) o[j] = pack_bf16(acc[0][0][8 * e + 2 * j], acc[0][0][8 * e + 2 * j + 1]);
            st16(ro, vo, e * (last ? 32 : 1024), o);
        }
        if (!last) {
            float sum[2] = {0.f, 0.f}, sq[2] = {0.f, 0.f};
#pragma unroll
            for (int r = 0; r < 16; ++r) { sum[r & 1] += acc[0][0][r]; sq[r & 1] = fmaf(acc[0][0][r], acc[0][0][r], sq[r & 1]); }
            const float a = half_sum(sum[0] + sum[1]), b = half_sum(sq[0] + sq[1]);
            if (hh == 0) __builtin_amdgcn_raw_buffer_store_b64(u32x2{__float_as_uint(a), __float_as_uint(b)}, rws,
                                                               r32 * 16 + (PH == PH_WO ? lw + WS_STB : WS_LAYER + WS_STA) + (rb * MT + wm) * 4096 + cb * 512 + wn * 8, 0, 16);
        }
        lds_barrier();
        ST(4);
        if (!last) {
            if constexpr (PH == PH_WO) poller = publish(ws, lw + WS_FL + PH_GLU * FL_PH + (rb >> 1) * 14 * FR, 14, 1, (rb & 1) * 8 + cb, tag, lds0 + LDS_CNT, lane);
            else poller = publish(ws, WS_LAYER + WS_FL + PH_QKV * FL_PH + rb * 8 * FR, 8, 1, cb, tag, lds0 + LDS_CNT, lane);
        }
    }
    ST(5); ST_OUT(layer * 5 + PH, 1);
}

#define AT_E(C, i) "v_exp_f32 v[" #C "+" #i "], v[" #C "+" #i "]\n\t"
#define AT_C(P, C, i) "v_cvt_pk_bf16_f32 v[" #P "], v[" #C "+" #i "], v[" #C "+" #i "+1]\n\t"
#define AT_HALF(C, O, V0, PRE, MID)                                                                               \
    AT_E(C, 0) AT_E(C, 1) PRE AT_E(C, 2) AT_E(C, 3) AT_E(C, 4) AT_E(C, 5) MID                                     \
    AT_E(C, 6) AT_E(C, 7) AT_C(108, C, 0) AT_C(109, C, 2) AT_C(110, C, 4) AT_E(C, 8) AT_C(111, C, 6) AT_E(C, 9) AT_E(C, 10) \
    "v_mfma_f32_32x32x16_bf16 " O ", v[" #V0 ":" #V0 "+3], v[108:111], " O "\n\t"                                 \
    AT_E(C, 11) AT_E(C, 12) AT_E(C, 13) AT_E(C, 14) AT_E(C, 15)                                                   \
    AT_C(112, C, 8) AT_C(113, C, 10) AT_C(114, C, 12) AT_C(115, C, 14)
#define AT_TILE(U, V0C, V0N, SYNC)                                                                                \
    AT_HALF(64, "%[o0]", V0C,                                                                                     \
            "v_mfma_f32_32x32x16_bf16 %[o1], v[104:107], v[112:115], %[o1]\n\t"                                   \
            "ds_read_b128 v[104:107], %[va] offset:" #U "*1024+512\n\t",                                          \
            "v_mfma_f32_32x32x16_bf16 v[80:95], v[96:99], %[q1], %[nm1]\n\t" SYNC                                 \
            "ds_read_b128 v[96:99], %[ka] offset:" #U "*1024+1024\n\t"                                            \
            "ds_read_b128 v[" #V0N ":" #V0N "+3], %[va] offset:" #U "*1024+1024\n\t")                             \
    AT_HALF(80, "%[o1]", V0C,                                                                                     \
            "s_waitcnt lgkmcnt(0)\n\t"                                                                            \
            "v_mfma_f32_32x32x16_bf16 %[o0], v[104:107], v[112:115], %[o0]\n\t",                                  \
            "v_mfma_f32_32x32x16_bf16 v[64:79], v[96:99], %[q0], %[nm0]\n\t")

DI f32x16 neg_tile_max(const bf16x8& k0, const bf16x8& q) {
    const f32x16 s0 = MFMA32(k0, q, f32x16{});
    float m = s0[0];
#pragma unroll
    for (int r = 1; r < 16; ++r) m = fmaxf(m, s0[r]);
    m = half_max(m);
    f32x16 n;
#pragma unroll
    for (int r = 0; r < 16; ++r) n[r] = -m;
    return n;
}
DI f32x16 attn_exact(const bf16x8& q, rsrc_t rws, unsigned kv, unsigned ones, int lane) {
    const bool vlane = (lane & 31) < 16;
    const bf16x8 pad = lds_b128(ones);
    float m = -INFINITY;
    f32x16 o = f32x16{};
#pragma unroll 1
    for (int t = 0; t < 64; ++t) {
        const bf16x8 kf = __builtin_bit_cast(bf16x8, ld16(rws, lane * 16, kv + t * 1024));
        const unsigned vo = (lane >> 5) * 256 + (lane & 15) * 16;
        bf16x8 v0 = __builtin_bit_cast(bf16x8, ld16(rws, vo, kv + 65536 + t * 1024)), v1 = __builtin_bit_cast(bf16x8, ld16(rws, vo + 512, kv + 65536 + t * 1024));
        if (!vlane) { v0 = pad; v1 = pad; }
        const f32x16 s = MFMA32(kf, q, f32x16{});
        float tm = s[0];
#pragma unroll
        for (int r = 1; r < 16; ++r) tm = fmaxf(tm, s[r]);
        const float mn = fmaxf(m, half_max(tm));
        const float alpha = __builtin_amdgcn_exp2f(m - mn);
        m = mn;
#pragma unroll
        for (int r = 0; r < 16; ++r) o[r] *= alpha;
        u32x4 p0, p1;
#pragma unroll
        for (int j = 0; j < 4; ++j) {
            p0[j] = pack_bf16(__builtin_amdgcn_exp2f(s[2 * j] - m), __builtin_amdgcn_exp2f(s[2 * j + 1] - m));
            p1[j] = pack_bf16(__builtin_amdgcn_exp2f(s[8 + 2 * j] - m), __builtin_amdgcn_exp2f(s[8 + 2 * j + 1] - m));
        }
        o = MFMA32(v0, __builtin_bit_cast(bf16x8, p0), o);
        o = MFMA32(v1, __builtin_bit_cast(bf16x8, p1), o);
    }
    return o;
}

DI void attn_load(const MkArgs& p, rsrc_t rws, int layer, unsigned lds0, int wid, int lane, u64 tag) {
    ST_DECL ST(0);
    char* ws = p.ws;
    FRESH(ws, lds0, wid, lane, bid);
    const int x = bid & 7, idx = bid >> 3, head = x * 4 + (idx >> 3), qb = idx & 7;
    const unsigned lw = layer * WS_LAYER;
    const unsigned vo = lane * 16;
    const char* src = ws + lw + WS_KV + (size_t)head * 131072 + wid * 2048;
    auto chunk = [&](int c) {
        const char* s = src + ((qb + c) & 7) * 8192;
        glds_run<2>(s, vo, lds0 + AT_K + c * 8192 + wid * 2048);
        glds_run<2>(s + 65536, vo, lds0 + AT_V + c * 8192 + wid * 2048);
    };
    ST(1);
    asm volatile("s_barrier" ::: "memory");
    ST(2);
    glds_run<2>(ws + lw + WS_Q + (size_t)(head * 64 + qb * 8 + wid * 2) * 1024, vo, lds0 + AT_Q + wid * 2048);
    chunk(0);
    wait_vm_barrier<0>();
    ST(3);
    wait_tags(ws, lw + WS_FL + PH_ATT * FL_PH + (head * 8 + qb) * FR, 32, tag, lane);
    chunk(1);
    chunk(2); chunk(3); chunk(4); chunk(5); chunk(6); chunk(7);
    wait_vm_barrier<0>();
    ST(4);
    if (layer == 0) {
#pragma unroll 1
        for (int u = bid * 4 + wid; u < 1856; u += 1024) {
            const char* s; size_t dst; int ld2;
            if (u < 896) { const int mat = u >= 448, v = u - mat * 448, pan = v >> 4, kb = v & 15;
                s = (mat ? p.Wu : p.Wg) + (size_t)kb * 32 * (FF * 2) + pan * 128; ld2 = FF * 2; dst = Geo<PH_GLU>::packed_off(pan >> 1, mat * 2 + (pan & 1), kb); }
            else if (u < 1024) { const int v = u - 896, pan = v & 7, kb = v >> 3;
                s = p.Wo + (size_t)kb * 32 * 1024 + pan * 128; ld2 = 1024; dst = Geo<PH_WO>::packed_off(pan, 0, kb); }
            else if (u < 1472) { const int v = u - 1024, pan = v & 7, kb = v >> 3;
                s = p.Wd + (size_t)kb * 32 * 1024 + pan * 128; ld2 = 1024; dst = Geo<PH_DN>::packed_off(pan, 0, kb); }
            else { const int v = u - 1472, pan = v >> 4, kb = v & 15;
                s = p.Wqkv + (size_t)kb * 32 * 3072 + pan * 128; ld2 = 3072; dst = Geo<PH_QKV>::packed_off(pan & 7, pan >> 3, kb); }
            u32x4 v[4];
            load_unit(v, s, ld2, lane);
            store_unit_packed(v, rws, (unsigned)dst, lane);
            if (u < 896) unit_colsum(v, lds0 + LDS_ROWG + wid * 256, lane);
        }
        if (arrive(lds0 + LDS_CNT + 4, lane)) {
            if (bid < 224) { store_colsum(lds0 + LDS_ROWG, rws, WS_CSG + bid * 256, lane); asm volatile("s_waitcnt vmcnt(0)" ::: "memory"); }
            if (lane == 0) __hip_atomic_store((GAS u64*)(ws + WS_FL + FL_PK + bid * 8), tag, __ATOMIC_RELAXED, __HIP_MEMORY_SCOPE_AGENT);
            __builtin_amdgcn_s_sleep(24);
#pragma unroll 1
            for (int i = 0; i < 4; ++i) wait_tags(ws, WS_FL + FL_PK + ((bid + i) & 3) * 512, 64, tag, lane);
        }
    }
    lds_barrier();
    prefetch_b<PH_WO>(ws, lds0, wid, lane);
    lds_barrier();
    ST(5); ST_OUT(layer * 5 + PH_ATT, 0);
}

DI void attn_comp(char* ws, rsrc_t rws, int layer, unsigned lds0, int cw, int lane, u64 tag, bool& poller) {
    ST_DECL ST(0);
    FRESH(ws, lds0, cw, lane, bid);
    const int x = bid & 7, idx = bid >> 3, head = x * 4 + (idx >> 3), qb = idx & 7;
    const unsigned lw = layer * WS_LAYER;
    if (poller) wait_tags(ws, lw + WS_FL + PH_ATT * FL_PH + (head * 8 + qb) * FR + qb * 32, 4, tag, lane);
    lds_barrier();
    const int r32 = lane & 31, hh = lane >> 5;
    const unsigned ka0 = lds0 + AT_K + lane * 16;
    const bool vlane = r32 < 16;
    const unsigned va0 = lds0 + (vlane ? AT_V : AT_ONES) + hh * 256 + (r32 & 15) * 16;
    const unsigned vstep = vlane ? 8192u : 0u;

    ST(1);
    lds_barrier();
    ST(2);
    const bf16x8 q0 = lds_b128(lds0 + AT_Q + cw * 2048 + lane * 16), q1 = lds_b128(lds0 + AT_Q + cw * 2048 + 1024 + lane * 16);
    const bf16x8 k0 = lds_b128(ka0);
    const f32x16 nm0 = neg_tile_max(k0, q0), nm1 = neg_tile_max(k0, q1);
    f32x16 o0 = f32x16{}, o1 = f32x16{};
    STC(6);
    {
        unsigned ka = ka0, va = va0; int cnt, tmp;
        asm volatile(
            "ds_read_b128 v[96:99], %[ka]\n\t"
            "ds_read_b128 v[100:103], %[va]\n\t"
            "v_mov_b32 v104, 0\n\tv_mov_b32 v105, 0\n\tv_mov_b32 v106, 0\n\tv_mov_b32 v107, 0\n\t"
            "v_mov_b32 v112, 0\n\tv_mov_b32 v113, 0\n\tv_mov_b32 v114, 0\n\tv_mov_b32 v115, 0\n\t"
            "s_mov_b32 %[cnt], 0\n\t"
            "s_waitcnt lgkmcnt(0)\n\t"
            "v_mfma_f32_32x32x16_bf16 v[64:79], v[96:99], %[q0], %[nm0]\n\t"
            "s_nop 12\n\t"
            "1:\n\t"
            AT_TILE(0, 100, 116, "") AT_TILE(1, 116, 100, "") AT_TILE(2, 100, 116, "") AT_TILE(3, 116, 100, "")
            AT_TILE(4, 100, 116, "") AT_TILE(5, 116, 100, "") AT_TILE(6, 100, 116, "")
            AT_TILE(7, 116, 100, "s_and_b32 %[tmp], %[cnt], 3\n\ts_cbranch_scc1 2f\n\ts_barrier\n\t2:\n\t")
            "v_add_u32 %[ka], 0x2000, %[ka]\n\t"
            "v_add_u32 %[va], %[vstep], %[va]\n\t"
            "s_add_u32 %[cnt], %[cnt], 1\n\t"
            "s_cmp_lt_u32 %[cnt], 8\n\t"
            "s_cbranch_scc1 1b\n\t"
            "v_mfma_f32_32x32x16_bf16 %[o1], v[104:107], v[112:115], %[o1]\n\t"
            "s_nop 15\n\t"
            : [o0] "+v"(o0), [o1] "+v"(o1), [ka] "+v"(ka), [va] "+v"(va), [cnt] "=&s"(cnt), [tmp] "=&s"(tmp)
            : [q0] "v"(q0), [q1] "v"(q1), [nm0] "v"(nm0), [nm1] "v"(nm1), [vstep] "v"(vstep)
            : "memory", "scc", "v64", "v65", "v66", "v67", "v68", "v69", "v70", "v71", "v72", "v73", "v74", "v75", "v76", "v77", "v78", "v79",
              "v80", "v81", "v82", "v83", "v84", "v85", "v86", "v87", "v88", "v89", "v90", "v91", "v92", "v93", "v94", "v95",
              "v96", "v97", "v98", "v99", "v100", "v101", "v102", "v103", "v104", "v105", "v106", "v107",
              "v108", "v109", "v110", "v111", "v112", "v113", "v114", "v115", "v116", "v117", "v118", "v119");
    }
    STC(7); ST(3);
    if (__builtin_expect(__any(!(o0[8] < 1.0e18f) || !(o1[8] < 1.0e18f)), 0)) {
        o0 = attn_exact(q0, rws, lw + WS_KV + head * 131072, lds0 + AT_ONES + (r32 & 15) * 16, lane);
        o1 = attn_exact(q1, rws, lw + WS_KV + head * 131072, lds0 + AT_ONES + (r32 & 15) * 16, lane);
    }
    const float i0 = 1.0f / o0[8], i1 = 1.0f / o1[8];
    u32x4 w0, w1;
#pragma unroll
    for (int j = 0; j < 4; ++j) { w0[j] = pack_bf16(o0[2 * j] * i0, o0[2 * j + 1] * i0); w1[j] = pack_bf16(o1[2 * j] * i1, o1[2 * j + 1] * i1); }
    const int qtile = qb * 8 + cw * 2;
    st16(rws, lane * 16, lw + WS_O + qtile * 32768 + head * 1024, w0);
    st16(rws, lane * 16, lw + WS_O + (qtile + 1) * 32768 + head * 1024, w1);
    lds_barrier();
    ST(4);
    poller = publish(ws, lw + WS_FL + PH_WO * FL_PH + qb * 32 * FR, 32, 1, head, tag, lds0 + LDS_CNT, lane);
    ST(5); ST_OUT(layer * 5 + PH_ATT, 1);
}

__global__ __launch_bounds__(512) void mk_kernel(MkArgs p) {
    extern __shared__ __attribute__((aligned(16))) char smem[];
    const unsigned lds0 = (unsigned)(uintptr_t)(LAS char*)smem;
    const int tid = threadIdx.x, lane = tid & 63, wid = __builtin_amdgcn_readfirstlane(tid >> 6);
    const rsrc_t rws = make_rsrc(p.ws, WS_END);
    const bool glu = (blockIdx.x >> 3) < 28;
    if (wid < 4) {
        {
            int rb, cb; Geo<PH_QKV>::tile(blockIdx.x, rb, cb);
            const RawQkv rq = raw_qkv_b(p.Wqkv, cb, wid, lane);
#pragma unroll
            for (int k = 0; k < Geo<PH_QKV>::NS; ++k) {
                if (k == 2) asm volatile("s_barrier" ::: "memory");
#pragma unroll
                for (int g = 0; g < 3; ++g) glds_run<1>(rq.sB[g] + ((k + RAW_ROT(rb)) & 15) * (32 * 3072), rq.vB, lds0 + Geo<PH_QKV>::b_off(k) + (wid * 3 + g) * 1024);
            }
        }
        u64 tag = 0;
#pragma unroll 1
        for (int layer = 0; layer < 2; ++layer) {
            gemm_load<PH_QKV>(p.ws, p.x, p.Wqkv, layer, lds0, wid, lane, false);
            if (layer == 0) { const u32x2 t = *(LAS u32x2*)(uintptr_t)(lds0 + LDS_CNT + 8); tag = ((u64)__builtin_amdgcn_readfirstlane(t[1]) << 32) | (unsigned)__builtin_amdgcn_readfirstlane(t[0]); }
            attn_load(p, rws, layer, lds0, wid, lane, tag);
            gemm_load<PH_WO>(p.ws, p.x, p.Wqkv, layer, lds0, wid, lane, !glu);
            if (glu) gemm_load<PH_GLU>(p.ws, p.x, p.Wqkv, layer, lds0, wid, lane, false);
            else prefetch_b<PH_DN>(p.ws, lds0, wid, lane);
            gemm_load<PH_DN>(p.ws, p.x, p.Wqkv, layer, lds0, wid, lane, layer == 1);
        }
    } else {
        const rsrc_t rout = make_rsrc(p.out, SEQ * DM * 2), rx = make_rsrc(p.x, SEQ * DM * 2);
        __builtin_amdgcn_s_setprio(3);
        u32x4 sv[4]; u64 epoch;
        startup_load(p, wid - 4, lane, sv, epoch);
        {
            const unsigned ov = ((tid & 15) == 0 || (tid & 15) == 4) ? 0x3f803f80u : 0u;
            *(LAS u32x4*)(uintptr_t)(lds0 + AT_ONES + (tid - 256) * 16) = u32x4{ov, ov, ov, ov};
            *(LAS u32x4*)(uintptr_t)(lds0 + AT_ONES + tid * 16) = u32x4{ov, ov, ov, ov};
            if (tid < 320) *(LAS u32x4*)(uintptr_t)(lds0 + AT_ONES + (tid + 256) * 16) = u32x4{ov, ov, ov, ov};
            if (tid == 256) *(LAS u32x2*)(uintptr_t)(lds0 + LDS_CNT) = u32x2{0, 0};
        }
        lds_barrier();
        bool poller = startup_job(rws, lds0, wid - 4, lane, sv);
        const u64 tag = (((u64)__builtin_amdgcn_readfirstlane((unsigned)(epoch >> 32)) << 32) | (unsigned)__builtin_amdgcn_readfirstlane((unsigned)epoch)) + 1;
        if (tid == 256) *(LAS u32x2*)(uintptr_t)(lds0 + LDS_CNT + 8) = u32x2{(unsigned)tag, (unsigned)(tag >> 32)};
        __builtin_amdgcn_s_setprio(0);
#pragma unroll 1
        for (int layer = 0; layer < 2; ++layer) {
            gemm_comp<PH_QKV>(p.ws, rws, rout, layer, lds0, wid - 4, lane, tag, poller);
            attn_comp(p.ws, rws, layer, lds0, wid - 4, lane, tag, poller);
            gemm_comp<PH_WO>(p.ws, rws, rx, layer, lds0, wid - 4, lane, tag, poller);
            if (glu) gemm_comp<PH_GLU>(p.ws, rws, rout, layer, lds0, wid - 4, lane, tag, poller);
            gemm_comp<PH_DN>(p.ws, rws, rout, layer, lds0, wid - 4, lane, tag, poller);
        }
        if (blockIdx.x == 0 && tid == 256) __hip_atomic_store((GAS u64*)(p.ws + WS_FL + FL_EPOCH), tag, __ATOMIC_RELAXED, __HIP_MEMORY_SCOPE_AGENT);
    }
}

extern "C" void kernel_launch(void* const* d_in, const int* in_sizes, int n_in, void* d_out, int out_size, void* d_ws, size_t ws_size,
                              hipStream_t stream) {
    (void)hipFuncSetAttribute((const void*)mk_kernel, hipFuncAttributeMaxDynamicSharedMemorySize, LDS_BYTES);
    mk_kernel<<<256, 512, LDS_BYTES, stream>>>(MkArgs{(const char*)d_in[0], (const char*)d_in[1], (const char*)d_in[2], (const char*)d_in[4], (const char*)d_in[5],
                                                      (const char*)d_in[6], (char*)d_ws, (char*)d_out});
}
```

```cpp
#include <hip/hip_runtime.h>
#include <hip/hip_bf16.h>
#include <stdint.h>
#include <utility>

typedef short bf16x8 __attribute__((ext_vector_type(8)));
typedef float f32x16 __attribute__((ext_vector_type(16)));
typedef float f32x4 __attribute__((ext_vector_type(4)));
typedef float f32x2 __attribute__((ext_vector_type(2)));
typedef unsigned u32x4 __attribute__((ext_vector_type(4)));
typedef unsigned u32x2 __attribute__((ext_vector_type(2)));
typedef __bf16 bf16x2_t __attribute__((ext_vector_type(2)));
typedef __amdgpu_buffer_rsrc_t rsrc_t;
#define LAS __attribute__((address_space(3)))
#define GAS __attribute__((address_space(1)))
#define DI __device__ __forceinline__
#define MFMA32(a, b, c) __builtin_amdgcn_mfma_f32_32x32x16_bf16((a), (b), (c), 0, 0, 0)
#define SBAR() __builtin_amdgcn_sched_barrier(0)

constexpr int SEQ = 2048, DM = 512, FF = 1792;
constexpr float LN_EPS = 1e-5f;
constexpr float QSCALE = 0.25f * 1.4426950408889634f;

enum { PH_QKV = 0, PH_ATT = 1, PH_WO = 2, PH_GLU = 3, PH_DN = 4 };
#define LMAX 1.0e18f
constexpr unsigned MB = 1u << 20;
constexpr unsigned WS_LAYER = 20 * MB;
constexpr unsigned WS_Q = 0, WS_KV = 2 * MB, WS_O = 6 * MB, WS_XA = 8 * MB, WS_XB = 10 * MB, WS_H = 12 * MB, WS_STA = 19 * MB, WS_STB = WS_STA + MB / 4, WS_FL = WS_STB + MB / 4;
constexpr unsigned TS = 8, FR = 32 * TS;
constexpr unsigned FL_PH = 256 * FR, FL_PK = 5 * FL_PH, FL_EPOCH = FL_PK + 2048;
constexpr unsigned WS_WP = 2 * WS_LAYER;
constexpr unsigned WP_QKV = WS_WP, WP_O = WP_QKV + DM * 3 * DM * 2, WP_GU = WP_O + DM * DM * 2, WP_D = WP_GU + DM * FF * 4;
constexpr unsigned WS_CSQ = WP_D + FF * DM * 2, WS_CSG = WS_CSQ + 3 * DM * 16, WS_END = WS_CSG + 2 * FF * 16;
typedef unsigned long long u64;

template <int... I, class Fn> DI void static_for_impl(std::integer_sequence<int, I...>, Fn&& f) { (f(std::integral_constant<int, I>{}), ...); }
template <int N, class Fn> DI void static_for(Fn&& f) { static_for_impl(std::make_integer_sequence<int, N>{}, f); }

DI unsigned pack_bf16(float lo, float hi) { bf16x2_t p; p.x = (__bf16)lo; p.y = (__bf16)hi; return __builtin_bit_cast(unsigned, p); }
DI float bf_lo(unsigned u) { return __uint_as_float(u << 16); }
DI float bf_hi(unsigned u) { return __uint_as_float(u & 0xffff0000u); }
template <int CTRL> DI float dpp(float v) { return __uint_as_float(__builtin_amdgcn_update_dpp(0u, __float_as_uint(v), CTRL, 0xf, 0xf, true)); }
DI float half_sum(float v) {
    auto r = __builtin_amdgcn_permlane32_swap(__float_as_uint(v), __float_as_uint(v), false, false);
    return __uint_as_float(r[0]) + __uint_as_float(r[1]);
}
DI float half_max(float v) {
    auto r = __builtin_amdgcn_permlane32_swap(__float_as_uint(v), __float_as_uint(v), false, false);
    return fmaxf(__uint_as_float(r[0]), __uint_as_float(r[1]));
}
DI void st16(rsrc_t r, unsigned voff, unsigned soff, u32x4 v) { __builtin_amdgcn_raw_buffer_store_b128(v, r, voff + soff, 0, 16); }
DI u32x4 ld16(rsrc_t r, unsigned voff, unsigned soff) { return __builtin_amdgcn_raw_buffer_load_b128(r, voff, soff, 16); }
template <int N> DI void wait_vm_barrier() { asm volatile("s_waitcnt vmcnt(%0) lgkmcnt(0)\n\ts_barrier" ::"i"(N) : "memory"); }
DI void lds_barrier() { asm volatile("s_waitcnt lgkmcnt(0)\n\ts_barrier" ::: "memory"); }
DI bf16x8 lds_b128(unsigned addr) { return *(const LAS bf16x8*)(uintptr_t)addr; }
DI bf16x8 lds_tr_frag(unsigned addr) {
    typedef short v4i16_t __attribute__((ext_vector_type(4)));
    v4i16_t lo = __builtin_amdgcn_ds_read_tr16_b64_v4i16((LAS v4i16_t*)(uintptr_t)addr);
    v4i16_t hi = __builtin_amdgcn_ds_read_tr16_b64_v4i16((LAS v4i16_t*)(uintptr_t)(addr + 512));
    return __builtin_shufflevector(lo, hi, 0, 1, 2, 3, 4, 5, 6, 7);
}
template <int N> DI void glds_run(const char* sbase, unsigned voff, unsigned lds_dst) {
    static_assert(N >= 1 && N <= 4, "");
#define GL(o) "\n\tglobal_load_lds_dwordx4 %0, %1 offset:" #o " sc1"
    if constexpr (N == 1) asm volatile("s_mov_b32 m0, %2\n\ts_nop 3" GL(0)::"v"(voff), "s"(sbase), "s"(lds_dst) : "memory");
    if constexpr (N == 2) asm volatile("s_mov_b32 m0, %2\n\ts_nop 3" GL(0) GL(1024)::"v"(voff), "s"(sbase), "s"(lds_dst) : "memory");
    if constexpr (N == 3) asm volatile("s_mov_b32 m0, %2\n\ts_nop 3" GL(0) GL(1024) GL(2048)::"v"(voff), "s"(sbase), "s"(lds_dst) : "memory");
    if constexpr (N == 4) asm volatile("s_mov_b32 m0, %2\n\ts_nop 3" GL(0) GL(1024) GL(2048) GL(3072)::"v"(voff), "s"(sbase), "s"(lds_dst) : "memory");
#undef GL
}
DI void glds_in(const char* sbase, unsigned voff, unsigned lds_dst) {
    asm volatile("s_mov_b32 m0, %2\n\ts_nop 3\n\tglobal_load_lds_dwordx4 %0, %1" ::"v"(voff), "s"(sbase), "s"(lds_dst) : "memory");
}
DI int arrive(unsigned cnt_addr, int lane) {
    asm volatile("s_waitcnt vmcnt(0) lgkmcnt(0)" ::: "memory");
    unsigned old = 0;
    if (lane == 0) old = __hip_atomic_fetch_add((LAS unsigned*)(uintptr_t)cnt_addr, 1u, __ATOMIC_RELAXED, __HIP_MEMORY_SCOPE_WORKGROUP);
    asm volatile("" ::: "memory");
    return __builtin_amdgcn_readfirstlane(old) & 3;
}
DI void signal(char* ws, unsigned first_line_off, int n, int lstride, int me, u64 tag, int lane) {
    if (lane < n) __hip_atomic_store((GAS u64*)(ws + first_line_off + lane * lstride * FR + me * TS), tag, __ATOMIC_RELAXED, __HIP_MEMORY_SCOPE_AGENT);
}
DI bool publish(char* ws, unsigned first_line_off, int n, int lstride, int me, u64 tag, unsigned cnt_addr, int lane) {
    const int rank = arrive(cnt_addr, lane);
    if (rank == 3) signal(ws, first_line_off, n, lstride, me, tag, lane);
    return rank == 3;
}
DI void wait_tags(const char* ws, unsigned off, int n, u64 tag, int lane) {
    const GAS u64* f = (const GAS u64*)(ws + off + (lane < n ? lane : 0) * TS);
#pragma unroll 1
    for (int spin = 0; spin < (1 << 16); ++spin) {
        const u64 v = __hip_atomic_load(f, __ATOMIC_RELAXED, __HIP_MEMORY_SCOPE_AGENT);
        if (__all(v == tag)) break;
        __builtin_amdgcn_s_sleep(1);
    }
    asm volatile("s_waitcnt vmcnt(0)" ::: "memory");
}
DI void dep_barrier(bool poller, const char* ws, unsigned off, int n, u64 tag, int lane) {
    if (__builtin_amdgcn_readfirstlane((int)poller)) wait_tags(ws, off, n, tag, lane);
    lds_barrier();
}

#define ST_DECL
#define ST(i)
#define STC(i)
#define ST_OUT(kind, role)

template <int PH> struct Cfg;
template <> struct Cfg<PH_QKV> { static constexpr int KTOT = 512, MT = 2, NP = 3, WM = 2, WN = 2, BK = 32, NS = 8, PA = 8, KG = 2, NCB = 8, XC = 2; };
template <> struct Cfg<PH_WO> { static constexpr int KTOT = 512, MT = 2, NP = 1, WM = 2, WN = 2, BK = 64, NS = 8, PA = 4, KG = 4, NCB = 8, XC = 2; };
template <> struct Cfg<PH_GLU> { static constexpr int KTOT = 512, MT = 4, NP = 4, WM = 1, WN = 4, BK = 32, NS = 5, PA = 3, KG = 1, NCB = 14, XC = 2; };
template <> struct Cfg<PH_DN> { static constexpr int KTOT = FF, MT = 2, NP = 1, WM = 2, WN = 2, BK = 64, NS = 8, PA = 4, KG = 4, NCB = 8, XC = 2; };

constexpr int AT_K = 0, AT_V = 65536, AT_ONES = 131072, AT_Q = AT_ONES + 9216, LDS_ROWQ = AT_Q + 8192, LDS_ROWG = LDS_ROWQ + 1024, LDS_CNT = LDS_ROWG + 1024, LDS_BYTES = LDS_CNT + 16;

template <int PH> struct Geo : Cfg<PH> {
    using C = Cfg<PH>;
    static constexpr int TM = C::MT / C::WM, TN = C::NP * 2 / C::WN, NT = C::KTOT / C::BK, KS = C::BK / 16, UPS = KS / C::KG, NU = NT * UPS;
    static constexpr int ABYTES = C::MT * C::BK * 64, PBYTES = C::BK * 128, BBYTES = C::NP * PBYTES, SBYTES = ABYTES + BBYTES;
    static constexpr int GA = ABYTES / 4096, GB = BBYTES / 4096, G = GA + GB;
    static constexpr bool LN = PH == PH_QKV || PH == PH_GLU;
    static constexpr int ROWF = PH == PH_QKV ? LDS_ROWQ : LDS_ROWG;
    static constexpr int NRB = SEQ / (32 * C::MT), AUX = PH == PH_GLU ? 4 : 0;
    static constexpr unsigned WP = PH == PH_QKV ? WP_QKV : PH == PH_WO ? WP_O : PH == PH_GLU ? WP_GU : WP_D;
    static constexpr unsigned A = PH == PH_QKV ? WS_XA : PH == PH_WO ? WS_O : PH == PH_GLU ? WS_XB : WS_H;
    static_assert(C::WM * C::WN == 4 && GA * 4096 == ABYTES && GB * 4096 == BBYTES && GA <= KS && KS % GA == 0 && GB <= 4, "pieces must divide over the waves");
    static_assert(C::NS * SBYTES <= 131072 && C::NS <= NT, "LDS budget");
    static constexpr int a_off(int k) {
        if (PH == PH_WO) return (k < 4 ? 32768 : 65536) + k * 8192;
        return ((k + (PH == PH_QKV ? 6 : PH == PH_DN ? 1 : 0)) % C::NS) * SBYTES;
    }
    static constexpr int b_off(int k) { return PH == PH_WO ? (k < 4 ? 0 : 32768) + k * 8192 : a_off(k) + ABYTES; }
    static DI void tile(int b, int& rb, int& cb) {
        const int x = b & 7, i = b >> 3;
        constexpr int CH = C::NCB / C::XC, RH = NRB * C::XC / 8;
        rb = (x / C::XC) * RH + i / CH; cb = (x % C::XC) * CH + i % CH;
    }
    static DI size_t packed_off(int cb, int pan, int kb) {
        return (size_t)WP + (size_t)cb * (NT * BBYTES) + (size_t)((kb * 32) / C::BK) * BBYTES + pan * PBYTES + ((kb * 32) % C::BK) * 128;
    }
};

DI unsigned tr_lane_off(int lane, int j) {
    const int hh = lane >> 5, blk = (lane >> 4) & 1, q = (lane & 15) >> 2, pp = lane & 3, pi = ((pp & 1) << 1) | (pp >> 1);
    const int ch = (4 * j + 2 * blk + (pi >> 1)) ^ (((q >> 1) & 1) << 2);
    return (8 * hh + q) * 128 + ch * 16 + 8 * (pi & 1);
}
DI int slot_col(int r32) { const int pp = (r32 >> 2) & 3; return (r32 & 16) + 4 * (((pp & 1) << 1) | (pp >> 1)) + (r32 & 3); }
DI bf16x8 corr_frag(float v, bool second_lo, int hh) {
    const unsigned h = pack_bf16(v, 0.f) & 0xffffu, l = pack_bf16(v - bf_lo(h), 0.f) & 0xffffu;
    u32x4 u; u[0] = second_lo ? (h | (l << 16)) : (h | (h << 16)); u[1] = second_lo ? h : l; u[2] = 0; u[3] = 0;
    if (hh) { u[0] = 0; u[1] = 0; }
    return __builtin_bit_cast(bf16x8, u);
}

#define FRESH(ws, lds0, w, lane, b) int b = blockIdx.x; asm volatile("" : "+s"(ws), "+s"(lds0), "+s"(w), "+s"(b)); asm volatile("" : "+v"(lane))

enum { OP_A, OP_F, OP_X, OP_W, OP_N, OP_E };
struct Sched { int n; int op[128]; int arg[128]; int pend[128]; int nn[128]; int xw; };
template <int PH, int NX> constexpr Sched make_sched() {
    using G_ = Geo<PH>; using N_ = Geo<NX>;
    Sched sc{}; int cnt = 0, ncnt = 0, lastop[64] = {}, xpos = 0; bool ndone[8] = {};
    int next = 0;
    auto push = [&](int op, int arg) { sc.op[sc.n] = op; sc.arg[sc.n] = arg; sc.pend[sc.n] = 0; ++sc.n; };
    auto item = [&](int s) -> int {
        if (next < G_::NS) { push(OP_A, next); cnt += G_::GA; lastop[next] = cnt; ++next; return G_::GA; }
        if (next == G_::NS) { ++next; if (G_::AUX) { push(OP_X, 0); cnt += G_::AUX; xpos = cnt; return G_::AUX; } }
        const int k = next - 1;
        if (k < G_::NT && k <= s - 2 + G_::NS) { push(OP_F, k); cnt += G_::G; lastop[k] = cnt; ++next; return G_::G; }
        return 0;
    };
    auto nextb = [&](int s) {
        if (PH == PH_QKV) return;
        for (int j = 0; j < N_::NS; ++j) {
            if (ndone[j]) continue;
            bool ok = next > G_::NT;
            for (int k = s; k < G_::NT && ok; ++k) {
                const int lo = N_::b_off(j), hi = lo + N_::BBYTES;
                if (lo < G_::a_off(k) + G_::ABYTES && G_::a_off(k) < hi) ok = false;
                if (lo < G_::b_off(k) + G_::BBYTES && G_::b_off(k) < hi) ok = false;
            }
            if (ok) { push(OP_N, j); cnt += N_::GB; ncnt += N_::GB; ndone[j] = true; }
        }
    };
    for (int k = 0; k < G_::PA; ++k) item(0);
    sc.xw = -1;
    for (int s = 0; s < G_::NT; ++s) {
        push(OP_W, s); sc.pend[sc.n - 1] = cnt - lastop[s]; sc.nn[sc.n - 1] = ncnt;
        if (G_::AUX && sc.xw < 0 && xpos && lastop[s] > xpos) sc.xw = s;
        for (int q = 0; q < G_::G;) { const int r = item(s); if (!r) break; q += r; }
        nextb(s > 0 ? s - 1 : 0);
    }
    push(OP_E, 0);
    nextb(G_::NT);
    return sc;
}
template <int PH> struct NextPh { static constexpr int v = PH == PH_WO ? PH_GLU : PH == PH_GLU ? PH_DN : PH_QKV; };
template <int PH> struct SchedOf { static constexpr Sched v = make_sched<PH, NextPh<PH>::v>(); };

template <int PH> DI void issue_b(const char* ws, int k, int cb, unsigned lds0, int wid, unsigned vo) {
    using G_ = Geo<PH>;
    glds_run<G_::GB>(ws + G_::WP + (size_t)cb * (G_::NT * G_::BBYTES) + k * G_::BBYTES + wid * G_::GB * 1024, vo, lds0 + G_::b_off(k) + wid * G_::GB * 1024);
}
template <int PH> DI void prefetch_b(char* ws, unsigned lds0, int wid, int lane) {
    FRESH(ws, lds0, wid, lane, bid);
    int rb, cb; Geo<PH>::tile(bid, rb, cb);
#pragma unroll
    for (int k = 0; k < Geo<PH>::NS; ++k) issue_b<PH>(ws, k, cb, lds0, wid, lane * 16);
}

DI void first_load(const char* x0, const char* wqkv, unsigned lds0, int wid, int lane) {
    using G_ = Geo<PH_QKV>;
    ST_DECL ST(0); ST(1); ST(2);
    int rb, cb; G_::tile(blockIdx.x, rb, cb);
    const int rot = 2 * ((rb + cb) & 3);
    const char* sA = x0 + (size_t)(rb * 64 + wid * 16) * 1024;
    const unsigned vA0 = (lane >> 3) * 1024 + (((lane & 7) ^ (lane >> 4)) << 4), vA1 = 8192 + (vA0 ^ 64);
    const unsigned vB = (lane >> 3) * 3072 + (((lane & 7) ^ (((lane >> 4) & 1) << 2)) << 4);
    const char* sB[3];
#pragma unroll
    for (int g = 0; g < 3; ++g) { const int b = wid * 3 + g; sB[g] = wqkv + (size_t)(b & 3) * (8 * 3072) + (b >> 2) * 1024 + cb * 128; }
    auto issue = [&](int j) {
        const int jj = (j + rot) & 7;
        const unsigned da = lds0 + G_::a_off(2 * j) + (wid >> 1) * G_::SBYTES + (wid & 1) * 2048;
        glds_in(sA + jj * 128, vA0, da); glds_in(sA + jj * 128, vA1, da + 1024);
#pragma unroll
        for (int h = 0; h < 2; ++h)
#pragma unroll
            for (int g = 0; g < 3; ++g) glds_in(sB[g] + (2 * jj + h) * (32 * 3072), vB, lds0 + G_::b_off(2 * j + h) + (wid * 3 + g) * 1024);
    };
    struct S { static constexpr int issued(int s) { int a = 2 + s, b = s < 2 ? 4 : (s + G_::NS - 2) / 2; a = a < b ? a : b; return a < 8 ? a : 8; } };
    issue(0);
    asm volatile("s_barrier" ::: "memory");
    issue(1);
    static_for<G_::NT>([&](auto sc) {
        constexpr int s = decltype(sc)::value;
        wait_vm_barrier<8 * (S::issued(s) - 1 - s / 2) + (s & 1 ? 0 : 3)>();
        if constexpr (s == 0) ST(3);
        if constexpr (s == G_::NT - 1) ST(4);
#pragma unroll
        for (int j = S::issued(s); j < S::issued(s + 1); ++j) issue(j);
    });
    lds_barrier();
    ST(5); ST_OUT(PH_QKV, 0);
}

template <int PH> DI void gemm_load(char* ws, int layer, unsigned lds0, int wid, int lane, bool no_next) {
    using G_ = Geo<PH>;
    constexpr int NX = NextPh<PH>::v;
    constexpr int MT = G_::MT, NT = G_::NT, KS = G_::KS, GA = G_::GA;
    ST_DECL ST(0);
    FRESH(ws, lds0, wid, lane, bid);
    int rb, cb, nrb, ncb; G_::tile(bid, rb, cb); Geo<NX>::tile(bid, nrb, ncb);
    const unsigned lw = layer * WS_LAYER;
    const unsigned vo = lane * 16;
    const char* sA = ws + lw + G_::A + (size_t)(rb * MT + (wid * GA) / KS) * (64 * G_::KTOT) + ((wid * GA) % KS) * 1024;
    ST(1);
    asm volatile("s_barrier" ::: "memory");
    ST(2);
    f32x4 stp[4];
    static_for<SchedOf<PH>::v.n>([&](auto ic) {
        constexpr int i = decltype(ic)::value, op = SchedOf<PH>::v.op[i], k = SchedOf<PH>::v.arg[i];
        if constexpr (op == OP_A || op == OP_F) glds_run<GA>(sA + k * (KS * 1024), vo, lds0 + G_::a_off(k) + wid * GA * 1024);
        if constexpr (op == OP_F) issue_b<PH>(ws, k, cb, lds0, wid, vo);
        if constexpr (op == OP_N) { if (!no_next) issue_b<NX>(ws, k, ncb, lds0, wid, vo); }
        if constexpr (op == OP_X) {
            const char* sp = ws + lw + WS_STB + (size_t)(rb * MT + wid % MT) * 4096;
            const unsigned so = (lane >> 5) * 2048 + (lane & 31) * 16;
            asm volatile("s_nop 4\n\tglobal_load_dwordx4 %0, %4, %5 sc1\n\tglobal_load_dwordx4 %1, %4, %5 offset:512 sc1\n\t"
                         "global_load_dwordx4 %2, %4, %5 offset:1024 sc1\n\tglobal_load_dwordx4 %3, %4, %5 offset:1536 sc1"
                         : "=&v"(stp[0]), "=&v"(stp[1]), "=&v"(stp[2]), "=&v"(stp[3]) : "v"(so), "s"(sp) : "memory");
        }
        if constexpr (op == OP_W) {
            if constexpr (G_::AUX && k == SchedOf<PH>::v.xw + 1) {
                asm volatile("" : "+v"(stp[0]), "+v"(stp[1]), "+v"(stp[2]), "+v"(stp[3]));
                const f32x4 t = (stp[0] + stp[1]) + (stp[2] + stp[3]);
                const float mean = half_sum(t[0] + t[2]) * (1.0f / DM), var = fmaxf(half_sum(t[1] + t[3]) * (1.0f / DM) - mean * mean, 0.f);
                if (lane < 32) ((LAS f32x2*)(uintptr_t)(lds0 + G_::ROWF))[(wid % MT) * 32 + lane] = f32x2{mean, __builtin_amdgcn_rsqf(var + LN_EPS)};
            }
            static_assert(SchedOf<PH>::v.pend[i] <= 63, "vmcnt");
            if (SchedOf<PH>::v.nn[i] && no_next) wait_vm_barrier<SchedOf<PH>::v.pend[i] - SchedOf<PH>::v.nn[i]>();
            else wait_vm_barrier<SchedOf<PH>::v.pend[i]>();
            if constexpr (k == 0) ST(3);
            if constexpr (k == NT - 1) ST(4);
        }
        if constexpr (op == OP_E) { lds_barrier(); ST(5); }
    });
    ST_OUT(layer * 5 + PH, 0);
}

DI void load_unit(u32x4 (&v)[4], const char* src, int ld2, int lane) {
    const GAS char* a = (const GAS char*)src + (size_t)(lane >> 3) * ld2 + (lane & 7) * 16;
#pragma unroll
    for (int i = 0; i < 4; ++i) v[i] = *(const GAS u32x4*)(a + (size_t)i * 8 * ld2);
}
DI void store_unit_packed(const u32x4 (&v)[4], rsrc_t rws, unsigned dst, int lane) {
#pragma unroll
    for (int i = 0; i < 4; ++i) st16(rws, ((lane ^ (((lane >> 4) & 1) << 2)) << 4) + i * 1024, dst, v[i]);
}
DI void unit_colsum(const u32x4 (&v)[4], unsigned lds_dst, int lane) {
    float c[8];
#pragma unroll
    for (int e = 0; e < 4; ++e) {
        c[2 * e] = (bf_lo(v[0][e]) + bf_lo(v[1][e])) + (bf_lo(v[2][e]) + bf_lo(v[3][e]));
        c[2 * e + 1] = (bf_hi(v[0][e]) + bf_hi(v[1][e])) + (bf_hi(v[2][e]) + bf_hi(v[3][e]));
    }
#pragma unroll
    for (int e = 0; e < 4; ++e) { auto r = __builtin_amdgcn_permlane32_swap(__float_as_uint(c[e]), __float_as_uint(c[e + 4]), false, false); c[e] = __uint_as_float(r[0]) + __uint_as_float(r[1]); }
#pragma unroll
    for (int e = 0; e < 2; ++e) { auto r = __builtin_amdgcn_permlane16_swap(__float_as_uint(c[e]), __float_as_uint(c[e + 2]), false, false); c[e] = __uint_as_float(r[0]) + __uint_as_float(r[1]); }
    const bool b3 = lane & 8;
    const float t = (b3 ? c[1] : c[0]) + dpp<0x128>(b3 ? c[0] : c[1]);
    *(LAS float*)(uintptr_t)(lds_dst + ((lane & 7) * 8 + (lane >> 3)) * 4) = t;
}
DI void store_colsum(unsigned lds_src, rsrc_t rws, unsigned dst, int lane) {
    const LAS float* r = (const LAS float*)(uintptr_t)lds_src + lane;
    __builtin_amdgcn_raw_buffer_store_b32(__float_as_uint((r[0] + r[64]) + (r[128] + r[192])), rws, dst + lane * 4, 0, 16);
}
DI rsrc_t make_rsrc(const void* p, unsigned bytes) { return __builtin_amdgcn_make_buffer_rsrc((void*)p, 0, bytes, 0x00020000); }

struct MkArgs { const char* x; const char* Wqkv; const char* Wo; const char* Wg; const char* Wu; const char* Wd; char* ws; char* out; };

DI void startup_load(const MkArgs& p, int cw, int lane, u32x4 (&v)[4]) {
    const int bid = blockIdx.x < 224 ? blockIdx.x : 0;
    const char* src = bid < 128 ? p.x + (size_t)(bid >> 1) * 32768 + ((bid & 1) * 4 + cw) * 128 : p.Wqkv + (size_t)((bid & 3) * 4 + cw) * 32 * 3072 + ((bid - 128) >> 2) * 128;
    load_unit(v, src, bid < 128 ? 1024 : 3072, lane);
}
DI bool startup_job(rsrc_t rws, unsigned lds0, int cw, int lane, const u32x4 (&v)[4]) {
    const int bid = blockIdx.x;
    if (bid >= 224) return false;
    if (bid < 128) {
#pragma unroll
        for (int i = 0; i < 4; ++i) {
            float sum = 0.f, sq = 0.f;
#pragma unroll
            for (int j = 0; j < 4; ++j) { const float lo = bf_lo(v[i][j]), hi = bf_hi(v[i][j]); sum += lo + hi; sq = fmaf(lo, lo, fmaf(hi, hi, sq)); }
            sum += dpp<0xB1>(sum); sq += dpp<0xB1>(sq);
            sum += dpp<0x4E>(sum); sq += dpp<0x4E>(sq);
            if ((lane & 3) == 0) *(LAS f32x2*)(uintptr_t)(lds0 + AT_Q + cw * 512 + (i * 8 + (lane >> 3)) * 16 + ((lane >> 2) & 1) * 8) = f32x2{sum, sq};
        }
    } else unit_colsum(v, lds0 + AT_Q + cw * 256, lane);
    if (arrive(lds0 + LDS_CNT, lane) != 3) return false;
    if (bid < 128) {
#pragma unroll
        for (int e = 0; e < 2; ++e) st16(rws, lane * 16 + e * 1024, WS_STA + bid * 2048, *(LAS u32x4*)(uintptr_t)(lds0 + AT_Q + e * 1024 + lane * 16));
    } else store_colsum(lds0 + AT_Q, rws, WS_CSQ + (bid - 128) * 256, lane);
    return true;
}
DI void startup_signal(char* ws, int bid, int lane, u64 tag) {
    asm volatile("s_waitcnt vmcnt(0)" ::: "memory");
    if (bid < 128) signal(ws, WS_FL + PH_QKV * FL_PH + (bid >> 2) * 8 * FR, 8, 1, bid & 3, tag, lane);
    else { const int pan = (bid - 128) >> 2; signal(ws, WS_FL + PH_QKV * FL_PH + (pan & 7) * FR, 32, 8, 4 + (pan >> 3) * 4 + (bid & 3), tag, lane); }
}

template <int PH> DI void gemm_comp(char* ws, rsrc_t rws, rsrc_t rio, int layer, unsigned lds0, int cw, int lane, u64 tag, bool& poller) {
    using G_ = Geo<PH>;
    constexpr int MT = G_::MT, WN = G_::WN, NS = G_::NS, NT = G_::NT, KS = G_::KS, KG = G_::KG, UPS = G_::UPS, NU = G_::NU, TM = G_::TM, TN = G_::TN;
    constexpr int PBYTES = G_::PBYTES;
    constexpr bool LN = G_::LN;
    constexpr int CORR_U = (PH == PH_QKV ? 15 : 10) * UPS, SIG_U = 3, POLL_U = 9, STAT_U = 12;
    static_assert(!G_::AUX || (SchedOf<PH>::v.xw >= 0 && CORR_U >= (SchedOf<PH>::v.xw + 1) * UPS), "row table must be ready");
    ST_DECL ST(0);
    FRESH(ws, lds0, cw, lane, bid);
    int rb, cb; G_::tile(bid, rb, cb);
    const unsigned lw = layer * WS_LAYER;
    const int r32 = lane & 31, hh = lane >> 5, wm = cw / WN, wn = cw % WN;
    u32x4 resid[2], stp[4];
    float cpart[TN][2];
    auto load_cs = [&]() {
        constexpr int NPAN = PH == PH_QKV ? 8 : 28;
#pragma unroll
        for (int t = 0; t < TN; ++t)
#pragma unroll
            for (int e = 0; e < 2; ++e)
                cpart[t][e] = __uint_as_float(__builtin_amdgcn_raw_buffer_load_b32(rws, slot_col(r32) * 4 + hh * 512 + e * 256,
                                                                                   (PH == PH_QKV ? WS_CSQ : WS_CSG) + (t * NPAN + cb * (WN / 2) + (wn >> 1)) * 1024 + (wn & 1) * 128, 16));
    };
    bf16x8 wf[TN];
    auto make_wf = [&]() {
#pragma unroll
        for (int t = 0; t < TN; ++t) wf[t] = corr_frag(half_sum(cpart[t][0] + cpart[t][1]), false, hh);
    };
    if constexpr (!LN) {
        static_assert(Cfg<PH_WO>::XC == Cfg<PH_DN>::XC, "the residual tile stays with its workgroup");
        const bool raw = PH == PH_WO && layer == 0;
        const unsigned vo = (rb * MT + wm) * 32768 + (raw ? r32 * 1024 + hh * 16 + (cb * 8 + wn * 4) * 16 : lane * 16 + lw + (PH == PH_WO ? WS_XA : WS_XB) + (cb * 8 + wn * 4) * 512);
#pragma unroll
        for (int e = 0; e < 2; ++e) resid[e] = ld16(raw ? rio : rws, vo + e * (raw ? 32 : 1024), 0);
    }
    auto load_st = [&]() {
#pragma unroll
        for (int k = 0; k < 4; ++k) stp[k] = ld16(rws, hh * 2048 + r32 * 16 + k * 512, lw + WS_STA + (rb * MT + wm) * 4096);
    };
    if (PH == PH_GLU || (PH == PH_QKV && layer)) load_cs();
    if (PH != PH_QKV || layer)
        dep_barrier(poller, ws, lw + WS_FL + PH * FL_PH + (rb * G_::NCB + cb) * FR, PH == PH_QKV ? 8 : PH == PH_WO ? 32 : PH == PH_GLU ? 16 : 14, tag, lane);
    if (PH == PH_QKV && layer) load_st();
    LAS f32x2* rowf = (LAS f32x2*)(uintptr_t)(lds0 + G_::ROWF);
    unsigned aoff = lds0 + wm * TM * KS * 1024 + lane * 16;
    unsigned boff = lds0 + (wn >> 1) * PBYTES + tr_lane_off(lane, wn & 1);
    unsigned aoff2 = aoff + 49152, boff2 = boff + 49152, aoff3 = aoff + 98304, boff3 = boff + 98304;
    asm volatile("" : "+v"(aoff2), "+v"(boff2), "+v"(aoff3), "+v"(boff3));
    unsigned aq[2][4];
    if constexpr (PH == PH_QKV) {
#pragma unroll
        for (int v = 0; v < 4; ++v) {
            aq[0][v] = layer ? aoff + (v >> 1) * G_::SBYTES + (v & 1) * 1024 : lds0 + wm * G_::SBYTES + r32 * 128 + (((2 * v + hh) ^ ((r32 >> 1) & 7)) << 4);
            aq[1][v] = aq[0][v] + 65536;
            asm volatile("" : "+v"(aq[0][v]), "+v"(aq[1][v]));
        }
    }
    constexpr int TSTR = (WN / 2) * PBYTES;

    f32x16 acc[TM][TN];
#pragma unroll
    for (int i = 0; i < TM; ++i)
#pragma unroll
        for (int t = 0; t < TN; ++t) acc[i][t] = f32x16{};
    if constexpr (!LN) {
#pragma unroll
        for (int r = 0; r < 16; r += 2) { acc[0][0][r] = bf_lo(resid[r >> 3][(r >> 1) & 3]); acc[0][0][r + 1] = bf_hi(resid[r >> 3][(r >> 1) & 3]); }
        asm volatile("" : "+v"(acc[0][0]));
    }
    if constexpr (PH == PH_GLU) { make_wf(); asm volatile("" : "+v"(wf[0]), "+v"(wf[1])); }
    float rstd[TM];
    u64 seen = 0;
    constexpr int NF = KG * (TM + TN), NM = KG * TM * TN;
    bf16x8 fr[2][NF];
    auto rd = [&](auto uc, auto fc) {
        constexpr int u = decltype(uc)::value, f = decltype(fc)::value;
        constexpr int ks = (u % UPS) * KG + f / (TM + TN), e = f % (TM + TN);
        constexpr int so = e < TN ? G_::b_off(u / UPS) : G_::a_off(u / UPS), hi = so / 49152, lo = so % 49152;
        if constexpr (e < TN) fr[u & 1][f] = lds_tr_frag((hi == 0 ? boff : hi == 1 ? boff2 : boff3) + lo + e * TSTR + ks * 2048);
        else if constexpr (PH == PH_QKV) fr[u & 1][f] = lds_b128(aq[G_::a_off(u & ~1) >> 16][(u & 1) * 2 + ks] + (G_::a_off(u & ~1) & 65535));
        else fr[u & 1][f] = lds_b128((hi == 0 ? aoff : hi == 1 ? aoff2 : aoff3) + lo + ((e - TN) * KS + ks) * 1024);
    };
    auto mma = [&](auto tc, f32x16& c, const bf16x8& af, const bf16x8& bf) {
        if constexpr (PH == PH_QKV && decltype(tc)::value == 2) c = MFMA32(af, bf, c);
        else c = MFMA32(bf, af, c);
    };
    ST(1);
    lds_barrier();
    ST(2); STC(6);
    static_for<NF>([&](auto fc) { rd(std::integral_constant<int, 0>{}, fc); });
    SBAR();
    static_for<NU>([&](auto uc) {
        constexpr int u = decltype(uc)::value;
        if constexpr (PH == PH_QKV && u == SIG_U) { if (layer == 0 && __builtin_amdgcn_readfirstlane((int)poller)) { __builtin_amdgcn_s_setprio(3); startup_signal(ws, bid, lane, tag); __builtin_amdgcn_s_setprio(0); } SBAR(); }
        if constexpr (PH == PH_QKV && u == POLL_U) {
            if (layer == 0) {
                __builtin_amdgcn_s_setprio(3);
                seen = __hip_atomic_load((const GAS u64*)(ws + WS_FL + PH * FL_PH + (rb * G_::NCB + cb) * FR + (lane & 15) * TS), __ATOMIC_RELAXED, __HIP_MEMORY_SCOPE_AGENT);
                __builtin_amdgcn_s_setprio(0);
            }
            SBAR();
        }
        if constexpr (PH == PH_QKV && u == STAT_U) {
            if (layer == 0) {
                __builtin_amdgcn_s_setprio(3);
                if (!__all(seen == tag)) wait_tags(ws, WS_FL + PH * FL_PH + (rb * G_::NCB + cb) * FR, 16, tag, lane);
                asm volatile("" ::: "memory");
                load_st(); load_cs();
                __builtin_amdgcn_s_setprio(0);
            }
            SBAR();
        }
        if constexpr (LN && u == CORR_U) {
            if constexpr (PH == PH_QKV) make_wf();
#pragma unroll
            for (int i = 0; i < TM; ++i) {
                f32x2 st;
                if constexpr (PH == PH_QKV) {
                    const f32x4 t = (__builtin_bit_cast(f32x4, stp[0]) + __builtin_bit_cast(f32x4, stp[1])) + (__builtin_bit_cast(f32x4, stp[2]) + __builtin_bit_cast(f32x4, stp[3]));
                    const float mean = half_sum(t[0] + t[2]) * (1.0f / DM), var = fmaxf(half_sum(t[1] + t[3]) * (1.0f / DM) - mean * mean, 0.f);
                    st = f32x2{mean, __builtin_amdgcn_rsqf(var + LN_EPS)};
                    if (hh == 0) rowf[cw * 32 + r32] = st;
                } else st = rowf[(wm * TM + i) * 32 + r32];
                rstd[i] = st.y;
                const bf16x8 xf = corr_frag(-st.x, true, hh);
                static_for<TN>([&](auto tc) { mma(tc, acc[i][decltype(tc)::value], xf, wf[decltype(tc)::value]); });
            }
            SBAR();
        }
        if constexpr (u + 1 < NU && (u + 1) % UPS == 0) { asm volatile("s_barrier" ::: "memory"); SBAR(); }
        static_for<NM>([&](auto mc) {
            constexpr int m = decltype(mc)::value, g = m / (TM * TN), i = (m % (TM * TN)) / TN, t = m % TN;
            mma(std::integral_constant<int, t>{}, acc[i][t], fr[u & 1][g * (TM + TN) + TN + i], fr[u & 1][g * (TM + TN) + t]);
            if constexpr (u + 1 < NU) {
                SBAR();
                constexpr int PER = PH == PH_GLU ? 2 : 4;
                static_for<PER>([&](auto kc) {
                    constexpr int f = m * PER + decltype(kc)::value;
                    if constexpr (f < NF) rd(std::integral_constant<int, u + 1>{}, std::integral_constant<int, f>{});
                });
                SBAR();
            }
        });
    });

    STC(7); ST(3);
    if constexpr (PH == PH_QKV) {
        const int tile = rb * MT + wm;
        const int head0 = cb * 4 + wn * 2;
        const float rq = rstd[0] * QSCALE, rk = rstd[0];
#pragma unroll
        for (int e = 0; e < 2; ++e) {
            u32x4 q, k;
#pragma unroll
            for (int j = 0; j < 4; ++j) {
                q[j] = pack_bf16(rq * acc[0][0][8 * e + 2 * j], rq * acc[0][0][8 * e + 2 * j + 1]);
                k[j] = pack_bf16(rk * acc[0][1][8 * e + 2 * j], rk * acc[0][1][8 * e + 2 * j + 1]);
            }
            st16(rws, lane * 16, lw + WS_KV + ((head0 + e) * 128 + tile) * 1024, k);
            st16(rws, lane * 16, lw + WS_Q + ((head0 + e) * 64 + tile) * 1024, q);
        }
#pragma unroll
        for (int e = 0; e < 2; ++e) {
            u32x4 o;
#pragma unroll
            for (int j = 0; j < 4; ++j) {
                const f32x4 st = *(LAS f32x4*)&rowf[cw * 32 + 16 * e + 8 * (j >> 1) + 4 * hh + 2 * (j & 1)];
                o[j] = pack_bf16(st[1] * acc[0][2][8 * e + 2 * j], st[3] * acc[0][2][8 * e + 2 * j + 1]);
            }
            st16(rws, (r32 >> 4) * 131072 + e * 512 + hh * 256 + (r32 & 15) * 16, lw + WS_KV + (head0 * 128 + 64 + tile) * 1024, o);
        }
        lds_barrier();
        ST(4);
        poller = publish(ws, lw + WS_FL + PH_ATT * FL_PH + cb * 32 * FR, 32, 1, rb, tag, lds0 + LDS_CNT, lane);
    } else if constexpr (PH == PH_GLU) {
#pragma unroll
        for (int i = 0; i < TM; ++i) {
            const float r2 = rstd[i] * rstd[i];
#pragma unroll
            for (int e = 0; e < 2; ++e) {
                u32x4 o;
#pragma unroll
                for (int j = 0; j < 4; ++j) {
                    const int r = 8 * e + 2 * j;
                    o[j] = pack_bf16(fmaxf(r2 * acc[i][0][r], 0.f) * acc[i][1][r], fmaxf(r2 * acc[i][0][r + 1], 0.f) * acc[i][1][r + 1]);
                }
                st16(rws, lane * 16 + e * 1024, lw + WS_H + (rb * MT + i) * (64 * FF) + (cb * 16 + wn * 4) * 512, o);
            }
        }
        lds_barrier();
        ST(4);
        poller = publish(ws, lw + WS_FL + PH_DN * FL_PH + rb * 16 * FR, 16, 1, cb, tag, lds0 + LDS_CNT, lane);
    } else {
        const bool last = PH == PH_DN && layer == 1;
        const unsigned rbo = (rb * MT + wm) * 32768;
        const unsigned vo = last ? r32 * 1024 + hh * 16 + rbo + (cb * 8 + wn * 4) * 16 : lane * 16 + (PH == PH_WO ? lw + WS_XB : WS_LAYER + WS_XA) + rbo + (cb * 8 + wn * 4) * 512;
        const rsrc_t ro = last ? rio : rws;
#pragma unroll
        for (int e = 0; e < 2; ++e) {
            u32x4 o;
#pragma unroll
            for (int j = 0; j < 4; ++j) o[j] = pack_bf16(acc[0][0][8 * e + 2 * j], acc[0][0][8 * e + 2 * j + 1]);
            st16(ro, vo, e * (last ? 32 : 1024), o);
        }
        if (!last) {
            float sum[2] = {0.f, 0.f}, sq[2] = {0.f, 0.f};
#pragma unroll
            for (int r = 0; r < 16; ++r) { sum[r & 1] += acc[0][0][r]; sq[r & 1] = fmaf(acc[0][0][r], acc[0][0][r], sq[r & 1]); }
            const float a = half_sum(sum[0] + sum[1]), b = half_sum(sq[0] + sq[1]);
            if (hh == 0) __builtin_amdgcn_raw_buffer_store_b64(u32x2{__float_as_uint(a), __float_as_uint(b)}, rws,
                                                               r32 * 16 + (PH == PH_WO ? lw + WS_STB : WS_LAYER + WS_STA) + (rb * MT + wm) * 4096 + cb * 512 + wn * 8, 0, 16);
        }
        lds_barrier();
        ST(4);
        if (!last) {
            if constexpr (PH == PH_WO) poller = publish(ws, lw + WS_FL + PH_GLU * FL_PH + (rb >> 1) * 14 * FR, 14, 1, (rb & 1) * 8 + cb, tag, lds0 + LDS_CNT, lane);
            else poller = publish(ws, WS_LAYER + WS_FL + PH_QKV * FL_PH + rb * 8 * FR, 8, 1, cb, tag, lds0 + LDS_CNT, lane);
        }
    }
    ST(5); ST_OUT(layer * 5 + PH, 1);
}

#define AT_E(C, i) "v_exp_f32 v[" #C "+" #i "], v[" #C "+" #i "]\n\t"
#define AT_C(P, C, i) "v_cvt_pk_bf16_f32 v[" #P "], v[" #C "+" #i "], v[" #C "+" #i "+1]\n\t"
#define AT_HALF(C, O, V0, PRE, MID, POST)                                                                         \
    AT_E(C, 0) AT_E(C, 1) PRE AT_E(C, 2) AT_E(C, 3) AT_E(C, 4) AT_E(C, 5) MID                                     \
    AT_E(C, 6) AT_E(C, 7) AT_C(108, C, 0) AT_C(109, C, 2) AT_C(110, C, 4) AT_E(C, 8) AT_C(111, C, 6) AT_E(C, 9) AT_E(C, 10) \
    "v_mfma_f32_32x32x16_bf16 " O ", v[" #V0 ":" #V0 "+3], v[108:111], " O "\n\t" POST                            \
    AT_E(C, 11) AT_E(C, 12) AT_E(C, 13) AT_E(C, 14) AT_E(C, 15)                                                   \
    AT_C(112, C, 8) AT_C(113, C, 10) AT_C(114, C, 12) AT_C(115, C, 14)
#define AT_TILE(U, V0C, V0N, SYNC)                                                                                \
    AT_HALF(64, "%[o0]", V0C,                                                                                     \
            "v_mfma_f32_32x32x16_bf16 %[o1], v[104:107], v[112:115], %[o1]\n\t"                                   \
            "ds_read_b128 v[104:107], %[va] offset:" #U "*1024+512\n\t",                                          \
            "v_mfma_f32_32x32x16_bf16 v[80:95], v[96:99], %[q1], %[nm1]\n\t" SYNC                                 \
            "ds_read_b128 v[96:99], %[ka] offset:" #U "*1024+1024\n\t"                                            \
            "ds_read_b128 v[" #V0N ":" #V0N "+3], %[va] offset:" #U "*1024+1024\n\t",                             \
            "s_waitcnt lgkmcnt(2)\n\t")                                                                           \
    AT_HALF(80, "%[o1]", V0C,                                                                                     \
            "v_mfma_f32_32x32x16_bf16 %[o0], v[104:107], v[112:115], %[o0]\n\t"                                   \
            "s_waitcnt lgkmcnt(0)\n\t",                                                                           \
            "v_mfma_f32_32x32x16_bf16 v[64:79], v[96:99], %[q0], %[nm0]\n\t", "")

DI f32x16 neg_tile_max(const bf16x8& k0, const bf16x8& q) {
    const f32x16 s0 = MFMA32(k0, q, f32x16{});
    float m = s0[0];
#pragma unroll
    for (int r = 1; r < 16; ++r) m = fmaxf(m, s0[r]);
    m = half_max(m);
    f32x16 n;
#pragma unroll
    for (int r = 0; r < 16; ++r) n[r] = -m;
    return n;
}
DI f32x16 attn_exact(const bf16x8& q, rsrc_t rws, unsigned kv, unsigned ones, int lane) {
    const bool vlane = (lane & 31) < 16;
    const bf16x8 pad = lds_b128(ones);
    float m = -INFINITY;
    f32x16 o = f32x16{};
#pragma unroll 1
    for (int t = 0; t < 64; ++t) {
        const bf16x8 kf = __builtin_bit_cast(bf16x8, ld16(rws, lane * 16, kv + t * 1024));
        const unsigned vo = (lane >> 5) * 256 + (lane & 15) * 16;
        bf16x8 v0 = __builtin_bit_cast(bf16x8, ld16(rws, vo, kv + 65536 + t * 1024)), v1 = __builtin_bit_cast(bf16x8, ld16(rws, vo + 512, kv + 65536 + t * 1024));
        if (!vlane) { v0 = pad; v1 = pad; }
        const f32x16 s = MFMA32(kf, q, f32x16{});
        float tm = s[0];
#pragma unroll
        for (int r = 1; r < 16; ++r) tm = fmaxf(tm, s[r]);
        const float mn = fmaxf(m, half_max(tm));
        const float alpha = __builtin_amdgcn_exp2f(m - mn);
        m = mn;
#pragma unroll
        for (int r = 0; r < 16; ++r) o[r] *= alpha;
        u32x4 p0, p1;
#pragma unroll
        for (int j = 0; j < 4; ++j) {
            p0[j] = pack_bf16(__builtin_amdgcn_exp2f(s[2 * j] - m), __builtin_amdgcn_exp2f(s[2 * j + 1] - m));
            p1[j] = pack_bf16(__builtin_amdgcn_exp2f(s[8 + 2 * j] - m), __builtin_amdgcn_exp2f(s[8 + 2 * j + 1] - m));
        }
        o = MFMA32(v0, __builtin_bit_cast(bf16x8, p0), o);
        o = MFMA32(v1, __builtin_bit_cast(bf16x8, p1), o);
    }
    return o;
}

DI void attn_load(const MkArgs& p, rsrc_t rws, int layer, unsigned lds0, int wid, int lane, u64 tag) {
    ST_DECL ST(0);
    char* ws = p.ws;
    FRESH(ws, lds0, wid, lane, bid);
    const int x = bid & 7, idx = bid >> 3, head = x * 4 + (idx >> 3), qb = idx & 7;
    const unsigned lw = layer * WS_LAYER;
    const unsigned vo = lane * 16;
    const char* src = ws + lw + WS_KV + (size_t)head * 131072 + wid * 2048;
    auto chunk = [&](int c) {
        const char* s = src + ((qb + c) & 7) * 8192;
        glds_run<2>(s, vo, lds0 + AT_K + c * 8192 + wid * 2048);
        glds_run<2>(s + 65536, vo, lds0 + AT_V + c * 8192 + wid * 2048);
    };
    ST(1);
    asm volatile("s_barrier" ::: "memory");
    ST(2);
    glds_run<2>(ws + lw + WS_Q + (size_t)(head * 64 + qb * 8 + wid * 2) * 1024, vo, lds0 + AT_Q + wid * 2048);
    chunk(0);
    wait_vm_barrier<0>();
    ST(3);
    wait_tags(ws, lw + WS_FL + PH_ATT * FL_PH + (head * 8 + qb) * FR, 32, tag, lane);
    chunk(1);
    chunk(2); chunk(3); chunk(4); chunk(5); chunk(6); chunk(7);
    wait_vm_barrier<0>();
    ST(4);
    if (layer == 0) {
#pragma unroll 1
        for (int u = bid * 4 + wid; u < 1856; u += 1024) {
            const char* s; size_t dst; int ld2;
            if (u < 896) { const int mat = u >= 448, v = u - mat * 448, pan = v >> 4, kb = v & 15;
                s = (mat ? p.Wu : p.Wg) + (size_t)kb * 32 * (FF * 2) + pan * 128; ld2 = FF * 2; dst = Geo<PH_GLU>::packed_off(pan >> 1, mat * 2 + (pan & 1), kb); }
            else if (u < 1024) { const int v = u - 896, pan = v & 7, kb = v >> 3;
                s = p.Wo + (size_t)kb * 32 * 1024 + pan * 128; ld2 = 1024; dst = Geo<PH_WO>::packed_off(pan, 0, kb); }
            else if (u < 1472) { const int v = u - 1024, pan = v & 7, kb = v >> 3;
                s = p.Wd + (size_t)kb * 32 * 1024 + pan * 128; ld2 = 1024; dst = Geo<PH_DN>::packed_off(pan, 0, kb); }
            else { const int v = u - 1472, pan = v >> 4, kb = v & 15;
                s = p.Wqkv + (size_t)kb * 32 * 3072 + pan * 128; ld2 = 3072; dst = Geo<PH_QKV>::packed_off(pan & 7, pan >> 3, kb); }
            u32x4 v[4];
            load_unit(v, s, ld2, lane);
            store_unit_packed(v, rws, (unsigned)dst, lane);
            if (u < 896) unit_colsum(v, lds0 + LDS_ROWG + wid * 256, lane);
        }
        if (arrive(lds0 + LDS_CNT + 4, lane) == 3) {
            if (bid < 224) { store_colsum(lds0 + LDS_ROWG, rws, WS_CSG + bid * 256, lane); asm volatile("s_waitcnt vmcnt(0)" ::: "memory"); }
            if (lane == 0) __hip_atomic_store((GAS u64*)(ws + WS_FL + FL_PK + bid * 8), tag, __ATOMIC_RELAXED, __HIP_MEMORY_SCOPE_AGENT);
            __builtin_amdgcn_s_sleep(24);
#pragma unroll 1
            for (int i = 0; i < 4; ++i) wait_tags(ws, WS_FL + FL_PK + ((bid + i) & 3) * 512, 64, tag, lane);
        }
    }
    lds_barrier();
    prefetch_b<PH_WO>(ws, lds0, wid, lane);
    lds_barrier();
    ST(5); ST_OUT(layer * 5 + PH_ATT, 0);
}

DI void attn_comp(char* ws, rsrc_t rws, int layer, unsigned lds0, int cw, int lane, u64 tag, bool& poller) {
    ST_DECL ST(0);
    FRESH(ws, lds0, cw, lane, bid);
    const int x = bid & 7, idx = bid >> 3, head = x * 4 + (idx >> 3), qb = idx & 7;
    const unsigned lw = layer * WS_LAYER;
    dep_barrier(poller, ws, lw + WS_FL + PH_ATT * FL_PH + (head * 8 + qb) * FR + qb * 4 * TS, 4, tag, lane);
    const int r32 = lane & 31, hh = lane >> 5;
    const unsigned ka0 = lds0 + AT_K + lane * 16;
    const bool vlane = r32 < 16;
    const unsigned va0 = lds0 + (vlane ? AT_V : AT_ONES) + hh * 256 + (r32 & 15) * 16;
    const unsigned vstep = vlane ? 8192u : 0u;

    ST(1);
    lds_barrier();
    ST(2);
    const bf16x8 q0 = lds_b128(lds0 + AT_Q + cw * 2048 + lane * 16), q1 = lds_b128(lds0 + AT_Q + cw * 2048 + 1024 + lane * 16);
    const bf16x8 k0 = lds_b128(ka0);
    const f32x16 nm0 = neg_tile_max(k0, q0), nm1 = neg_tile_max(k0, q1);
    f32x16 o0 = f32x16{}, o1 = f32x16{};
    STC(6);
    {
        unsigned ka = ka0, va = va0; int cnt, tmp;
        asm volatile(
            "ds_read_b128 v[96:99], %[ka]\n\t"
            "ds_read_b128 v[100:103], %[va]\n\t"
            "v_mov_b32 v104, 0\n\tv_mov_b32 v105, 0\n\tv_mov_b32 v106, 0\n\tv_mov_b32 v107, 0\n\t"
            "v_mov_b32 v112, 0\n\tv_mov_b32 v113, 0\n\tv_mov_b32 v114, 0\n\tv_mov_b32 v115, 0\n\t"
            "s_mov_b32 %[cnt], 0\n\t"
            "s_waitcnt lgkmcnt(0)\n\t"
            "v_mfma_f32_32x32x16_bf16 v[64:79], v[96:99], %[q0], %[nm0]\n\t"
            "s_nop 12\n\t"
            "1:\n\t"
            AT_TILE(0, 100, 116, "") AT_TILE(1, 116, 100, "") AT_TILE(2, 100, 116, "") AT_TILE(3, 116, 100, "")
            AT_TILE(4, 100, 116, "") AT_TILE(5, 116, 100, "") AT_TILE(6, 100, 116, "")
            AT_TILE(7, 116, 100, "s_and_b32 %[tmp], %[cnt], 3\n\ts_cbranch_scc1 2f\n\ts_barrier\n\t2:\n\t")
            "v_add_u32 %[ka], 0x2000, %[ka]\n\t"
            "v_add_u32 %[va], %[vstep], %[va]\n\t"
            "s_add_u32 %[cnt], %[cnt], 1\n\t"
            "s_cmp_lt_u32 %[cnt], 8\n\t"
            "s_cbranch_scc1 1b\n\t"
            "v_mfma_f32_32x32x16_bf16 %[o1], v[104:107], v[112:115], %[o1]\n\t"
            "s_nop 15\n\t"
            : [o0] "+v"(o0), [o1] "+v"(o1), [ka] "+v"(ka), [va] "+v"(va), [cnt] "=&s"(cnt), [tmp] "=&s"(tmp)
            : [q0] "v"(q0), [q1] "v"(q1), [nm0] "v"(nm0), [nm1] "v"(nm1), [vstep] "v"(vstep)
            : "memory", "scc", "v64", "v65", "v66", "v67", "v68", "v69", "v70", "v71", "v72", "v73", "v74", "v75", "v76", "v77", "v78", "v79",
              "v80", "v81", "v82", "v83", "v84", "v85", "v86", "v87", "v88", "v89", "v90", "v91", "v92", "v93", "v94", "v95",
              "v96", "v97", "v98", "v99", "v100", "v101", "v102", "v103", "v104", "v105", "v106", "v107",
              "v108", "v109", "v110", "v111", "v112", "v113", "v114", "v115", "v116", "v117", "v118", "v119");
    }
    STC(7); ST(3);
    if (__builtin_expect(__any(!(o0[8] < LMAX) || !(o1[8] < LMAX)), 0)) {
        o0 = attn_exact(q0, rws, lw + WS_KV + head * 131072, lds0 + AT_ONES + (r32 & 15) * 16, lane);
        o1 = attn_exact(q1, rws, lw + WS_KV + head * 131072, lds0 + AT_ONES + (r32 & 15) * 16, lane);
    }
    const float i0 = 1.0f / o0[8], i1 = 1.0f / o1[8];
    u32x4 w0, w1;
#pragma unroll
    for (int j = 0; j < 4; ++j) { w0[j] = pack_bf16(o0[2 * j] * i0, o0[2 * j + 1] * i0); w1[j] = pack_bf16(o1[2 * j] * i1, o1[2 * j + 1] * i1); }
    const int qtile = qb * 8 + cw * 2;
    st16(rws, lane * 16, lw + WS_O + qtile * 32768 + head * 1024, w0);
    st16(rws, lane * 16, lw + WS_O + (qtile + 1) * 32768 + head * 1024, w1);
    lds_barrier();
    ST(4);
    poller = publish(ws, lw + WS_FL + PH_WO * FL_PH + qb * 32 * FR, 32, 1, head, tag, lds0 + LDS_CNT, lane);
    ST(5); ST_OUT(layer * 5 + PH_ATT, 1);
}

__global__ __launch_bounds__(512, 2) void mk_kernel(MkArgs p) {
    extern __shared__ __attribute__((aligned(16))) char smem[];
    const unsigned lds0 = (unsigned)(uintptr_t)(LAS char*)smem;
    const int tid = threadIdx.x, lane = tid & 63, wid = __builtin_amdgcn_readfirstlane(tid >> 6);
    const rsrc_t rws = make_rsrc(p.ws, WS_END);
    const bool glu = (blockIdx.x >> 3) < 28;
    u64 epoch;
    asm volatile("s_load_dwordx2 %0, %1, 0x0" : "=s"(epoch) : "s"(p.ws + WS_FL + FL_EPOCH) : "memory");
    if (wid < 4) {
        first_load(p.x, p.Wqkv, lds0, wid, lane);
        asm volatile("s_waitcnt lgkmcnt(0)" : "+s"(epoch));
        const u64 tag = epoch + 1;
#pragma unroll 1
        for (int layer = 0; layer < 2; ++layer) {
            if (layer) gemm_load<PH_QKV>(p.ws, layer, lds0, wid, lane, false);
            attn_load(p, rws, layer, lds0, wid, lane, tag);
            gemm_load<PH_WO>(p.ws, layer, lds0, wid, lane, !glu);
            if (glu) gemm_load<PH_GLU>(p.ws, layer, lds0, wid, lane, false);
            else prefetch_b<PH_DN>(p.ws, lds0, wid, lane);
            gemm_load<PH_DN>(p.ws, layer, lds0, wid, lane, layer == 1);
        }
    } else {
        const rsrc_t rout = make_rsrc(p.out, SEQ * DM * 2), rx = make_rsrc(p.x, SEQ * DM * 2);
        __builtin_amdgcn_s_setprio(3);
        u32x4 sv[4];
        startup_load(p, wid - 4, lane, sv);
        {
            const unsigned ov = ((tid & 15) == 0 || (tid & 15) == 4) ? 0x3f803f80u : 0u;
            *(LAS u32x4*)(uintptr_t)(lds0 + AT_ONES + (tid - 256) * 16) = u32x4{ov, ov, ov, ov};
            *(LAS u32x4*)(uintptr_t)(lds0 + AT_ONES + tid * 16) = u32x4{ov, ov, ov, ov};
            if (tid < 320) *(LAS u32x4*)(uintptr_t)(lds0 + AT_ONES + (tid + 256) * 16) = u32x4{ov, ov, ov, ov};
            if (tid == 256) *(LAS u32x2*)(uintptr_t)(lds0 + LDS_CNT) = u32x2{0, 0};
        }
        lds_barrier();
        asm volatile("" : "+v"(sv[0]), "+v"(sv[1]), "+v"(sv[2]), "+v"(sv[3]));
        bool poller = startup_job(rws, lds0, wid - 4, lane, sv);
        asm volatile("s_waitcnt lgkmcnt(0)" : "+s"(epoch));
        const u64 tag = epoch + 1;
        __builtin_amdgcn_s_setprio(0);
#pragma unroll 1
        for (int layer = 0; layer < 2; ++layer) {
            gemm_comp<PH_QKV>(p.ws, rws, rout, layer, lds0, wid - 4, lane, tag, poller);
            attn_comp(p.ws, rws, layer, lds0, wid - 4, lane, tag, poller);
            gemm_comp<PH_WO>(p.ws, rws, rx, layer, lds0, wid - 4, lane, tag, poller);
            if (glu) gemm_comp<PH_GLU>(p.ws, rws, rout, layer, lds0, wid - 4, lane, tag, poller);
            gemm_comp<PH_DN>(p.ws, rws, rout, layer, lds0, wid - 4, lane, tag, poller);
        }
        if (blockIdx.x == 0 && tid == 256) __hip_atomic_store((GAS u64*)(p.ws + WS_FL + FL_EPOCH), tag, __ATOMIC_RELAXED, __HIP_MEMORY_SCOPE_AGENT);
    }
}

extern "C" void kernel_launch(void* const* d_in, const int* in_sizes, int n_in, void* d_out, int out_size, void* d_ws, size_t ws_size,
                              hipStream_t stream) {
    (void)hipFuncSetAttribute((const void*)mk_kernel, hipFuncAttributeMaxDynamicSharedMemorySize, LDS_BYTES);
    mk_kernel<<<256, 512, LDS_BYTES, stream>>>(MkArgs{(const char*)d_in[0], (const char*)d_in[1], (const char*)d_in[2], (const char*)d_in[4], (const char*)d_in[5],
                                                      (const char*)d_in[6], (char*)d_ws, (char*)d_out});
}
```

```cpp
#include <hip/hip_runtime.h>
#include <hip/hip_bf16.h>
#include <stdint.h>
#include <utility>

typedef short bf16x8 __attribute__((ext_vector_type(8)));
typedef float f32x16 __attribute__((ext_vector_type(16)));
typedef float f32x4 __attribute__((ext_vector_type(4)));
typedef float f32x2 __attribute__((ext_vector_type(2)));
typedef unsigned u32x4 __attribute__((ext_vector_type(4)));
typedef unsigned u32x2 __attribute__((ext_vector_type(2)));
typedef __bf16 bf16x2_t __attribute__((ext_vector_type(2)));
typedef __amdgpu_buffer_rsrc_t rsrc_t;
#define LAS __attribute__((address_space(3)))
#define GAS __attribute__((address_space(1)))
#define DI __device__ __forceinline__
#define MFMA32(a, b, c) __builtin_amdgcn_mfma_f32_32x32x16_bf16((a), (b), (c), 0, 0, 0)
#define SBAR() __builtin_amdgcn_sched_barrier(0)

constexpr int SEQ = 2048, DM = 512, FF = 1792;
constexpr float LN_EPS = 1e-5f;
constexpr float QSCALE = 0.25f * 1.4426950408889634f;

enum { PH_QKV = 0, PH_ATT = 1, PH_WO = 2, PH_GLU = 3, PH_DN = 4 };
#define LMAX 1.0e18f
constexpr unsigned MB = 1u << 20;
constexpr unsigned WS_LAYER = 20 * MB;
constexpr unsigned WS_Q = 0, WS_KV = 2 * MB, WS_O = 6 * MB, WS_XA = 8 * MB, WS_XB = 10 * MB, WS_H = 12 * MB, WS_STA = 19 * MB, WS_STB = WS_STA + MB / 4, WS_FL = WS_STB + MB / 4;
constexpr unsigned TS = 8, FR = 32 * TS;
constexpr unsigned FL_PH = 256 * FR, FL_PK = 5 * FL_PH, FL_EPOCH = FL_PK + 2048;
constexpr unsigned WS_WP = 2 * WS_LAYER;
constexpr unsigned WP_QKV = WS_WP, WP_O = WP_QKV + DM * 3 * DM * 2, WP_GU = WP_O + DM * DM * 2, WP_D = WP_GU + DM * FF * 4;
constexpr unsigned WS_CSQ = WP_D + FF * DM * 2, WS_CSG = WS_CSQ + 3 * DM * 16, WS_END = WS_CSG + 2 * FF * 16;
typedef unsigned long long u64;

template <int... I, class Fn> DI void static_for_impl(std::integer_sequence<int, I...>, Fn&& f) { (f(std::integral_constant<int, I>{}), ...); }
template <int N, class Fn> DI void static_for(Fn&& f) { static_for_impl(std::make_integer_sequence<int, N>{}, f); }

DI unsigned pack_bf16(float lo, float hi) { bf16x2_t p; p.x = (__bf16)lo; p.y = (__bf16)hi; return __builtin_bit_cast(unsigned, p); }
DI float bf_lo(unsigned u) { return __uint_as_float(u << 16); }
DI float bf_hi(unsigned u) { return __uint_as_float(u & 0xffff0000u); }
template <int CTRL> DI float dpp(float v) { return __uint_as_float(__builtin_amdgcn_update_dpp(0u, __float_as_uint(v), CTRL, 0xf, 0xf, true)); }
DI float half_sum(float v) {
    auto r = __builtin_amdgcn_permlane32_swap(__float_as_uint(v), __float_as_uint(v), false, false);
    return __uint_as_float(r[0]) + __uint_as_float(r[1]);
}
DI float half_max(float v) {
    auto r = __builtin_amdgcn_permlane32_swap(__float_as_uint(v), __float_as_uint(v), false, false);
    return fmaxf(__uint_as_float(r[0]), __uint_as_float(r[1]));
}
DI void st16(rsrc_t r, unsigned voff, unsigned soff, u32x4 v) { __builtin_amdgcn_raw_buffer_store_b128(v, r, voff + soff, 0, 16); }
DI u32x4 ld16(rsrc_t r, unsigned voff, unsigned soff) { return __builtin_amdgcn_raw_buffer_load_b128(r, voff, soff, 16); }
template <int N> DI void wait_vm_barrier() { asm volatile("s_waitcnt vmcnt(%0) lgkmcnt(0)\n\ts_barrier" ::"i"(N) : "memory"); }
DI void lds_barrier() { asm volatile("s_waitcnt lgkmcnt(0)\n\ts_barrier" ::: "memory"); }
DI bf16x8 lds_b128(unsigned addr) { return *(const LAS bf16x8*)(uintptr_t)addr; }
DI bf16x8 lds_tr_frag(unsigned addr) {
    typedef short v4i16_t __attribute__((ext_vector_type(4)));
    v4i16_t lo = __builtin_amdgcn_ds_read_tr16_b64_v4i16((LAS v4i16_t*)(uintptr_t)addr);
    v4i16_t hi = __builtin_amdgcn_ds_read_tr16_b64_v4i16((LAS v4i16_t*)(uintptr_t)(addr + 512));
    return __builtin_shufflevector(lo, hi, 0, 1, 2, 3, 4, 5, 6, 7);
}
template <int N> DI void glds_run(const char* sbase, unsigned voff, unsigned lds_dst) {
    static_assert(N >= 1 && N <= 4, "");
#define GL(o) "\n\tglobal_load_lds_dwordx4 %0, %1 offset:" #o " sc1"
    if constexpr (N == 1) asm volatile("s_mov_b32 m0, %2\n\ts_nop 3" GL(0)::"v"(voff), "s"(sbase), "s"(lds_dst) : "memory");
    if constexpr (N == 2) asm volatile("s_mov_b32 m0, %2\n\ts_nop 3" GL(0) GL(1024)::"v"(voff), "s"(sbase), "s"(lds_dst) : "memory");
    if constexpr (N == 3) asm volatile("s_mov_b32 m0, %2\n\ts_nop 3" GL(0) GL(1024) GL(2048)::"v"(voff), "s"(sbase), "s"(lds_dst) : "memory");
    if constexpr (N == 4) asm volatile("s_mov_b32 m0, %2\n\ts_nop 3" GL(0) GL(1024) GL(2048) GL(3072)::"v"(voff), "s"(sbase), "s"(lds_dst) : "memory");
#undef GL
}
DI void glds_in(const char* sbase, unsigned voff, unsigned lds_dst) {
    asm volatile("s_mov_b32 m0, %2\n\ts_nop 3\n\tglobal_load_lds_dwordx4 %0, %1" ::"v"(voff), "s"(sbase), "s"(lds_dst) : "memory");
}
DI int arrive(unsigned cnt_addr, int lane) {
    asm volatile("s_waitcnt vmcnt(0) lgkmcnt(0)" ::: "memory");
    unsigned old = 0;
    if (lane == 0) old = __hip_atomic_fetch_add((LAS unsigned*)(uintptr_t)cnt_addr, 1u, __ATOMIC_RELAXED, __HIP_MEMORY_SCOPE_WORKGROUP);
    asm volatile("" ::: "memory");
    return __builtin_amdgcn_readfirstlane(old) & 3;
}
DI void signal(char* ws, unsigned first_line_off, int n, int lstride, int me, u64 tag, int lane) {
    if (lane < n) __hip_atomic_store((GAS u64*)(ws + first_line_off + lane * lstride * FR + me * TS), tag, __ATOMIC_RELAXED, __HIP_MEMORY_SCOPE_AGENT);
}
DI bool publish(char* ws, unsigned first_line_off, int n, int lstride, int me, u64 tag, unsigned cnt_addr, int lane) {
    const int rank = arrive(cnt_addr, lane);
    if (rank == 3) signal(ws, first_line_off, n, lstride, me, tag, lane);
    return rank == 3;
}
DI void wait_tags(const char* ws, unsigned off, int n, u64 tag, int lane) {
    const GAS u64* f = (const GAS u64*)(ws + off + (lane < n ? lane : 0) * TS);
#pragma unroll 1
    for (int spin = 0; spin < (1 << 16); ++spin) {
        const u64 v = __hip_atomic_load(f, __ATOMIC_RELAXED, __HIP_MEMORY_SCOPE_AGENT);
        if (__all(v == tag)) break;
        __builtin_amdgcn_s_sleep(1);
    }
    asm volatile("s_waitcnt vmcnt(0)" ::: "memory");
}
DI void dep_barrier(bool poller, const char* ws, unsigned off, int n, u64 tag, int lane) {
    if (__builtin_amdgcn_readfirstlane((int)poller)) wait_tags(ws, off, n, tag, lane);
    lds_barrier();
}

#define ST_DECL
#define ST(i)
#define STC(i)
#define ST_OUT(kind, role)

template <int PH> struct Cfg;
template <> struct Cfg<PH_QKV> { static constexpr int KTOT = 512, MT = 2, NP = 3, WM = 2, WN = 2, WK = 1, BK = 32, NS = 8, PA = 8, KG = 2, NCB = 8, XC = 2; };
template <> struct Cfg<PH_WO> { static constexpr int KTOT = 512, MT = 2, NP = 1, WM = 2, WN = 2, WK = 1, BK = 64, NS = 8, PA = 4, KG = 4, NCB = 8, XC = 2; };
template <> struct Cfg<PH_GLU> { static constexpr int KTOT = 512, MT = 4, NP = 4, WM = 1, WN = 4, WK = 1, BK = 32, NS = 5, PA = 3, KG = 1, NCB = 14, XC = 2; };
template <> struct Cfg<PH_DN> { static constexpr int KTOT = FF, MT = 2, NP = 1, WM = 1, WN = 2, WK = 2, BK = 64, NS = 8, PA = 4, KG = 2, NCB = 8, XC = 2; };

constexpr int AT_K = 0, AT_V = 65536, AT_ONES = 131072, AT_Q = AT_ONES + 9216, LDS_ROWQ = AT_Q + 8192, LDS_ROWG = LDS_ROWQ + 1024, LDS_CNT = LDS_ROWG + 1024, LDS_X = LDS_CNT + 16, LDS_BYTES = LDS_X + 8192;

template <int PH> struct Geo : Cfg<PH> {
    using C = Cfg<PH>;
    static constexpr int TM = C::MT / C::WM, TN = C::NP * 2 / C::WN, NT = C::KTOT / C::BK, KS = C::BK / 16, UPS = KS / C::WK / C::KG, NU = NT * UPS;
    static constexpr int ABYTES = C::MT * C::BK * 64, PBYTES = C::BK * 128, BBYTES = C::NP * PBYTES, SBYTES = ABYTES + BBYTES;
    static constexpr int GA = ABYTES / 4096, GB = BBYTES / 4096, G = GA + GB;
    static constexpr bool LN = PH == PH_QKV || PH == PH_GLU;
    static constexpr int ROWF = PH == PH_QKV ? LDS_ROWQ : LDS_ROWG;
    static constexpr int NRB = SEQ / (32 * C::MT), AUX = PH == PH_GLU ? 4 : 0;
    static constexpr unsigned WP = PH == PH_QKV ? WP_QKV : PH == PH_WO ? WP_O : PH == PH_GLU ? WP_GU : WP_D;
    static constexpr unsigned A = PH == PH_QKV ? WS_XA : PH == PH_WO ? WS_O : PH == PH_GLU ? WS_XB : WS_H;
    static_assert(C::WM * C::WN * C::WK == 4 && GA * 4096 == ABYTES && GB * 4096 == BBYTES && GA <= KS && KS % GA == 0 && GB <= 4, "pieces must divide over the waves");
    static_assert(C::NS * SBYTES <= 131072 && C::NS <= NT, "LDS budget");
    static constexpr int a_off(int k) {
        if (PH == PH_WO) return (k < 4 ? 32768 : 65536) + k * 8192;
        if (PH == PH_DN && k >= 8) return (((k - 8) / 2 + 1) % 8) * SBYTES + ((k - 8) & 1) * ABYTES;
        return ((k + (PH == PH_QKV ? 6 : PH == PH_DN ? 1 : 0)) % C::NS) * SBYTES;
    }
    static constexpr int b_off(int k) { return PH == PH_WO ? (k < 4 ? 0 : 32768) + k * 8192 : a_off(k) + ABYTES; }
    static constexpr bool held(int k) { return PH == PH_DN && k >= 8; }
    static constexpr bool uses(int k, int lo, int hi) { return (lo < a_off(k) + ABYTES && a_off(k) < hi) || (!held(k) && lo < b_off(k) + BBYTES && b_off(k) < hi); }
    static constexpr bool SWAPIN = PH == PH_DN || PH == PH_QKV;
    static DI void tile(int b, int& rb, int& cb) {
        const int x = b & 7, i = b >> 3;
        constexpr int CH = C::NCB / C::XC, RH = NRB * C::XC / 8;
        rb = (x / C::XC) * RH + i / CH; cb = (x % C::XC) * CH + i % CH;
    }
    static DI size_t packed_off(int cb, int pan, int kb) {
        return (size_t)WP + (size_t)cb * (NT * BBYTES) + (size_t)((kb * 32) / C::BK) * BBYTES + pan * PBYTES + ((kb * 32) % C::BK) * 128;
    }
};

DI unsigned tr_lane_off(int lane, int j) {
    const int hh = lane >> 5, blk = (lane >> 4) & 1, q = (lane & 15) >> 2, pp = lane & 3, pi = ((pp & 1) << 1) | (pp >> 1);
    const int ch = (4 * j + 2 * blk + (pi >> 1)) ^ (((q >> 1) & 1) << 2);
    return (8 * hh + q) * 128 + ch * 16 + 8 * (pi & 1);
}
DI int slot_col(int r32) { const int pp = (r32 >> 2) & 3; return (r32 & 16) + 4 * (((pp & 1) << 1) | (pp >> 1)) + (r32 & 3); }
DI bf16x8 corr_frag(float v, bool second_lo, int hh) {
    const unsigned h = pack_bf16(v, 0.f) & 0xffffu, l = pack_bf16(v - bf_lo(h), 0.f) & 0xffffu;
    u32x4 u; u[0] = second_lo ? (h | (l << 16)) : (h | (h << 16)); u[1] = second_lo ? h : l; u[2] = 0; u[3] = 0;
    if (hh) { u[0] = 0; u[1] = 0; }
    return __builtin_bit_cast(bf16x8, u);
}

#define FRESH(ws, lds0, w, lane, b) int b = blockIdx.x; asm volatile("" : "+s"(ws), "+s"(lds0), "+s"(w), "+s"(b)); asm volatile("" : "+v"(lane))

enum { OP_A, OP_F, OP_X, OP_W, OP_N, OP_E, OP_R };
struct Sched { int n; int op[128]; int arg[128]; int pend[128]; int nn[128]; int xw; };
template <int PH, int NX> constexpr Sched make_sched() {
    using G_ = Geo<PH>; using N_ = Geo<NX>;
    Sched sc{}; int cnt = 0, ncnt = 0, lastop[64] = {}, xpos = 0; bool ndone[8] = {};
    int next = 0;
    auto push = [&](int op, int arg) { sc.op[sc.n] = op; sc.arg[sc.n] = arg; sc.pend[sc.n] = 0; ++sc.n; };
    auto item = [&](int s) -> int {
        if (next < G_::NS) { push(OP_A, next); cnt += G_::GA; lastop[next] = cnt; ++next; return G_::GA; }
        if (next == G_::NS) { ++next; if (G_::AUX) { push(OP_X, 0); cnt += G_::AUX; xpos = cnt; return G_::AUX; } }
        const int k = next - 1;
        bool ok = k < G_::NT && k <= s + 8;
        for (int j = s > 0 ? s - 1 : 0; j < k && ok; ++j) ok = !G_::uses(j, G_::a_off(k), G_::a_off(k) + G_::ABYTES);
        if (ok) { const int g = G_::held(k) ? G_::GA : G_::G; push(G_::held(k) ? OP_A : OP_F, k); cnt += g; lastop[k] = cnt; ++next; return g; }
        return 0;
    };
    auto nextb = [&](int s) {
        if (PH == PH_QKV || (PH == PH_DN && s < G_::NT && (s & 1))) return;
        for (int j = 0; j < N_::NS; ++j) {
            if (ndone[j]) continue;
            bool ok = next > G_::NT;
            for (int k = s; k < G_::NT && ok; ++k) ok = !G_::uses(k, N_::b_off(j), N_::b_off(j) + N_::BBYTES);
            if (ok) { push(OP_N, j); cnt += N_::GB; ncnt += N_::GB; ndone[j] = true; if (PH == PH_DN && s < G_::NT) return; }
        }
    };
    for (int k = 0; k < G_::PA; ++k) item(0);
    sc.xw = -1;
    for (int s = 0; s < G_::NT; ++s) {
        push(OP_W, s); sc.pend[sc.n - 1] = cnt - lastop[s]; sc.nn[sc.n - 1] = ncnt;
        if (G_::AUX && sc.xw < 0 && xpos && lastop[s] > xpos) sc.xw = s;
        for (int q = 0; q < G_::G;) { const int r = item(s); if (!r) break; q += r; }
        if (G_::WK > 1 && s == G_::NT - 1) { push(OP_R, 0); nextb(G_::NT); }
        else nextb(s > 0 ? s - 1 : 0);
    }
    push(OP_E, 0);
    nextb(G_::NT);
    return sc;
}
template <int PH> struct NextPh { static constexpr int v = PH == PH_WO ? PH_GLU : PH == PH_GLU ? PH_DN : PH_QKV; };
template <int PH> struct SchedOf { static constexpr Sched v = make_sched<PH, NextPh<PH>::v>(); };

template <int PH> DI void issue_b(const char* ws, int k, int cb, unsigned lds0, int wid, unsigned vo) {
    using G_ = Geo<PH>;
    glds_run<G_::GB>(ws + G_::WP + (size_t)cb * (G_::NT * G_::BBYTES) + k * G_::BBYTES + wid * G_::GB * 1024, vo, lds0 + G_::b_off(k) + wid * G_::GB * 1024);
}
template <int PH> DI void prefetch_b(char* ws, unsigned lds0, int wid, int lane) {
    FRESH(ws, lds0, wid, lane, bid);
    int rb, cb; Geo<PH>::tile(bid, rb, cb);
#pragma unroll
    for (int k = 0; k < Geo<PH>::NS; ++k) issue_b<PH>(ws, k, cb, lds0, wid, lane * 16);
}

DI void first_load(const char* x0, const char* wqkv, unsigned lds0, int wid, int lane) {
    using G_ = Geo<PH_QKV>;
    ST_DECL ST(0); ST(1); ST(2);
    int rb, cb; G_::tile(blockIdx.x, rb, cb);
    const int rot = 2 * ((rb + cb) & 3);
    const char* sA = x0 + (size_t)(rb * 64 + wid * 16) * 1024;
    const unsigned vA0 = (lane >> 3) * 1024 + (((lane & 7) ^ (lane >> 4)) << 4), vA1 = 8192 + (vA0 ^ 64);
    const unsigned vB = (lane >> 3) * 3072 + (((lane & 7) ^ (((lane >> 4) & 1) << 2)) << 4);
    const char* sB[3];
#pragma unroll
    for (int g = 0; g < 3; ++g) { const int b = wid * 3 + g; sB[g] = wqkv + (size_t)(b & 3) * (8 * 3072) + (b >> 2) * 1024 + cb * 128; }
    auto issue = [&](int j) {
        const int jj = (j + rot) & 7;
        const unsigned da = lds0 + G_::a_off(2 * j) + (wid >> 1) * G_::SBYTES + (wid & 1) * 2048;
        glds_in(sA + jj * 128, vA0, da); glds_in(sA + jj * 128, vA1, da + 1024);
#pragma unroll
        for (int h = 0; h < 2; ++h)
#pragma unroll
            for (int g = 0; g < 3; ++g) glds_in(sB[g] + (2 * jj + h) * (32 * 3072), vB, lds0 + G_::b_off(2 * j + h) + (wid * 3 + g) * 1024);
    };
    struct S { static constexpr int issued(int s) { int a = 2 + s, b = s < 2 ? 4 : (s + G_::NS - 2) / 2; a = a < b ? a : b; return a < 8 ? a : 8; } };
    issue(0);
    asm volatile("s_barrier" ::: "memory");
    issue(1);
    static_for<G_::NT>([&](auto sc) {
        constexpr int s = decltype(sc)::value;
        wait_vm_barrier<8 * (S::issued(s) - 1 - s / 2) + (s & 1 ? 0 : 3)>();
        if constexpr (s == 0) ST(3);
        if constexpr (s == G_::NT - 1) ST(4);
#pragma unroll
        for (int j = S::issued(s); j < S::issued(s + 1); ++j) issue(j);
    });
    lds_barrier();
    ST(5); ST_OUT(PH_QKV, 0);
}

template <int PH> DI void gemm_load(char* ws, int layer, unsigned lds0, int wid, int lane, bool no_next, u64 tag, bool poller) {
    using G_ = Geo<PH>;
    constexpr int NX = NextPh<PH>::v;
    constexpr int MT = G_::MT, NT = G_::NT, KS = G_::KS, GA = G_::GA;
    ST_DECL ST(0);
    FRESH(ws, lds0, wid, lane, bid);
    int rb, cb, nrb, ncb; G_::tile(bid, rb, cb); Geo<NX>::tile(bid, nrb, ncb);
    const unsigned lw = layer * WS_LAYER;
    const unsigned vo = lane * 16;
    const char* sA = ws + lw + G_::A + (size_t)(rb * MT + (wid * GA) / KS) * (64 * G_::KTOT) + ((wid * GA) % KS) * 1024;
    ST(1);
    if constexpr (G_::SWAPIN) dep_barrier(poller, ws, lw + WS_FL + PH * FL_PH + (rb * G_::NCB + cb) * FR, PH == PH_QKV ? 8 : 14, tag, lane);
    else asm volatile("s_barrier" ::: "memory");
    ST(2);
    f32x4 stp[4];
    static_for<SchedOf<PH>::v.n>([&](auto ic) {
        constexpr int i = decltype(ic)::value, op = SchedOf<PH>::v.op[i], k = SchedOf<PH>::v.arg[i];
        if constexpr (op == OP_A || op == OP_F) glds_run<GA>(sA + k * (KS * 1024), vo, lds0 + G_::a_off(k) + wid * GA * 1024);
        if constexpr (op == OP_F) issue_b<PH>(ws, k, cb, lds0, wid, vo);
        if constexpr (op == OP_N) { if (!no_next) issue_b<NX>(ws, k, ncb, lds0, wid, vo); }
        if constexpr (op == OP_X) {
            const char* sp = ws + lw + WS_STB + (size_t)(rb * MT + wid % MT) * 4096;
            const unsigned so = (lane >> 5) * 2048 + (lane & 31) * 16;
            asm volatile("s_nop 4\n\tglobal_load_dwordx4 %0, %4, %5 sc1\n\tglobal_load_dwordx4 %1, %4, %5 offset:512 sc1\n\t"
                         "global_load_dwordx4 %2, %4, %5 offset:1024 sc1\n\tglobal_load_dwordx4 %3, %4, %5 offset:1536 sc1"
                         : "=&v"(stp[0]), "=&v"(stp[1]), "=&v"(stp[2]), "=&v"(stp[3]) : "v"(so), "s"(sp) : "memory");
        }
        if constexpr (op == OP_W) {
            if constexpr (G_::AUX && k == SchedOf<PH>::v.xw + 1) {
                asm volatile("" : "+v"(stp[0]), "+v"(stp[1]), "+v"(stp[2]), "+v"(stp[3]));
                const f32x4 t = (stp[0] + stp[1]) + (stp[2] + stp[3]);
                const float mean = half_sum(t[0] + t[2]) * (1.0f / DM), var = fmaxf(half_sum(t[1] + t[3]) * (1.0f / DM) - mean * mean, 0.f);
                if (lane < 32) ((LAS f32x2*)(uintptr_t)(lds0 + G_::ROWF))[(wid % MT) * 32 + lane] = f32x2{mean, __builtin_amdgcn_rsqf(var + LN_EPS)};
            }
            static_assert(SchedOf<PH>::v.pend[i] <= 63, "vmcnt");
            if (SchedOf<PH>::v.nn[i] && no_next) wait_vm_barrier<SchedOf<PH>::v.pend[i] - SchedOf<PH>::v.nn[i]>();
            else wait_vm_barrier<SchedOf<PH>::v.pend[i]>();
            if constexpr (k == 0) ST(3);
            if constexpr (k == NT - 1) ST(4);
        }
        if constexpr (op == OP_R) asm volatile("s_barrier" ::: "memory");
        if constexpr (op == OP_E) { if constexpr (G_::WK == 1) lds_barrier(); ST(5); }
    });
    ST_OUT(layer * 5 + PH, 0);
}

DI void load_unit(u32x4 (&v)[4], const char* src, int ld2, int lane) {
    const GAS char* a = (const GAS char*)src + (size_t)(lane >> 3) * ld2 + (lane & 7) * 16;
#pragma unroll
    for (int i = 0; i < 4; ++i) v[i] = *(const GAS u32x4*)(a + (size_t)i * 8 * ld2);
}
DI void store_unit_packed(const u32x4 (&v)[4], rsrc_t rws, unsigned dst, int lane) {
#pragma unroll
    for (int i = 0; i < 4; ++i) st16(rws, ((lane ^ (((lane >> 4) & 1) << 2)) << 4) + i * 1024, dst, v[i]);
}
DI void unit_colsum(const u32x4 (&v)[4], unsigned lds_dst, int lane) {
    float c[8];
#pragma unroll
    for (int e = 0; e < 4; ++e) {
        c[2 * e] = (bf_lo(v[0][e]) + bf_lo(v[1][e])) + (bf_lo(v[2][e]) + bf_lo(v[3][e]));
        c[2 * e + 1] = (bf_hi(v[0][e]) + bf_hi(v[1][e])) + (bf_hi(v[2][e]) + bf_hi(v[3][e]));
    }
#pragma unroll
    for (int e = 0; e < 4; ++e) { auto r = __builtin_amdgcn_permlane32_swap(__float_as_uint(c[e]), __float_as_uint(c[e + 4]), false, false); c[e] = __uint_as_float(r[0]) + __uint_as_float(r[1]); }
#pragma unroll
    for (int e = 0; e < 2; ++e) { auto r = __builtin_amdgcn_permlane16_swap(__float_as_uint(c[e]), __float_as_uint(c[e + 2]), false, false); c[e] = __uint_as_float(r[0]) + __uint_as_float(r[1]); }
    const bool b3 = lane & 8;
    const float t = (b3 ? c[1] : c[0]) + dpp<0x128>(b3 ? c[0] : c[1]);
    *(LAS float*)(uintptr_t)(lds_dst + ((lane & 7) * 8 + (lane >> 3)) * 4) = t;
}
DI void store_colsum(unsigned lds_src, rsrc_t rws, unsigned dst, int lane) {
    const LAS float* r = (const LAS float*)(uintptr_t)lds_src + lane;
    __builtin_amdgcn_raw_buffer_store_b32(__float_as_uint((r[0] + r[64]) + (r[128] + r[192])), rws, dst + lane * 4, 0, 16);
}
DI rsrc_t make_rsrc(const void* p, unsigned bytes) { return __builtin_amdgcn_make_buffer_rsrc((void*)p, 0, bytes, 0x00020000); }

struct MkArgs { const char* x; const char* Wqkv; const char* Wo; const char* Wg; const char* Wu; const char* Wd; char* ws; char* out; };

DI void startup_load(const MkArgs& p, int cw, int lane, u32x4 (&v)[4]) {
    const int bid = blockIdx.x < 224 ? blockIdx.x : 0;
    const char* src = bid < 128 ? p.x + (size_t)(bid >> 1) * 32768 + ((bid & 1) * 4 + cw) * 128 : p.Wqkv + (size_t)((bid & 3) * 4 + cw) * 32 * 3072 + ((bid - 128) >> 2) * 128;
    load_unit(v, src, bid < 128 ? 1024 : 3072, lane);
}
DI bool startup_job(rsrc_t rws, unsigned lds0, int cw, int lane, const u32x4 (&v)[4]) {
    const int bid = blockIdx.x;
    if (bid >= 224) return false;
    if (bid < 128) {
#pragma unroll
        for (int i = 0; i < 4; ++i) {
            float sum = 0.f, sq = 0.f;
#pragma unroll
            for (int j = 0; j < 4; ++j) { const float lo = bf_lo(v[i][j]), hi = bf_hi(v[i][j]); sum += lo + hi; sq = fmaf(lo, lo, fmaf(hi, hi, sq)); }
            sum += dpp<0xB1>(sum); sq += dpp<0xB1>(sq);
            sum += dpp<0x4E>(sum); sq += dpp<0x4E>(sq);
            if ((lane & 3) == 0) *(LAS f32x2*)(uintptr_t)(lds0 + AT_Q + cw * 512 + (i * 8 + (lane >> 3)) * 16 + ((lane >> 2) & 1) * 8) = f32x2{sum, sq};
        }
    } else unit_colsum(v, lds0 + AT_Q + cw * 256, lane);
    if (arrive(lds0 + LDS_CNT, lane) != 3) return false;
    if (bid < 128) {
#pragma unroll
        for (int e = 0; e < 2; ++e) st16(rws, lane * 16 + e * 1024, WS_STA + bid * 2048, *(LAS u32x4*)(uintptr_t)(lds0 + AT_Q + e * 1024 + lane * 16));
    } else store_colsum(lds0 + AT_Q, rws, WS_CSQ + (bid - 128) * 256, lane);
    return true;
}
DI void startup_signal(char* ws, int bid, int lane, u64 tag) {
    asm volatile("s_waitcnt vmcnt(0)" ::: "memory");
    if (bid < 128) signal(ws, WS_FL + PH_QKV * FL_PH + (bid >> 2) * 8 * FR, 8, 1, bid & 3, tag, lane);
    else { const int pan = (bid - 128) >> 2; signal(ws, WS_FL + PH_QKV * FL_PH + (pan & 7) * FR, 32, 8, 4 + (pan >> 3) * 4 + (bid & 3), tag, lane); }
}

template <int PH> DI void gemm_comp(char* ws, rsrc_t rws, rsrc_t rio, int layer, unsigned lds0, int cw, int lane, u64 tag, bool& poller, const bf16x8 (&wd)[40]) {
    using G_ = Geo<PH>;
    constexpr int MT = G_::MT, WN = G_::WN, NS = G_::NS, NT = G_::NT, KS = G_::KS, KG = G_::KG, UPS = G_::UPS, NU = G_::NU, TM = G_::TM, TN = G_::TN;
    constexpr int PBYTES = G_::PBYTES;
    constexpr bool LN = G_::LN;
    constexpr int CORR_U = (PH == PH_QKV ? 15 : 10) * UPS, SIG_U = 3, POLL_U = 9, STAT_U = 12;
    static_assert(!G_::AUX || (SchedOf<PH>::v.xw >= 0 && CORR_U >= (SchedOf<PH>::v.xw + 1) * UPS), "row table must be ready");
    ST_DECL ST(0);
    FRESH(ws, lds0, cw, lane, bid);
    int rb, cb; G_::tile(bid, rb, cb);
    const unsigned lw = layer * WS_LAYER;
    const int r32 = lane & 31, hh = lane >> 5, wm = cw / WN, wn = cw % WN;
    u32x4 resid[2], stp[4];
    float cpart[TN][2];
    auto load_cs = [&]() {
        constexpr int NPAN = PH == PH_QKV ? 8 : 28;
#pragma unroll
        for (int t = 0; t < TN; ++t)
#pragma unroll
            for (int e = 0; e < 2; ++e)
                cpart[t][e] = __uint_as_float(__builtin_amdgcn_raw_buffer_load_b32(rws, slot_col(r32) * 4 + hh * 512 + e * 256,
                                                                                   (PH == PH_QKV ? WS_CSQ : WS_CSG) + (t * NPAN + cb * (WN / 2) + (wn >> 1)) * 1024 + (wn & 1) * 128, 16));
    };
    bf16x8 wf[TN];
    auto make_wf = [&]() {
#pragma unroll
        for (int t = 0; t < TN; ++t) wf[t] = corr_frag(half_sum(cpart[t][0] + cpart[t][1]), false, hh);
    };
    auto load_res = [&]() {
        static_assert(Cfg<PH_WO>::XC == Cfg<PH_DN>::XC, "the residual tile stays with its workgroup");
        const bool raw = PH == PH_WO && layer == 0;
        const unsigned vo = (rb * MT + wm) * 32768 + (raw ? r32 * 1024 + hh * 16 + (cb * 8 + wn * 4) * 16 : lane * 16 + lw + (PH == PH_WO ? WS_XA : WS_XB) + (cb * 8 + wn * 4) * 512);
#pragma unroll
        for (int e = 0; e < 2; ++e) resid[e] = ld16(raw ? rio : rws, vo + e * (raw ? 32 : 1024), 0);
    };
    if constexpr (PH == PH_WO) load_res();
    auto load_st = [&]() {
#pragma unroll
        for (int k = 0; k < 4; ++k) stp[k] = ld16(rws, hh * 2048 + r32 * 16 + k * 512, lw + WS_STA + (rb * MT + wm) * 4096);
    };
    if (PH == PH_GLU || (PH == PH_QKV && layer)) load_cs();
    if constexpr (G_::SWAPIN) {
        if (PH != PH_QKV || layer) wait_vm_barrier<0>();
    } else dep_barrier(poller, ws, lw + WS_FL + PH * FL_PH + (rb * G_::NCB + cb) * FR, PH == PH_WO ? 32 : 16, tag, lane);
    if constexpr (PH == PH_DN) load_res();
    if (PH == PH_QKV && layer) load_st();
    LAS f32x2* rowf = (LAS f32x2*)(uintptr_t)(lds0 + G_::ROWF);
    constexpr bool KSP = G_::WK > 1;
    unsigned aoff = lds0 + (KSP ? wm * (KS * 1024 + 2048) : wm * TM * KS * 1024) + lane * 16;
    unsigned boff = lds0 + (wn >> 1) * PBYTES + tr_lane_off(lane, wn & 1) + (KSP ? wm * 4096 : 0);
    unsigned aoth = aoff + (1 - 2 * wm) * (KS * 1024), aoth2 = aoth + 65536, aown2 = aoff + 65536;
    if constexpr (KSP) asm volatile("" : "+v"(aoth), "+v"(aoth2), "+v"(aown2));
    unsigned aoff2 = aoff + 49152, boff2 = boff + 49152, aoff3 = aoff + 98304, boff3 = boff + 98304;
    asm volatile("" : "+v"(aoff2), "+v"(boff2), "+v"(aoff3), "+v"(boff3));
    unsigned aq[2][4];
    if constexpr (PH == PH_QKV) {
#pragma unroll
        for (int v = 0; v < 4; ++v) {
            aq[0][v] = layer ? aoff + (v >> 1) * G_::SBYTES + (v & 1) * 1024 : lds0 + wm * G_::SBYTES + r32 * 128 + (((2 * v + hh) ^ ((r32 >> 1) & 7)) << 4);
            aq[1][v] = aq[0][v] + 65536;
            asm volatile("" : "+v"(aq[0][v]), "+v"(aq[1][v]));
        }
    }
    constexpr int TSTR = (WN / 2) * PBYTES;

    f32x16 acc[TM][TN];
#pragma unroll
    for (int i = 0; i < TM; ++i)
#pragma unroll
        for (int t = 0; t < TN; ++t) acc[i][t] = f32x16{};
    if constexpr (!LN) {
#pragma unroll
        for (int r = 0; r < 16; r += 2) { acc[0][0][r] = bf_lo(resid[r >> 3][(r >> 1) & 3]); acc[0][0][r + 1] = bf_hi(resid[r >> 3][(r >> 1) & 3]); }
        asm volatile("" : "+v"(acc[0][0]));
    }
    if constexpr (PH == PH_GLU) { make_wf(); asm volatile("" : "+v"(wf[0]), "+v"(wf[1])); }
    float rstd[TM];
    u64 seen = 0;
    constexpr int NF = KG * (TM + TN), NM = KG * TM * TN;
    bf16x8 fr[2][NF];
    auto rd = [&](auto uc, auto fc) {
        constexpr int u = decltype(uc)::value, f = decltype(fc)::value;
        constexpr int ks = (u % UPS) * KG + f / (TM + TN), e = f % (TM + TN);
        constexpr int so = e < TN ? G_::b_off(u / UPS) : G_::a_off(u / UPS), hi = so / 49152, lo = so % 49152;
        if constexpr (e < TN && G_::held(u / UPS)) {}
        else if constexpr (e < TN) fr[u & 1][f] = lds_tr_frag((hi == 0 ? boff : hi == 1 ? boff2 : boff3) + lo + e * TSTR + ks * 2048);
        else if constexpr (KSP) fr[u & 1][f] = lds_b128((e == TN ? (so < 65536 ? aoff : aown2) : (so < 65536 ? aoth : aoth2)) + (so & 65535) + ks * 1024);
        else if constexpr (PH == PH_QKV) fr[u & 1][f] = lds_b128(aq[G_::a_off(u & ~1) >> 16][(u & 1) * 2 + ks] + (G_::a_off(u & ~1) & 65535));
        else fr[u & 1][f] = lds_b128((hi == 0 ? aoff : hi == 1 ? aoff2 : aoff3) + lo + ((e - TN) * KS + ks) * 1024);
    };
    auto mma = [&](auto tc, f32x16& c, const bf16x8& af, const bf16x8& bf) {
        if constexpr (PH == PH_QKV && decltype(tc)::value == 2) c = MFMA32(af, bf, c);
        else c = MFMA32(bf, af, c);
    };
    ST(1);
    lds_barrier();
    ST(2); STC(6);
    static_for<NF>([&](auto fc) { rd(std::integral_constant<int, 0>{}, fc); });
    SBAR();
    static_for<NU>([&](auto uc) {
        constexpr int u = decltype(uc)::value;
        if constexpr (PH == PH_QKV && u == SIG_U) { if (layer == 0 && __builtin_amdgcn_readfirstlane((int)poller)) { __builtin_amdgcn_s_setprio(3); startup_signal(ws, bid, lane, tag); __builtin_amdgcn_s_setprio(0); } SBAR(); }
        if constexpr (PH == PH_QKV && u == POLL_U) {
            if (layer == 0) {
                __builtin_amdgcn_s_setprio(3);
                seen = __hip_atomic_load((const GAS u64*)(ws + WS_FL + PH * FL_PH + (rb * G_::NCB + cb) * FR + (lane & 15) * TS), __ATOMIC_RELAXED, __HIP_MEMORY_SCOPE_AGENT);
                __builtin_amdgcn_s_setprio(0);
            }
            SBAR();
        }
        if constexpr (PH == PH_QKV && u == STAT_U) {
            if (layer == 0) {
                __builtin_amdgcn_s_setprio(3);
                if (!__all(seen == tag)) wait_tags(ws, WS_FL + PH * FL_PH + (rb * G_::NCB + cb) * FR, 16, tag, lane);
                asm volatile("" ::: "memory");
                load_st(); load_cs();
                __builtin_amdgcn_s_setprio(0);
            }
            SBAR();
        }
        if constexpr (LN && u == CORR_U) {
            if constexpr (PH == PH_QKV) make_wf();
#pragma unroll
            for (int i = 0; i < TM; ++i) {
                f32x2 st;
                if constexpr (PH == PH_QKV) {
                    const f32x4 t = (__builtin_bit_cast(f32x4, stp[0]) + __builtin_bit_cast(f32x4, stp[1])) + (__builtin_bit_cast(f32x4, stp[2]) + __builtin_bit_cast(f32x4, stp[3]));
                    const float mean = half_sum(t[0] + t[2]) * (1.0f / DM), var = fmaxf(half_sum(t[1] + t[3]) * (1.0f / DM) - mean * mean, 0.f);
                    st = f32x2{mean, __builtin_amdgcn_rsqf(var + LN_EPS)};
                    if (hh == 0) rowf[cw * 32 + r32] = st;
                } else st = rowf[(wm * TM + i) * 32 + r32];
                rstd[i] = st.y;
                const bf16x8 xf = corr_frag(-st.x, true, hh);
                static_for<TN>([&](auto tc) { mma(tc, acc[i][decltype(tc)::value], xf, wf[decltype(tc)::value]); });
            }
            SBAR();
        }
        if constexpr (u + 1 < NU && (u + 1) % UPS == 0) { asm volatile("s_barrier" ::: "memory"); SBAR(); }
        static_for<NM>([&](auto mc) {
            constexpr int m = decltype(mc)::value, g = m / (TM * TN), i = (m % (TM * TN)) / TN, t = m % TN;
            if constexpr (G_::held(u / UPS)) mma(std::integral_constant<int, t>{}, acc[i][t], fr[u & 1][g * (TM + TN) + TN + i], wd[(u - 8) * 2 + g]);
            else mma(std::integral_constant<int, t>{}, acc[i][t], fr[u & 1][g * (TM + TN) + TN + i], fr[u & 1][g * (TM + TN) + t]);
            if constexpr (u + 1 < NU) {
                SBAR();
                constexpr int PER = PH == PH_GLU ? 1 : 4;
                static_for<PER>([&](auto kc) {
                    constexpr int f = m * PER + decltype(kc)::value;
                    if constexpr (f < NF) rd(std::integral_constant<int, u + 1>{}, std::integral_constant<int, f>{});
                });
                SBAR();
            }
        });
    });

    if constexpr (KSP) {
        auto xbuf = [&](int w) { return lds0 + (w < 2 ? AT_Q : LDS_X) + (w & 1) * 4096 + lane * 16; };
#pragma unroll
        for (int g = 0; g < 4; ++g) *(LAS f32x4*)(uintptr_t)(xbuf(cw) + g * 1024) = f32x4{acc[1][0][4 * g], acc[1][0][4 * g + 1], acc[1][0][4 * g + 2], acc[1][0][4 * g + 3]};
        lds_barrier();
#pragma unroll
        for (int g = 0; g < 4; ++g) {
            const f32x4 o = *(LAS f32x4*)(uintptr_t)(xbuf(cw ^ 2) + g * 1024);
#pragma unroll
            for (int j = 0; j < 4; ++j) acc[0][0][4 * g + j] += o[j];
        }
    }
    STC(7); ST(3);
    if constexpr (PH == PH_QKV) {
        const int tile = rb * MT + wm;
        const int head0 = cb * 4 + wn * 2;
        const float rq = rstd[0] * QSCALE, rk = rstd[0];
#pragma unroll
        for (int e = 0; e < 2; ++e) {
            u32x4 q, k;
#pragma unroll
            for (int j = 0; j < 4; ++j) {
                q[j] = pack_bf16(rq * acc[0][0][8 * e + 2 * j], rq * acc[0][0][8 * e + 2 * j + 1]);
                k[j] = pack_bf16(rk * acc[0][1][8 * e + 2 * j], rk * acc[0][1][8 * e + 2 * j + 1]);
            }
            st16(rws, lane * 16, lw + WS_KV + ((head0 + e) * 128 + tile) * 1024, k);
            st16(rws, lane * 16, lw + WS_Q + ((head0 + e) * 64 + tile) * 1024, q);
        }
#pragma unroll
        for (int e = 0; e < 2; ++e) {
            u32x4 o;
#pragma unroll
            for (int j = 0; j < 4; ++j) {
                const f32x4 st = *(LAS f32x4*)&rowf[cw * 32 + 16 * e + 8 * (j >> 1) + 4 * hh + 2 * (j & 1)];
                o[j] = pack_bf16(st[1] * acc[0][2][8 * e + 2 * j], st[3] * acc[0][2][8 * e + 2 * j + 1]);
            }
            st16(rws, (r32 >> 4) * 131072 + e * 512 + hh * 256 + (r32 & 15) * 16, lw + WS_KV + (head0 * 128 + 64 + tile) * 1024, o);
        }
        lds_barrier();
        ST(4);
        poller = publish(ws, lw + WS_FL + PH_ATT * FL_PH + cb * 32 * FR, 32, 1, rb, tag, lds0 + LDS_CNT, lane);
    } else if constexpr (PH == PH_GLU) {
#pragma unroll
        for (int i = 0; i < TM; ++i) {
            const float r2 = rstd[i] * rstd[i];
#pragma unroll
            for (int e = 0; e < 2; ++e) {
                u32x4 o;
#pragma unroll
                for (int j = 0; j < 4; ++j) {
                    const int r = 8 * e + 2 * j;
                    o[j] = pack_bf16(fmaxf(r2 * acc[i][0][r], 0.f) * acc[i][1][r], fmaxf(r2 * acc[i][0][r + 1], 0.f) * acc[i][1][r + 1]);
                }
                st16(rws, lane * 16 + e * 1024, lw + WS_H + (rb * MT + i) * (64 * FF) + (cb * 16 + wn * 4) * 512, o);
            }
        }
        lds_barrier();
        ST(4);
        poller = publish(ws, lw + WS_FL + PH_DN * FL_PH + rb * 16 * FR, 16, 1, cb, tag, lds0 + LDS_CNT, lane);
    } else {
        const bool last = PH == PH_DN && layer == 1;
        const unsigned rbo = (rb * MT + wm) * 32768;
        const unsigned vo = last ? r32 * 1024 + hh * 16 + rbo + (cb * 8 + wn * 4) * 16 : lane * 16 + (PH == PH_WO ? lw + WS_XB : WS_LAYER + WS_XA) + rbo + (cb * 8 + wn * 4) * 512;
        const rsrc_t ro = last ? rio : rws;
#pragma unroll
        for (int e = 0; e < 2; ++e) {
            u32x4 o;
#pragma unroll
            for (int j = 0; j < 4; ++j) o[j] = pack_bf16(acc[0][0][8 * e + 2 * j], acc[0][0][8 * e + 2 * j + 1]);
            st16(ro, vo, e * (last ? 32 : 1024), o);
        }
        if (!last) {
            float sum[2] = {0.f, 0.f}, sq[2] = {0.f, 0.f};
#pragma unroll
            for (int r = 0; r < 16; ++r) { sum[r & 1] += acc[0][0][r]; sq[r & 1] = fmaf(acc[0][0][r], acc[0][0][r], sq[r & 1]); }
            const float a = half_sum(sum[0] + sum[1]), b = half_sum(sq[0] + sq[1]);
            if (hh == 0) __builtin_amdgcn_raw_buffer_store_b64(u32x2{__float_as_uint(a), __float_as_uint(b)}, rws,
                                                               r32 * 16 + (PH == PH_WO ? lw + WS_STB : WS_LAYER + WS_STA) + (rb * MT + wm) * 4096 + cb * 512 + wn * 8, 0, 16);
        }
        if constexpr (!KSP) lds_barrier();
        ST(4);
        if (!last) {
            if constexpr (PH == PH_WO) poller = publish(ws, lw + WS_FL + PH_GLU * FL_PH + (rb >> 1) * 14 * FR, 14, 1, (rb & 1) * 8 + cb, tag, lds0 + LDS_CNT, lane);
            else poller = publish(ws, WS_LAYER + WS_FL + PH_QKV * FL_PH + rb * 8 * FR, 8, 1, cb, tag, lds0 + LDS_CNT, lane);
        }
    }
    ST(5); ST_OUT(layer * 5 + PH, 1);
}

#define AT_E(C, i) "v_exp_f32 v[" #C "+" #i "], v[" #C "+" #i "]\n\t"
#define AT_C(P, C, i) "v_cvt_pk_bf16_f32 v[" #P "], v[" #C "+" #i "], v[" #C "+" #i "+1]\n\t"
#define AT_HALF(C, O, V0, PRE, MID, POST)                                                                         \
    AT_E(C, 0) AT_E(C, 1) PRE AT_E(C, 2) AT_E(C, 3) AT_E(C, 4) AT_E(C, 5) MID                                     \
    AT_E(C, 6) AT_E(C, 7) AT_C(108, C, 0) AT_C(109, C, 2) AT_C(110, C, 4) AT_E(C, 8) AT_C(111, C, 6) AT_E(C, 9) AT_E(C, 10) \
    "v_mfma_f32_32x32x16_bf16 " O ", v[" #V0 ":" #V0 "+3], v[108:111], " O "\n\t" POST                            \
    AT_E(C, 11) AT_E(C, 12) AT_E(C, 13) AT_E(C, 14) AT_E(C, 15)                                                   \
    AT_C(112, C, 8) AT_C(113, C, 10) AT_C(114, C, 12) AT_C(115, C, 14)
#define AT_TILE(U, V0C, V0N, SYNC)                                                                                \
    AT_HALF(64, "%[o0]", V0C,                                                                                     \
            "v_mfma_f32_32x32x16_bf16 %[o1], v[104:107], v[112:115], %[o1]\n\t"                                   \
            "ds_read_b128 v[104:107], %[va] offset:" #U "*1024+512\n\t",                                          \
            "v_mfma_f32_32x32x16_bf16 v[80:95], v[96:99], %[q1], %[nm1]\n\t" SYNC                                 \
            "ds_read_b128 v[96:99], %[ka] offset:" #U "*1024+1024\n\t"                                            \
            "ds_read_b128 v[" #V0N ":" #V0N "+3], %[va] offset:" #U "*1024+1024\n\t",                             \
            "s_waitcnt lgkmcnt(2)\n\t")                                                                           \
    AT_HALF(80, "%[o1]", V0C,                                                                                     \
            "v_mfma_f32_32x32x16_bf16 %[o0], v[104:107], v[112:115], %[o0]\n\t"                                   \
            "s_waitcnt lgkmcnt(0)\n\t",                                                                           \
            "v_mfma_f32_32x32x16_bf16 v[64:79], v[96:99], %[q0], %[nm0]\n\t", "")

DI f32x16 neg_tile_max(const bf16x8& k0, const bf16x8& q) {
    const f32x16 s0 = MFMA32(k0, q, f32x16{});
    float m = s0[0];
#pragma unroll
    for (int r = 1; r < 16; ++r) m = fmaxf(m, s0[r]);
    m = half_max(m);
    f32x16 n;
#pragma unroll
    for (int r = 0; r < 16; ++r) n[r] = -m;
    return n;
}
DI f32x16 attn_exact(const bf16x8& q, rsrc_t rws, unsigned kv, unsigned ones, int lane) {
    const bool vlane = (lane & 31) < 16;
    const bf16x8 pad = lds_b128(ones);
    float m = -INFINITY;
    f32x16 o = f32x16{};
#pragma unroll 1
    for (int t = 0; t < 64; ++t) {
        const bf16x8 kf = __builtin_bit_cast(bf16x8, ld16(rws, lane * 16, kv + t * 1024));
        const unsigned vo = (lane >> 5) * 256 + (lane & 15) * 16;
        bf16x8 v0 = __builtin_bit_cast(bf16x8, ld16(rws, vo, kv + 65536 + t * 1024)), v1 = __builtin_bit_cast(bf16x8, ld16(rws, vo + 512, kv + 65536 + t * 1024));
        if (!vlane) { v0 = pad; v1 = pad; }
        const f32x16 s = MFMA32(kf, q, f32x16{});
        float tm = s[0];
#pragma unroll
        for (int r = 1; r < 16; ++r) tm = fmaxf(tm, s[r]);
        const float mn = fmaxf(m, half_max(tm));
        const float alpha = __builtin_amdgcn_exp2f(m - mn);
        m = mn;
#pragma unroll
        for (int r = 0; r < 16; ++r) o[r] *= alpha;
        u32x4 p0, p1;
#pragma unroll
        for (int j = 0; j < 4; ++j) {
            p0[j] = pack_bf16(__builtin_amdgcn_exp2f(s[2 * j] - m), __builtin_amdgcn_exp2f(s[2 * j + 1] - m));
            p1[j] = pack_bf16(__builtin_amdgcn_exp2f(s[8 + 2 * j] - m), __builtin_amdgcn_exp2f(s[8 + 2 * j + 1] - m));
        }
        o = MFMA32(v0, __builtin_bit_cast(bf16x8, p0), o);
        o = MFMA32(v1, __builtin_bit_cast(bf16x8, p1), o);
    }
    return o;
}

DI void attn_load(const MkArgs& p, rsrc_t rws, int layer, unsigned lds0, int wid, int lane, u64 tag, bf16x8 (&wd)[40]) {
    ST_DECL ST(0);
    char* ws = p.ws;
    FRESH(ws, lds0, wid, lane, bid);
    const int x = bid & 7, idx = bid >> 3, head = x * 4 + (idx >> 3), qb = idx & 7;
    const unsigned lw = layer * WS_LAYER;
    const unsigned vo = lane * 16;
    const char* src = ws + lw + WS_KV + (size_t)head * 131072 + wid * 2048;
    auto chunk = [&](int c) {
        const char* s = src + ((qb + c) & 7) * 8192;
        glds_run<2>(s, vo, lds0 + AT_K + c * 8192 + wid * 2048);
        glds_run<2>(s + 65536, vo, lds0 + AT_V + c * 8192 + wid * 2048);
    };
    ST(1);
    asm volatile("s_barrier" ::: "memory");
    ST(2);
    glds_run<2>(ws + lw + WS_Q + (size_t)(head * 64 + qb * 8 + wid * 2) * 1024, vo, lds0 + AT_Q + wid * 2048);
    chunk(0);
    wait_vm_barrier<0>();
    ST(3);
    wait_tags(ws, lw + WS_FL + PH_ATT * FL_PH + (head * 8 + qb) * FR, 32, tag, lane);
    chunk(1);
    chunk(2); chunk(3); chunk(4); chunk(5); chunk(6); chunk(7);
    wait_vm_barrier<0>();
    ST(4);
    if (layer == 0) {
#pragma unroll 1
        for (int u = bid * 4 + wid; u < 1536; u += 1024) {
            const char* s; size_t dst; int ld2;
            if (u < 896) { const int mat = u >= 448, v = u - mat * 448, pan = v >> 4, kb = v & 15;
                s = (mat ? p.Wu : p.Wg) + (size_t)kb * 32 * (FF * 2) + pan * 128; ld2 = FF * 2; dst = Geo<PH_GLU>::packed_off(pan >> 1, mat * 2 + (pan & 1), kb); }
            else if (u < 1024) { const int v = u - 896, pan = v & 7, kb = v >> 3;
                s = p.Wo + (size_t)kb * 32 * 1024 + pan * 128; ld2 = 1024; dst = Geo<PH_WO>::packed_off(pan, 0, kb); }
            else if (u < 1152) { const int v = u - 1024, pan = v & 7, kb = v >> 3;
                s = p.Wd + (size_t)kb * 32 * 1024 + pan * 128; ld2 = 1024; dst = Geo<PH_DN>::packed_off(pan, 0, kb); }
            else { const int v = u - 1152, pan = v >> 4, kb = v & 15;
                s = p.Wqkv + (size_t)kb * 32 * 3072 + pan * 128; ld2 = 3072; dst = Geo<PH_QKV>::packed_off(pan & 7, pan >> 3, kb); }
            u32x4 v[4];
            load_unit(v, s, ld2, lane);
            store_unit_packed(v, rws, (unsigned)dst, lane);
            if (u < 896) unit_colsum(v, lds0 + LDS_ROWG + wid * 256, lane);
        }
        if (arrive(lds0 + LDS_CNT + 4, lane) == 3) {
            if (bid < 224) { store_colsum(lds0 + LDS_ROWG, rws, WS_CSG + bid * 256, lane); asm volatile("s_waitcnt vmcnt(0)" ::: "memory"); }
            if (lane == 0) __hip_atomic_store((GAS u64*)(ws + WS_FL + FL_PK + bid * 8), tag, __ATOMIC_RELAXED, __HIP_MEMORY_SCOPE_AGENT);
            __builtin_amdgcn_s_sleep(24);
#pragma unroll 1
            for (int i = 0; i < 4; ++i) wait_tags(ws, WS_FL + FL_PK + ((bid + i) & 3) * 512, 64, tag, lane);
        }
    }
    lds_barrier();
    const unsigned own = lds0 + Geo<PH_WO>::b_off(0) + (wid >> 1) * 65536 + (wid & 1) * 16384;
    if (layer == 0) {
        int rb, cb; Geo<PH_DN>::tile(bid, rb, cb);
        const char* sw = p.Wd + cb * 128 + (wid & 1) * 64 + (wid >> 1) * 32768;
        const unsigned vw = (lane >> 2) * 1024 + (lane & 3) * 16;
        const int pp = lane & 3;
        const unsigned ra = own + (8 * (lane >> 5) + ((lane & 15) >> 2)) * 64 + ((lane >> 4) & 1) * 32 + ((((pp & 1) << 1) | (pp >> 1)) << 3);
        typedef short v4i16_t __attribute__((ext_vector_type(4)));
#pragma unroll
        for (int r0 = 0; r0 < 40; r0 += 16) {
#pragma unroll
            for (int i = r0; i < r0 + 16 && i < 40; ++i) glds_in(sw + (size_t)((8 + (i >> 1)) * 64 + (i & 1) * 16) * 1024, vw, own + (i - r0) * 1024);
            asm volatile("s_waitcnt vmcnt(0)" ::: "memory");
#pragma unroll
            for (int i = r0; i < r0 + 16 && i < 40; ++i) {
                const v4i16_t lo = __builtin_amdgcn_ds_read_tr16_b64_v4i16((LAS v4i16_t*)(uintptr_t)(ra + (i - r0) * 1024));
                const v4i16_t hi = __builtin_amdgcn_ds_read_tr16_b64_v4i16((LAS v4i16_t*)(uintptr_t)(ra + (i - r0) * 1024 + 256));
                wd[i] = __builtin_shufflevector(lo, hi, 0, 1, 2, 3, 4, 5, 6, 7);
            }
#pragma unroll
            for (int i = r0; i < r0 + 16 && i < 40; ++i) asm volatile("" : "+v"(wd[i])::"memory");
        }
    }
    {
        int rb, cb; Geo<PH_WO>::tile(bid, rb, cb);
        static_assert(Geo<PH_WO>::b_off(2) == 16384 && Geo<PH_WO>::b_off(4) == 65536 && Geo<PH_WO>::b_off(6) == 81920, "");
#pragma unroll
        for (int i = 0; i < 4; ++i) glds_run<4>(ws + WP_O + cb * 65536 + wid * 16384 + i * 4096, vo, own + i * 4096);
    }
    lds_barrier();
    ST(5); ST_OUT(layer * 5 + PH_ATT, 0);
}

DI void attn_comp(char* ws, rsrc_t rws, int layer, unsigned lds0, int cw, int lane, u64 tag, bool& poller) {
    ST_DECL ST(0);
    FRESH(ws, lds0, cw, lane, bid);
    const int x = bid & 7, idx = bid >> 3, head = x * 4 + (idx >> 3), qb = idx & 7;
    const unsigned lw = layer * WS_LAYER;
    dep_barrier(poller, ws, lw + WS_FL + PH_ATT * FL_PH + (head * 8 + qb) * FR + qb * 4 * TS, 4, tag, lane);
    const int r32 = lane & 31, hh = lane >> 5;
    const unsigned ka0 = lds0 + AT_K + lane * 16;
    const bool vlane = r32 < 16;
    const unsigned va0 = lds0 + (vlane ? AT_V : AT_ONES) + hh * 256 + (r32 & 15) * 16;
    const unsigned vstep = vlane ? 8192u : 0u;

    ST(1);
    lds_barrier();
    ST(2);
    const bf16x8 q0 = lds_b128(lds0 + AT_Q + cw * 2048 + lane * 16), q1 = lds_b128(lds0 + AT_Q + cw * 2048 + 1024 + lane * 16);
    const bf16x8 k0 = lds_b128(ka0);
    const f32x16 nm0 = neg_tile_max(k0, q0), nm1 = neg_tile_max(k0, q1);
    f32x16 o0 = f32x16{}, o1 = f32x16{};
    STC(6);
    {
        unsigned ka = ka0, va = va0; int cnt, tmp;
        asm volatile(
            "ds_read_b128 v[96:99], %[ka]\n\t"
            "ds_read_b128 v[100:103], %[va]\n\t"
            "v_mov_b32 v104, 0\n\tv_mov_b32 v105, 0\n\tv_mov_b32 v106, 0\n\tv_mov_b32 v107, 0\n\t"
            "v_mov_b32 v112, 0\n\tv_mov_b32 v113, 0\n\tv_mov_b32 v114, 0\n\tv_mov_b32 v115, 0\n\t"
            "s_mov_b32 %[cnt], 0\n\t"
            "s_waitcnt lgkmcnt(0)\n\t"
            "v_mfma_f32_32x32x16_bf16 v[64:79], v[96:99], %[q0], %[nm0]\n\t"
            "s_nop 12\n\t"
            "1:\n\t"
            AT_TILE(0, 100, 116, "") AT_TILE(1, 116, 100, "") AT_TILE(2, 100, 116, "") AT_TILE(3, 116, 100, "")
            AT_TILE(4, 100, 116, "") AT_TILE(5, 116, 100, "") AT_TILE(6, 100, 116, "")
            AT_TILE(7, 116, 100, "s_and_b32 %[tmp], %[cnt], 3\n\ts_cbranch_scc1 2f\n\ts_barrier\n\t2:\n\t")
            "v_add_u32 %[ka], 0x2000, %[ka]\n\t"
            "v_add_u32 %[va], %[vstep], %[va]\n\t"
            "s_add_u32 %[cnt], %[cnt], 1\n\t"
            "s_cmp_lt_u32 %[cnt], 8\n\t"
            "s_cbranch_scc1 1b\n\t"
            "v_mfma_f32_32x32x16_bf16 %[o1], v[104:107], v[112:115], %[o1]\n\t"
            "s_nop 15\n\t"
            : [o0] "+v"(o0), [o1] "+v"(o1), [ka] "+v"(ka), [va] "+v"(va), [cnt] "=&s"(cnt), [tmp] "=&s"(tmp)
            : [q0] "v"(q0), [q1] "v"(q1), [nm0] "v"(nm0), [nm1] "v"(nm1), [vstep] "v"(vstep)
            : "memory", "scc", "v64", "v65", "v66", "v67", "v68", "v69", "v70", "v71", "v72", "v73", "v74", "v75", "v76", "v77", "v78", "v79",
              "v80", "v81", "v82", "v83", "v84", "v85", "v86", "v87", "v88", "v89", "v90", "v91", "v92", "v93", "v94", "v95",
              "v96", "v97", "v98", "v99", "v100", "v101", "v102", "v103", "v104", "v105", "v106", "v107",
              "v108", "v109", "v110", "v111", "v112", "v113", "v114", "v115", "v116", "v117", "v118", "v119");
    }
    STC(7); ST(3);
    if (__builtin_expect(__any(!(o0[8] < LMAX) || !(o1[8] < LMAX)), 0)) {
        o0 = attn_exact(q0, rws, lw + WS_KV + head * 131072, lds0 + AT_ONES + (r32 & 15) * 16, lane);
        o1 = attn_exact(q1, rws, lw + WS_KV + head * 131072, lds0 + AT_ONES + (r32 & 15) * 16, lane);
    }
    const float i0 = 1.0f / o0[8], i1 = 1.0f / o1[8];
    u32x4 w0, w1;
#pragma unroll
    for (int j = 0; j < 4; ++j) { w0[j] = pack_bf16(o0[2 * j] * i0, o0[2 * j + 1] * i0); w1[j] = pack_bf16(o1[2 * j] * i1, o1[2 * j + 1] * i1); }
    const int qtile = qb * 8 + cw * 2;
    st16(rws, lane * 16, lw + WS_O + qtile * 32768 + head * 1024, w0);
    st16(rws, lane * 16, lw + WS_O + (qtile + 1) * 32768 + head * 1024, w1);
    lds_barrier();
    ST(4);
    poller = publish(ws, lw + WS_FL + PH_WO * FL_PH + qb * 32 * FR, 32, 1, head, tag, lds0 + LDS_CNT, lane);
    ST(5); ST_OUT(layer * 5 + PH_ATT, 1);
}

__global__ __launch_bounds__(512, 2) void mk_kernel(MkArgs p) {
    extern __shared__ __attribute__((aligned(16))) char smem[];
    const unsigned lds0 = (unsigned)(uintptr_t)(LAS char*)smem;
    const int tid = threadIdx.x, lane = tid & 63, wid = __builtin_amdgcn_readfirstlane(tid >> 6);
    const rsrc_t rws = make_rsrc(p.ws, WS_END);
    const bool glu = (blockIdx.x >> 3) < 28;
    u64 epoch;
    asm volatile("s_load_dwordx2 %0, %1, 0x0" : "=s"(epoch) : "s"(p.ws + WS_FL + FL_EPOCH) : "memory");
    if (wid < 4) {
        first_load(p.x, p.Wqkv, lds0, wid, lane);
        asm volatile("s_waitcnt lgkmcnt(0)" : "+s"(epoch));
        const u64 tag = epoch + 1;
        const rsrc_t rout = make_rsrc(p.out, SEQ * DM * 2);
        bf16x8 wd[40];
        bool poller = false;
#pragma unroll 1
        for (int layer = 0; layer < 2; ++layer) {
            if (layer) gemm_load<PH_QKV>(p.ws, layer, lds0, wid, lane, false, tag, poller);
            attn_load(p, rws, layer, lds0, wid, lane, tag, wd);
            gemm_load<PH_WO>(p.ws, layer, lds0, wid, lane, !glu, tag, false);
            if (glu) gemm_load<PH_GLU>(p.ws, layer, lds0, wid, lane, false, tag, false);
            else prefetch_b<PH_DN>(p.ws, lds0, wid, lane);
            gemm_comp<PH_DN>(p.ws, rws, rout, layer, lds0, wid, lane, tag, poller, wd);
        }
    } else {
        const rsrc_t rx = make_rsrc(p.x, SEQ * DM * 2);
        bf16x8 none[40];
        __builtin_amdgcn_s_setprio(3);
        u32x4 sv[4];
        startup_load(p, wid - 4, lane, sv);
        {
            const unsigned ov = ((tid & 15) == 0 || (tid & 15) == 4) ? 0x3f803f80u : 0u;
            *(LAS u32x4*)(uintptr_t)(lds0 + AT_ONES + (tid - 256) * 16) = u32x4{ov, ov, ov, ov};
            *(LAS u32x4*)(uintptr_t)(lds0 + AT_ONES + tid * 16) = u32x4{ov, ov, ov, ov};
            if (tid < 320) *(LAS u32x4*)(uintptr_t)(lds0 + AT_ONES + (tid + 256) * 16) = u32x4{ov, ov, ov, ov};
            if (tid == 256) *(LAS u32x2*)(uintptr_t)(lds0 + LDS_CNT) = u32x2{0, 0};
        }
        lds_barrier();
        asm volatile("" : "+v"(sv[0]), "+v"(sv[1]), "+v"(sv[2]), "+v"(sv[3]));
        bool poller = startup_job(rws, lds0, wid - 4, lane, sv);
        asm volatile("s_waitcnt lgkmcnt(0)" : "+s"(epoch));
        const u64 tag = epoch + 1;
        __builtin_amdgcn_s_setprio(0);
#pragma unroll 1
        for (int layer = 0; layer < 2; ++layer) {
            gemm_comp<PH_QKV>(p.ws, rws, rx, layer, lds0, wid - 4, lane, tag, poller, none);
            attn_comp(p.ws, rws, layer, lds0, wid - 4, lane, tag, poller);
            gemm_comp<PH_WO>(p.ws, rws, rx, layer, lds0, wid - 4, lane, tag, poller, none);
            if (glu) gemm_comp<PH_GLU>(p.ws, rws, rx, layer, lds0, wid - 4, lane, tag, poller, none);
            gemm_load<PH_DN>(p.ws, layer, lds0, wid - 4, lane, layer == 1, tag, poller);
        }
        if (blockIdx.x == 0 && tid == 256) __hip_atomic_store((GAS u64*)(p.ws + WS_FL + FL_EPOCH), tag, __ATOMIC_RELAXED, __HIP_MEMORY_SCOPE_AGENT);
    }
}

extern "C" void kernel_launch(void* const* d_in, const int* in_sizes, int n_in, void* d_out, int out_size, void* d_ws, size_t ws_size,
                              hipStream_t stream) {
    (void)hipFuncSetAttribute((const void*)mk_kernel, hipFuncAttributeMaxDynamicSharedMemorySize, LDS_BYTES);
    mk_kernel<<<256, 512, LDS_BYTES, stream>>>(MkArgs{(const char*)d_in[0], (const char*)d_in[1], (const char*)d_in[2], (const char*)d_in[4], (const char*)d_in[5],
                                                      (const char*)d_in[6], (char*)d_ws, (char*)d_out});
}
```

```cpp
#include <hip/hip_runtime.h>
#include <hip/hip_bf16.h>
#include <stdint.h>
#include <utility>

typedef short bf16x8 __attribute__((ext_vector_type(8)));
typedef float f32x16 __attribute__((ext_vector_type(16)));
typedef float f32x4 __attribute__((ext_vector_type(4)));
typedef float f32x2 __attribute__((ext_vector_type(2)));
typedef unsigned u32x4 __attribute__((ext_vector_type(4)));
typedef unsigned u32x2 __attribute__((ext_vector_type(2)));
typedef __bf16 bf16x2_t __attribute__((ext_vector_type(2)));
typedef __amdgpu_buffer_rsrc_t rsrc_t;
#define LAS __attribute__((address_space(3)))
#define GAS __attribute__((address_space(1)))
#define DI __device__ __forceinline__
#define MFMA32(a, b, c) __builtin_amdgcn_mfma_f32_32x32x16_bf16((a), (b), (c), 0, 0, 0)
#define SBAR() __builtin_amdgcn_sched_barrier(0)

constexpr int SEQ = 2048, DM = 512, FF = 1792;
constexpr float LN_EPS = 1e-5f;
constexpr float QSCALE = 0.25f * 1.4426950408889634f;

enum { PH_QKV = 0, PH_ATT = 1, PH_WO = 2, PH_GLU = 3, PH_DN = 4 };
#define LMAX 1.0e18f
constexpr unsigned MB = 1u << 20;
constexpr unsigned WS_LAYER = 20 * MB;
constexpr unsigned WS_Q = 0, WS_KV = 2 * MB, WS_O = 6 * MB, WS_XA = 8 * MB, WS_XB = 10 * MB, WS_H = 12 * MB, WS_STA = 19 * MB, WS_STB = WS_STA + MB / 4, WS_FL = WS_STB + MB / 4;
constexpr unsigned TS = 8, FR = 32 * TS;
constexpr unsigned FL_PH = 256 * FR, FL_PK = 5 * FL_PH, FL_EPOCH = FL_PK + 2048;
constexpr unsigned WS_WP = 2 * WS_LAYER;
constexpr unsigned WP_QKV = WS_WP, WP_O = WP_QKV + DM * 3 * DM * 2, WP_GU = WP_O + DM * DM * 2, WP_D = WP_GU + DM * FF * 4;
constexpr unsigned WS_CSQ = WP_D + FF * DM * 2, WS_CSG = WS_CSQ + 3 * DM * 16, WS_END = WS_CSG + 2 * FF * 16;
typedef unsigned long long u64;

template <int... I, class Fn> DI void static_for_impl(std::integer_sequence<int, I...>, Fn&& f) { (f(std::integral_constant<int, I>{}), ...); }
template <int N, class Fn> DI void static_for(Fn&& f) { static_for_impl(std::make_integer_sequence<int, N>{}, f); }

DI unsigned pack_bf16(float lo, float hi) { bf16x2_t p; p.x = (__bf16)lo; p.y = (__bf16)hi; return __builtin_bit_cast(unsigned, p); }
DI float bf_lo(unsigned u) { return __uint_as_float(u << 16); }
DI float bf_hi(unsigned u) { return __uint_as_float(u & 0xffff0000u); }
template <int CTRL> DI float dpp(float v) { return __uint_as_float(__builtin_amdgcn_update_dpp(0u, __float_as_uint(v), CTRL, 0xf, 0xf, true)); }
DI float half_sum(float v) {
    auto r = __builtin_amdgcn_permlane32_swap(__float_as_uint(v), __float_as_uint(v), false, false);
    return __uint_as_float(r[0]) + __uint_as_float(r[1]);
}
DI float half_max(float v) {
    auto r = __builtin_amdgcn_permlane32_swap(__float_as_uint(v), __float_as_uint(v), false, false);
    return fmaxf(__uint_as_float(r[0]), __uint_as_float(r[1]));
}
DI void st16(rsrc_t r, unsigned voff, unsigned soff, u32x4 v) { __builtin_amdgcn_raw_buffer_store_b128(v, r, voff + soff, 0, 16); }
DI u32x4 ld16(rsrc_t r, unsigned voff, unsigned soff) { return __builtin_amdgcn_raw_buffer_load_b128(r, voff, soff, 16); }
template <int N> DI void wait_vm_barrier() { asm volatile("s_waitcnt vmcnt(%0) lgkmcnt(0)\n\ts_barrier" ::"i"(N) : "memory"); }
DI void lds_barrier() { asm volatile("s_waitcnt lgkmcnt(0)\n\ts_barrier" ::: "memory"); }
DI bf16x8 lds_b128(unsigned addr) { return *(const LAS bf16x8*)(uintptr_t)addr; }
DI bf16x8 lds_tr_frag(unsigned addr) {
    typedef short v4i16_t __attribute__((ext_vector_type(4)));
    v4i16_t lo = __builtin_amdgcn_ds_read_tr16_b64_v4i16((LAS v4i16_t*)(uintptr_t)addr);
    v4i16_t hi = __builtin_amdgcn_ds_read_tr16_b64_v4i16((LAS v4i16_t*)(uintptr_t)(addr + 512));
    return __builtin_shufflevector(lo, hi, 0, 1, 2, 3, 4, 5, 6, 7);
}
template <int N> DI void glds_run(const char* sbase, unsigned voff, unsigned lds_dst) {
    static_assert(N >= 1 && N <= 4, "");
#define GL(o) "\n\tglobal_load_lds_dwordx4 %0, %1 offset:" #o " sc1"
    if constexpr (N == 1) asm volatile("s_mov_b32 m0, %2\n\ts_nop 3" GL(0)::"v"(voff), "s"(sbase), "s"(lds_dst) : "memory");
    if constexpr (N == 2) asm volatile("s_mov_b32 m0, %2\n\ts_nop 3" GL(0) GL(1024)::"v"(voff), "s"(sbase), "s"(lds_dst) : "memory");
    if constexpr (N == 3) asm volatile("s_mov_b32 m0, %2\n\ts_nop 3" GL(0) GL(1024) GL(2048)::"v"(voff), "s"(sbase), "s"(lds_dst) : "memory");
    if constexpr (N == 4) asm volatile("s_mov_b32 m0, %2\n\ts_nop 3" GL(0) GL(1024) GL(2048) GL(3072)::"v"(voff), "s"(sbase), "s"(lds_dst) : "memory");
#undef GL
}
DI void glds_in(const char* sbase, unsigned voff, unsigned lds_dst) {
    asm volatile("s_mov_b32 m0, %2\n\ts_nop 3\n\tglobal_load_lds_dwordx4 %0, %1" ::"v"(voff), "s"(sbase), "s"(lds_dst) : "memory");
}
DI int arrive(unsigned cnt_addr, int lane) {
    asm volatile("s_waitcnt vmcnt(0) lgkmcnt(0)" ::: "memory");
    unsigned old = 0;
    if (lane == 0) old = __hip_atomic_fetch_add((LAS unsigned*)(uintptr_t)cnt_addr, 1u, __ATOMIC_RELAXED, __HIP_MEMORY_SCOPE_WORKGROUP);
    asm volatile("" ::: "memory");
    return __builtin_amdgcn_readfirstlane(old) & 3;
}
DI void signal(char* ws, unsigned first_line_off, int n, int lstride, int me, u64 tag, int lane) {
    if (lane < n) __hip_atomic_store((GAS u64*)(ws + first_line_off + lane * lstride * FR + me * TS), tag, __ATOMIC_RELAXED, __HIP_MEMORY_SCOPE_AGENT);
}
DI bool publish(char* ws, unsigned first_line_off, int n, int lstride, int me, u64 tag, unsigned cnt_addr, int lane) {
    const int rank = arrive(cnt_addr, lane);
    if (rank == 3) signal(ws, first_line_off, n, lstride, me, tag, lane);
    return rank == 3;
}
DI void wait_tags(const char* ws, unsigned off, int n, u64 tag, int lane) {
    const GAS u64* f = (const GAS u64*)(ws + off + (lane < n ? lane : 0) * TS);
#pragma unroll 1
    for (int spin = 0; spin < (1 << 16); ++spin) {
        const u64 v = __hip_atomic_load(f, __ATOMIC_RELAXED, __HIP_MEMORY_SCOPE_AGENT);
        if (__all(v == tag)) break;
        __builtin_amdgcn_s_sleep(1);
    }
    asm volatile("s_waitcnt vmcnt(0)" ::: "memory");
}
DI void dep_barrier(bool poller, const char* ws, unsigned off, int n, u64 tag, int lane) {
    if (__builtin_amdgcn_readfirstlane((int)poller)) wait_tags(ws, off, n, tag, lane);
    lds_barrier();
}

#define ST_DECL
#define ST(i)
#define STC(i)
#define ST_OUT(kind, role)

template <int PH> struct Cfg;
template <> struct Cfg<PH_QKV> { static constexpr int KTOT = 512, MT = 2, NP = 3, WM = 2, WN = 2, WK = 1, BK = 32, NS = 8, PA = 8, KG = 2, NCB = 8, XC = 2; };
template <> struct Cfg<PH_WO> { static constexpr int KTOT = 512, MT = 2, NP = 1, WM = 2, WN = 2, WK = 1, BK = 64, NS = 8, PA = 4, KG = 4, NCB = 8, XC = 2; };
template <> struct Cfg<PH_GLU> { static constexpr int KTOT = 512, MT = 4, NP = 4, WM = 1, WN = 4, WK = 1, BK = 32, NS = 5, PA = 3, KG = 1, NCB = 14, XC = 2; };
template <> struct Cfg<PH_DN> { static constexpr int KTOT = FF, MT = 2, NP = 1, WM = 1, WN = 2, WK = 2, BK = 64, NS = 8, PA = 4, KG = 2, NCB = 8, XC = 2; };

constexpr int AT_K = 0, AT_V = 65536, AT_ONES = 131072, AT_Q = AT_ONES + 9216, LDS_ROWQ = AT_Q + 8192, LDS_ROWG = LDS_ROWQ + 1024, LDS_CNT = LDS_ROWG + 1024, LDS_X = LDS_CNT + 16, LDS_BYTES = LDS_X + 8192;

template <int PH> struct Geo : Cfg<PH> {
    using C = Cfg<PH>;
    static constexpr int TM = C::MT / C::WM, TN = C::NP * 2 / C::WN, NT = C::KTOT / C::BK, KS = C::BK / 16, UPS = KS / C::WK / C::KG, NU = NT * UPS;
    static constexpr int ABYTES = C::MT * C::BK * 64, PBYTES = C::BK * 128, BBYTES = C::NP * PBYTES, SBYTES = ABYTES + BBYTES;
    static constexpr int GA = ABYTES / 4096, GB = BBYTES / 4096, G = GA + GB;
    static constexpr bool LN = PH == PH_QKV || PH == PH_GLU;
    static constexpr int ROWF = PH == PH_QKV ? LDS_ROWQ : LDS_ROWG;
    static constexpr int NRB = SEQ / (32 * C::MT), AUX = PH == PH_GLU ? 4 : 0;
    static constexpr unsigned WP = PH == PH_QKV ? WP_QKV : PH == PH_WO ? WP_O : PH == PH_GLU ? WP_GU : WP_D;
    static constexpr unsigned A = PH == PH_QKV ? WS_XA : PH == PH_WO ? WS_O : PH == PH_GLU ? WS_XB : WS_H;
    static_assert(C::WM * C::WN * C::WK == 4 && GA * 4096 == ABYTES && GB * 4096 == BBYTES && GA <= KS && KS % GA == 0 && GB <= 4, "pieces must divide over the waves");
    static_assert(C::NS * SBYTES <= 131072 && C::NS <= NT, "LDS budget");
    static constexpr int a_off(int k) {
        if (PH == PH_WO) return (k < 4 ? 32768 : 65536) + k * 8192;
        if (PH == PH_DN && k >= 8) return (((k - 8) / 2 + 1) % 8) * SBYTES + ((k - 8) & 1) * ABYTES;
        return ((k + (PH == PH_QKV ? 6 : PH == PH_DN ? 1 : 0)) % C::NS) * SBYTES;
    }
    static constexpr int b_off(int k) { return PH == PH_WO ? (k < 4 ? 0 : 32768) + k * 8192 : a_off(k) + ABYTES; }
    static constexpr bool held(int k) { return PH == PH_DN && k >= 8; }
    static constexpr bool uses(int k, int lo, int hi) { return (lo < a_off(k) + ABYTES && a_off(k) < hi) || (!held(k) && lo < b_off(k) + BBYTES && b_off(k) < hi); }
    static constexpr bool SWAPIN = PH == PH_DN || PH == PH_QKV;
    static DI void tile(int b, int& rb, int& cb) {
        const int x = b & 7, i = b >> 3;
        constexpr int CH = C::NCB / C::XC, RH = NRB * C::XC / 8;
        rb = (x / C::XC) * RH + i / CH; cb = (x % C::XC) * CH + i % CH;
    }
    static DI size_t packed_off(int cb, int pan, int kb) {
        return (size_t)WP + (size_t)cb * (NT * BBYTES) + (size_t)((kb * 32) / C::BK) * BBYTES + pan * PBYTES + ((kb * 32) % C::BK) * 128;
    }
};

DI unsigned tr_lane_off(int lane, int j) {
    const int hh = lane >> 5, blk = (lane >> 4) & 1, q = (lane & 15) >> 2, pp = lane & 3, pi = ((pp & 1) << 1) | (pp >> 1);
    const int ch = (4 * j + 2 * blk + (pi >> 1)) ^ (((q >> 1) & 1) << 2);
    return (8 * hh + q) * 128 + ch * 16 + 8 * (pi & 1);
}
DI int slot_col(int r32) { const int pp = (r32 >> 2) & 3; return (r32 & 16) + 4 * (((pp & 1) << 1) | (pp >> 1)) + (r32 & 3); }
DI bf16x8 corr_frag(float v, bool second_lo, int hh) {
    const unsigned h = pack_bf16(v, 0.f) & 0xffffu, l = pack_bf16(v - bf_lo(h), 0.f) & 0xffffu;
    u32x4 u; u[0] = second_lo ? (h | (l << 16)) : (h | (h << 16)); u[1] = second_lo ? h : l; u[2] = 0; u[3] = 0;
    if (hh) { u[0] = 0; u[1] = 0; }
    return __builtin_bit_cast(bf16x8, u);
}

#define FRESH(ws, lds0, w, lane, b) int b = blockIdx.x; asm volatile("" : "+s"(ws), "+s"(lds0), "+s"(w), "+s"(b)); asm volatile("" : "+v"(lane))

enum { OP_A, OP_F, OP_X, OP_W, OP_N, OP_E, OP_R };
struct Sched { int n; int op[128]; int arg[128]; int pend[128]; int nn[128]; int xw; };
template <int PH, int NX> constexpr Sched make_sched() {
    using G_ = Geo<PH>; using N_ = Geo<NX>;
    Sched sc{}; int cnt = 0, ncnt = 0, lastop[64] = {}, xpos = 0; bool ndone[8] = {};
    int next = 0;
    auto push = [&](int op, int arg) { sc.op[sc.n] = op; sc.arg[sc.n] = arg; sc.pend[sc.n] = 0; ++sc.n; };
    auto item = [&](int s) -> int {
        if (next < G_::NS) { push(OP_A, next); cnt += G_::GA; lastop[next] = cnt; ++next; return G_::GA; }
        if (next == G_::NS) { ++next; if (G_::AUX) { push(OP_X, 0); cnt += G_::AUX; xpos = cnt; return G_::AUX; } }
        const int k = next - 1;
        bool ok = k < G_::NT && k <= s + 8;
        for (int j = s > 0 ? s - 1 : 0; j < k && ok; ++j) ok = !G_::uses(j, G_::a_off(k), G_::a_off(k) + G_::ABYTES);
        if (ok) { const int g = G_::held(k) ? G_::GA : G_::G; push(G_::held(k) ? OP_A : OP_F, k); cnt += g; lastop[k] = cnt; ++next; return g; }
        return 0;
    };
    auto nextb = [&](int s) {
        if (PH == PH_QKV || (PH == PH_DN && s < G_::NT && (s & 1))) return;
        for (int j = 0; j < N_::NS; ++j) {
            if (ndone[j]) continue;
            bool ok = next > G_::NT;
            for (int k = s; k < G_::NT && ok; ++k) ok = !G_::uses(k, N_::b_off(j), N_::b_off(j) + N_::BBYTES);
            if (ok) { push(OP_N, j); cnt += N_::GB; ncnt += N_::GB; ndone[j] = true; if (PH == PH_DN && s < G_::NT) return; }
        }
    };
    for (int k = 0; k < G_::PA; ++k) item(0);
    sc.xw = -1;
    for (int s = 0; s < G_::NT; ++s) {
        push(OP_W, s); sc.pend[sc.n - 1] = cnt - lastop[s]; sc.nn[sc.n - 1] = ncnt;
        if (G_::AUX && sc.xw < 0 && xpos && lastop[s] > xpos) sc.xw = s;
        for (int q = 0; q < G_::G;) { const int r = item(s); if (!r) break; q += r; }
        if (G_::WK > 1 && s == G_::NT - 1) { push(OP_R, 0); nextb(G_::NT); }
        else nextb(s > 0 ? s - 1 : 0);
    }
    push(OP_E, 0);
    nextb(G_::NT);
    return sc;
}
template <int PH> struct NextPh { static constexpr int v = PH == PH_WO ? PH_GLU : PH == PH_GLU ? PH_DN : PH_QKV; };
template <int PH> struct SchedOf { static constexpr Sched v = make_sched<PH, NextPh<PH>::v>(); };

template <int PH> DI void issue_b(const char* ws, int k, int cb, unsigned lds0, int wid, unsigned vo) {
    using G_ = Geo<PH>;
    glds_run<G_::GB>(ws + G_::WP + (size_t)cb * (G_::NT * G_::BBYTES) + k * G_::BBYTES + wid * G_::GB * 1024, vo, lds0 + G_::b_off(k) + wid * G_::GB * 1024);
}
template <int PH> DI void prefetch_b(char* ws, unsigned lds0, int wid, int lane) {
    FRESH(ws, lds0, wid, lane, bid);
    int rb, cb; Geo<PH>::tile(bid, rb, cb);
#pragma unroll
    for (int k = 0; k < Geo<PH>::NS; ++k) issue_b<PH>(ws, k, cb, lds0, wid, lane * 16);
}

DI void first_load(const char* x0, const char* wqkv, unsigned lds0, int wid, int lane) {
    using G_ = Geo<PH_QKV>;
    ST_DECL ST(0); ST(1); ST(2);
    int rb, cb; G_::tile(blockIdx.x, rb, cb);
    const int rot = 2 * ((rb + cb) & 3);
    const char* sA = x0 + (size_t)(rb * 64 + wid * 16) * 1024;
    const unsigned vA0 = (lane >> 3) * 1024 + (((lane & 7) ^ (lane >> 4)) << 4), vA1 = 8192 + (vA0 ^ 64);
    const unsigned vB = (lane >> 3) * 3072 + (((lane & 7) ^ (((lane >> 4) & 1) << 2)) << 4);
    const char* sB[3];
#pragma unroll
    for (int g = 0; g < 3; ++g) { const int b = wid * 3 + g; sB[g] = wqkv + (size_t)(b & 3) * (8 * 3072) + (b >> 2) * 1024 + cb * 128; }
    auto issue = [&](int j) {
        const int jj = (j + rot) & 7;
        const unsigned da = lds0 + G_::a_off(2 * j) + (wid >> 1) * G_::SBYTES + (wid & 1) * 2048;
        glds_in(sA + jj * 128, vA0, da); glds_in(sA + jj * 128, vA1, da + 1024);
#pragma unroll
        for (int h = 0; h < 2; ++h)
#pragma unroll
            for (int g = 0; g < 3; ++g) glds_in(sB[g] + (2 * jj + h) * (32 * 3072), vB, lds0 + G_::b_off(2 * j + h) + (wid * 3 + g) * 1024);
    };
    struct S { static constexpr int issued(int s) { int a = 2 + s, b = s < 2 ? 4 : (s + G_::NS - 2) / 2; a = a < b ? a : b; return a < 8 ? a : 8; } };
    issue(0);
    asm volatile("s_barrier" ::: "memory");
    issue(1);
    static_for<G_::NT>([&](auto sc) {
        constexpr int s = decltype(sc)::value;
        wait_vm_barrier<8 * (S::issued(s) - 1 - s / 2) + (s & 1 ? 0 : 3)>();
        if constexpr (s == 0) ST(3);
        if constexpr (s == G_::NT - 1) ST(4);
#pragma unroll
        for (int j = S::issued(s); j < S::issued(s + 1); ++j) issue(j);
    });
    lds_barrier();
    ST(5); ST_OUT(PH_QKV, 0);
}

template <int PH> DI void gemm_load(char* ws, int layer, unsigned lds0, int wid, int lane, bool no_next, u64 tag, bool poller) {
    using G_ = Geo<PH>;
    constexpr int NX = NextPh<PH>::v;
    constexpr int MT = G_::MT, NT = G_::NT, KS = G_::KS, GA = G_::GA;
    ST_DECL ST(0);
    FRESH(ws, lds0, wid, lane, bid);
    int rb, cb, nrb, ncb; G_::tile(bid, rb, cb); Geo<NX>::tile(bid, nrb, ncb);
    const unsigned lw = layer * WS_LAYER;
    const unsigned vo = lane * 16;
    const char* sA = ws + lw + G_::A + (size_t)(rb * MT + (wid * GA) / KS) * (64 * G_::KTOT) + ((wid * GA) % KS) * 1024;
    ST(1);
    if constexpr (G_::SWAPIN) dep_barrier(poller, ws, lw + WS_FL + PH * FL_PH + (rb * G_::NCB + cb) * FR, PH == PH_QKV ? 8 : 14, tag, lane);
    else asm volatile("s_barrier" ::: "memory");
    ST(2);
    f32x4 stp[4];
    static_for<SchedOf<PH>::v.n>([&](auto ic) {
        constexpr int i = decltype(ic)::value, op = SchedOf<PH>::v.op[i], k = SchedOf<PH>::v.arg[i];
        if constexpr (op == OP_A || op == OP_F) glds_run<GA>(sA + k * (KS * 1024), vo, lds0 + G_::a_off(k) + wid * GA * 1024);
        if constexpr (op == OP_F) issue_b<PH>(ws, k, cb, lds0, wid, vo);
        if constexpr (op == OP_N) { if (!no_next) issue_b<NX>(ws, k, ncb, lds0, wid, vo); }
        if constexpr (op == OP_X) {
            const char* sp = ws + lw + WS_STB + (size_t)(rb * MT + wid % MT) * 4096;
            const unsigned so = (lane >> 5) * 2048 + (lane & 31) * 16;
            asm volatile("s_nop 4\n\tglobal_load_dwordx4 %0, %4, %5 sc1\n\tglobal_load_dwordx4 %1, %4, %5 offset:512 sc1\n\t"
                         "global_load_dwordx4 %2, %4, %5 offset:1024 sc1\n\tglobal_load_dwordx4 %3, %4, %5 offset:1536 sc1"
                         : "=&v"(stp[0]), "=&v"(stp[1]), "=&v"(stp[2]), "=&v"(stp[3]) : "v"(so), "s"(sp) : "memory");
        }
        if constexpr (op == OP_W) {
            if constexpr (G_::AUX && k == SchedOf<PH>::v.xw + 1) {
                asm volatile("" : "+v"(stp[0]), "+v"(stp[1]), "+v"(stp[2]), "+v"(stp[3]));
                const f32x4 t = (stp[0] + stp[1]) + (stp[2] + stp[3]);
                const float mean = half_sum(t[0] + t[2]) * (1.0f / DM), var = fmaxf(half_sum(t[1] + t[3]) * (1.0f / DM) - mean * mean, 0.f);
                if (lane < 32) ((LAS f32x2*)(uintptr_t)(lds0 + G_::ROWF))[(wid % MT) * 32 + lane] = f32x2{mean, __builtin_amdgcn_rsqf(var + LN_EPS)};
            }
            static_assert(SchedOf<PH>::v.pend[i] <= 63, "vmcnt");
            if (SchedOf<PH>::v.nn[i] && no_next) wait_vm_barrier<SchedOf<PH>::v.pend[i] - SchedOf<PH>::v.nn[i]>();
            else wait_vm_barrier<SchedOf<PH>::v.pend[i]>();
            if constexpr (k == 0) ST(3);
            if constexpr (k == NT - 1) ST(4);
        }
        if constexpr (op == OP_R) asm volatile("s_barrier" ::: "memory");
        if constexpr (op == OP_E) { if constexpr (G_::WK == 1) lds_barrier(); ST(5); }
    });
    ST_OUT(layer * 5 + PH, 0);
}

DI void load_unit(u32x4 (&v)[4], const char* src, int ld2, int lane) {
    const GAS char* a = (const GAS char*)src + (size_t)(lane >> 3) * ld2 + (lane & 7) * 16;
#pragma unroll
    for (int i = 0; i < 4; ++i) v[i] = *(const GAS u32x4*)(a + (size_t)i * 8 * ld2);
}
DI void store_unit_packed(const u32x4 (&v)[4], rsrc_t rws, unsigned dst, int lane) {
#pragma unroll
    for (int i = 0; i < 4; ++i) st16(rws, ((lane ^ (((lane >> 4) & 1) << 2)) << 4) + i * 1024, dst, v[i]);
}
DI void unit_colsum(const u32x4 (&v)[4], unsigned lds_dst, int lane) {
    float c[8];
#pragma unroll
    for (int e = 0; e < 4; ++e) {
        c[2 * e] = (bf_lo(v[0][e]) + bf_lo(v[1][e])) + (bf_lo(v[2][e]) + bf_lo(v[3][e]));
        c[2 * e + 1] = (bf_hi(v[0][e]) + bf_hi(v[1][e])) + (bf_hi(v[2][e]) + bf_hi(v[3][e]));
    }
#pragma unroll
    for (int e = 0; e < 4; ++e) { auto r = __builtin_amdgcn_permlane32_swap(__float_as_uint(c[e]), __float_as_uint(c[e + 4]), false, false); c[e] = __uint_as_float(r[0]) + __uint_as_float(r[1]); }
#pragma unroll
    for (int e = 0; e < 2; ++e) { auto r = __builtin_amdgcn_permlane16_swap(__float_as_uint(c[e]), __float_as_uint(c[e + 2]), false, false); c[e] = __uint_as_float(r[0]) + __uint_as_float(r[1]); }
    const bool b3 = lane & 8;
    const float t = (b3 ? c[1] : c[0]) + dpp<0x128>(b3 ? c[0] : c[1]);
    *(LAS float*)(uintptr_t)(lds_dst + ((lane & 7) * 8 + (lane >> 3)) * 4) = t;
}
DI void store_colsum(unsigned lds_src, rsrc_t rws, unsigned dst, int lane) {
    const LAS float* r = (const LAS float*)(uintptr_t)lds_src + lane;
    __builtin_amdgcn_raw_buffer_store_b32(__float_as_uint((r[0] + r[64]) + (r[128] + r[192])), rws, dst + lane * 4, 0, 16);
}
DI rsrc_t make_rsrc(const void* p, unsigned bytes) { return __builtin_amdgcn_make_buffer_rsrc((void*)p, 0, bytes, 0x00020000); }

struct MkArgs { const char* x; const char* Wqkv; const char* Wo; const char* Wg; const char* Wu; const char* Wd; char* ws; char* out; };

#define TLBS 65536u
#define TLBN 96
#define TLBPF 1
DI void startup_load(const MkArgs& p, int cw, int lane, u32x4 (&v)[4]) {
    const int bid = blockIdx.x < 224 ? blockIdx.x : 0;
    const char* src = bid < 128 ? p.x + (size_t)(bid >> 1) * 32768 + ((bid & 1) * 4 + cw) * 128 : p.Wqkv + (size_t)((bid & 3) * 4 + cw) * 32 * 3072 + ((bid - 128) >> 2) * 128;
    load_unit(v, src, bid < 128 ? 1024 : 3072, lane);
}
DI bool startup_job(rsrc_t rws, unsigned lds0, int cw, int lane, const u32x4 (&v)[4]) {
    const int bid = blockIdx.x;
    if (bid >= 224) return false;
    if (bid < 128) {
#pragma unroll
        for (int i = 0; i < 4; ++i) {
            float sum = 0.f, sq = 0.f;
#pragma unroll
            for (int j = 0; j < 4; ++j) { const float lo = bf_lo(v[i][j]), hi = bf_hi(v[i][j]); sum += lo + hi; sq = fmaf(lo, lo, fmaf(hi, hi, sq)); }
            sum += dpp<0xB1>(sum); sq += dpp<0xB1>(sq);
            sum += dpp<0x4E>(sum); sq += dpp<0x4E>(sq);
            if ((lane & 3) == 0) *(LAS f32x2*)(uintptr_t)(lds0 + AT_Q + cw * 512 + (i * 8 + (lane >> 3)) * 16 + ((lane >> 2) & 1) * 8) = f32x2{sum, sq};
        }
    } else unit_colsum(v, lds0 + AT_Q + cw * 256, lane);
    if (arrive(lds0 + LDS_CNT, lane) != 3) return false;
    if (bid < 128) {
#pragma unroll
        for (int e = 0; e < 2; ++e) st16(rws, lane * 16 + e * 1024, WS_STA + bid * 2048, *(LAS u32x4*)(uintptr_t)(lds0 + AT_Q + e * 1024 + lane * 16));
    } else store_colsum(lds0 + AT_Q, rws, WS_CSQ + (bid - 128) * 256, lane);
    return true;
}
DI void startup_signal(char* ws, int bid, int lane, u64 tag) {
    asm volatile("s_waitcnt vmcnt(0)" ::: "memory");
    if (bid < 128) signal(ws, WS_FL + PH_QKV * FL_PH + (bid >> 2) * 8 * FR, 8, 1, bid & 3, tag, lane);
    else { const int pan = (bid - 128) >> 2; signal(ws, WS_FL + PH_QKV * FL_PH + (pan & 7) * FR, 32, 8, 4 + (pan >> 3) * 4 + (bid & 3), tag, lane); }
}

template <int PH> DI void gemm_comp(char* ws, rsrc_t rws, rsrc_t rio, int layer, unsigned lds0, int cw, int lane, u64 tag, bool& poller, const bf16x8 (&wd)[40]) {
    using G_ = Geo<PH>;
    constexpr int MT = G_::MT, WN = G_::WN, NS = G_::NS, NT = G_::NT, KS = G_::KS, KG = G_::KG, UPS = G_::UPS, NU = G_::NU, TM = G_::TM, TN = G_::TN;
    constexpr int PBYTES = G_::PBYTES;
    constexpr bool LN = G_::LN;
    constexpr int CORR_U = (PH == PH_QKV ? 15 : 10) * UPS, SIG_U = 3, POLL_U = 9, STAT_U = 12;
    static_assert(!G_::AUX || (SchedOf<PH>::v.xw >= 0 && CORR_U >= (SchedOf<PH>::v.xw + 1) * UPS), "row table must be ready");
    ST_DECL ST(0);
    FRESH(ws, lds0, cw, lane, bid);
    int rb, cb; G_::tile(bid, rb, cb);
    const unsigned lw = layer * WS_LAYER;
    const int r32 = lane & 31, hh = lane >> 5, wm = cw / WN, wn = cw % WN;
    u32x4 resid[2], stp[4];
    float cpart[TN][2];
    auto load_cs = [&]() {
        constexpr int NPAN = PH == PH_QKV ? 8 : 28;
#pragma unroll
        for (int t = 0; t < TN; ++t)
#pragma unroll
            for (int e = 0; e < 2; ++e)
                cpart[t][e] = __uint_as_float(__builtin_amdgcn_raw_buffer_load_b32(rws, slot_col(r32) * 4 + hh * 512 + e * 256,
                                                                                   (PH == PH_QKV ? WS_CSQ : WS_CSG) + (t * NPAN + cb * (WN / 2) + (wn >> 1)) * 1024 + (wn & 1) * 128, 16));
    };
    bf16x8 wf[TN];
    auto make_wf = [&]() {
#pragma unroll
        for (int t = 0; t < TN; ++t) wf[t] = corr_frag(half_sum(cpart[t][0] + cpart[t][1]), false, hh);
    };
    auto load_res = [&]() {
        static_assert(Cfg<PH_WO>::XC == Cfg<PH_DN>::XC, "the residual tile stays with its workgroup");
        const bool raw = PH == PH_WO && layer == 0;
        const unsigned vo = (rb * MT + wm) * 32768 + (raw ? r32 * 1024 + hh * 16 + (cb * 8 + wn * 4) * 16 : lane * 16 + lw + (PH == PH_WO ? WS_XA : WS_XB) + (cb * 8 + wn * 4) * 512);
#pragma unroll
        for (int e = 0; e < 2; ++e) resid[e] = ld16(raw ? rio : rws, vo + e * (raw ? 32 : 1024), 0);
    };
    if constexpr (PH == PH_WO) load_res();
    auto load_st = [&]() {
#pragma unroll
        for (int k = 0; k < 4; ++k) stp[k] = ld16(rws, hh * 2048 + r32 * 16 + k * 512, lw + WS_STA + (rb * MT + wm) * 4096);
    };
    if (PH == PH_GLU || (PH == PH_QKV && layer)) load_cs();
    if constexpr (G_::SWAPIN) {
        if (PH != PH_QKV || layer) wait_vm_barrier<0>();
    } else dep_barrier(poller, ws, lw + WS_FL + PH * FL_PH + (rb * G_::NCB + cb) * FR, PH == PH_WO ? 32 : 16, tag, lane);
    if constexpr (PH == PH_DN) load_res();
    if (PH == PH_QKV && layer) load_st();
    LAS f32x2* rowf = (LAS f32x2*)(uintptr_t)(lds0 + G_::ROWF);
    constexpr bool KSP = G_::WK > 1;
    unsigned aoff = lds0 + (KSP ? wm * (KS * 1024 + 2048) : wm * TM * KS * 1024) + lane * 16;
    unsigned boff = lds0 + (wn >> 1) * PBYTES + tr_lane_off(lane, wn & 1) + (KSP ? wm * 4096 : 0);
    unsigned aoth = aoff + (1 - 2 * wm) * (KS * 1024), aoth2 = aoth + 65536, aown2 = aoff + 65536;
    if constexpr (KSP) asm volatile("" : "+v"(aoth), "+v"(aoth2), "+v"(aown2));
    unsigned aoff2 = aoff + 49152, boff2 = boff + 49152, aoff3 = aoff + 98304, boff3 = boff + 98304;
    asm volatile("" : "+v"(aoff2), "+v"(boff2), "+v"(aoff3), "+v"(boff3));
    unsigned aq[2][4];
    if constexpr (PH == PH_QKV) {
#pragma unroll
        for (int v = 0; v < 4; ++v) {
            aq[0][v] = layer ? aoff + (v >> 1) * G_::SBYTES + (v & 1) * 1024 : lds0 + wm * G_::SBYTES + r32 * 128 + (((2 * v + hh) ^ ((r32 >> 1) & 7)) << 4);
            aq[1][v] = aq[0][v] + 65536;
            asm volatile("" : "+v"(aq[0][v]), "+v"(aq[1][v]));
        }
    }
    constexpr int TSTR = (WN / 2) * PBYTES;

    f32x16 acc[TM][TN];
#pragma unroll
    for (int i = 0; i < TM; ++i)
#pragma unroll
        for (int t = 0; t < TN; ++t) acc[i][t] = f32x16{};
    if constexpr (!LN) {
#pragma unroll
        for (int r = 0; r < 16; r += 2) { acc[0][0][r] = bf_lo(resid[r >> 3][(r >> 1) & 3]); acc[0][0][r + 1] = bf_hi(resid[r >> 3][(r >> 1) & 3]); }
        asm volatile("" : "+v"(acc[0][0]));
    }
    if constexpr (PH == PH_GLU) { make_wf(); asm volatile("" : "+v"(wf[0]), "+v"(wf[1])); }
    float rstd[TM];
    u64 seen = 0;
    constexpr int NF = KG * (TM + TN), NM = KG * TM * TN;
    bf16x8 fr[2][NF];
    auto rd = [&](auto uc, auto fc) {
        constexpr int u = decltype(uc)::value, f = decltype(fc)::value;
        constexpr int ks = (u % UPS) * KG + f / (TM + TN), e = f % (TM + TN);
        constexpr int so = e < TN ? G_::b_off(u / UPS) : G_::a_off(u / UPS), hi = so / 49152, lo = so % 49152;
        if constexpr (e < TN && G_::held(u / UPS)) {}
        else if constexpr (e < TN) fr[u & 1][f] = lds_tr_frag((hi == 0 ? boff : hi == 1 ? boff2 : boff3) + lo + e * TSTR + ks * 2048);
        else if constexpr (KSP) fr[u & 1][f] = lds_b128((e == TN ? (so < 65536 ? aoff : aown2) : (so < 65536 ? aoth : aoth2)) + (so & 65535) + ks * 1024);
        else if constexpr (PH == PH_QKV) fr[u & 1][f] = lds_b128(aq[G_::a_off(u & ~1) >> 16][(u & 1) * 2 + ks] + (G_::a_off(u & ~1) & 65535));
        else fr[u & 1][f] = lds_b128((hi == 0 ? aoff : hi == 1 ? aoff2 : aoff3) + lo + ((e - TN) * KS + ks) * 1024);
    };
    auto mma = [&](auto tc, f32x16& c, const bf16x8& af, const bf16x8& bf) {
        if constexpr (PH == PH_QKV && decltype(tc)::value == 2) c = MFMA32(af, bf, c);
        else c = MFMA32(bf, af, c);
    };
    ST(1);
    lds_barrier();
    ST(2); STC(6);
    static_for<NF>([&](auto fc) { rd(std::integral_constant<int, 0>{}, fc); });
    SBAR();
    static_for<NU>([&](auto uc) {
        constexpr int u = decltype(uc)::value;
        if constexpr (PH == PH_QKV && u == SIG_U) { if (layer == 0 && __builtin_amdgcn_readfirstlane((int)poller)) { __builtin_amdgcn_s_setprio(3); startup_signal(ws, bid, lane, tag); __builtin_amdgcn_s_setprio(0); } SBAR(); }
        if constexpr (PH == PH_QKV && u == POLL_U) {
            if (layer == 0) {
                __builtin_amdgcn_s_setprio(3);
                seen = __hip_atomic_load((const GAS u64*)(ws + WS_FL + PH * FL_PH + (rb * G_::NCB + cb) * FR + (lane & 15) * TS), __ATOMIC_RELAXED, __HIP_MEMORY_SCOPE_AGENT);
                __builtin_amdgcn_s_setprio(0);
            }
            SBAR();
        }
        if constexpr (PH == PH_QKV && u == STAT_U) {
            if (layer == 0) {
                __builtin_amdgcn_s_setprio(3);
                if (!__all(seen == tag)) wait_tags(ws, WS_FL + PH * FL_PH + (rb * G_::NCB + cb) * FR, 16, tag, lane);
                asm volatile("" ::: "memory");
                load_st(); load_cs();
                __builtin_amdgcn_s_setprio(0);
            }
            SBAR();
        }
        if constexpr (LN && u == CORR_U) {
            if constexpr (PH == PH_QKV) make_wf();
#pragma unroll
            for (int i = 0; i < TM; ++i) {
                f32x2 st;
                if constexpr (PH == PH_QKV) {
                    const f32x4 t = (__builtin_bit_cast(f32x4, stp[0]) + __builtin_bit_cast(f32x4, stp[1])) + (__builtin_bit_cast(f32x4, stp[2]) + __builtin_bit_cast(f32x4, stp[3]));
                    const float mean = half_sum(t[0] + t[2]) * (1.0f / DM), var = fmaxf(half_sum(t[1] + t[3]) * (1.0f / DM) - mean * mean, 0.f);
                    st = f32x2{mean, __builtin_amdgcn_rsqf(var + LN_EPS)};
                    if (hh == 0) rowf[cw * 32 + r32] = st;
                } else st = rowf[(wm * TM + i) * 32 + r32];
                rstd[i] = st.y;
                const bf16x8 xf = corr_frag(-st.x, true, hh);
                static_for<TN>([&](auto tc) { mma(tc, acc[i][decltype(tc)::value], xf, wf[decltype(tc)::value]); });
            }
            SBAR();
        }
        if constexpr (u + 1 < NU && (u + 1) % UPS == 0) { asm volatile("s_barrier" ::: "memory"); SBAR(); }
        static_for<NM>([&](auto mc) {
            constexpr int m = decltype(mc)::value, g = m / (TM * TN), i = (m % (TM * TN)) / TN, t = m % TN;
            if constexpr (G_::held(u / UPS)) mma(std::integral_constant<int, t>{}, acc[i][t], fr[u & 1][g * (TM + TN) + TN + i], wd[(u - 8) * 2 + g]);
            else mma(std::integral_constant<int, t>{}, acc[i][t], fr[u & 1][g * (TM + TN) + TN + i], fr[u & 1][g * (TM + TN) + t]);
            if constexpr (u + 1 < NU) {
                SBAR();
                constexpr int PER = PH == PH_GLU ? 1 : 4;
                static_for<PER>([&](auto kc) {
                    constexpr int f = m * PER + decltype(kc)::value;
                    if constexpr (f < NF) rd(std::integral_constant<int, u + 1>{}, std::integral_constant<int, f>{});
                });
                SBAR();
            }
        });
    });

    if constexpr (KSP) {
        auto xbuf = [&](int w) { return lds0 + (w < 2 ? AT_Q : LDS_X) + (w & 1) * 4096 + lane * 16; };
#pragma unroll
        for (int g = 0; g < 4; ++g) *(LAS f32x4*)(uintptr_t)(xbuf(cw) + g * 1024) = f32x4{acc[1][0][4 * g], acc[1][0][4 * g + 1], acc[1][0][4 * g + 2], acc[1][0][4 * g + 3]};
        lds_barrier();
#pragma unroll
        for (int g = 0; g < 4; ++g) {
            const f32x4 o = *(LAS f32x4*)(uintptr_t)(xbuf(cw ^ 2) + g * 1024);
#pragma unroll
            for (int j = 0; j < 4; ++j) acc[0][0][4 * g + j] += o[j];
        }
    }
    STC(7); ST(3);
    if constexpr (PH == PH_QKV) {
        const int tile = rb * MT + wm;
        const int head0 = cb * 4 + wn * 2;
        const float rq = rstd[0] * QSCALE, rk = rstd[0];
#pragma unroll
        for (int e = 0; e < 2; ++e) {
            u32x4 q, k;
#pragma unroll
            for (int j = 0; j < 4; ++j) {
                q[j] = pack_bf16(rq * acc[0][0][8 * e + 2 * j], rq * acc[0][0][8 * e + 2 * j + 1]);
                k[j] = pack_bf16(rk * acc[0][1][8 * e + 2 * j], rk * acc[0][1][8 * e + 2 * j + 1]);
            }
            st16(rws, lane * 16, lw + WS_KV + ((head0 + e) * 128 + tile) * 1024, k);
            st16(rws, lane * 16, lw + WS_Q + ((head0 + e) * 64 + tile) * 1024, q);
        }
#pragma unroll
        for (int e = 0; e < 2; ++e) {
            u32x4 o;
#pragma unroll
            for (int j = 0; j < 4; ++j) {
                const f32x4 st = *(LAS f32x4*)&rowf[cw * 32 + 16 * e + 8 * (j >> 1) + 4 * hh + 2 * (j & 1)];
                o[j] = pack_bf16(st[1] * acc[0][2][8 * e + 2 * j], st[3] * acc[0][2][8 * e + 2 * j + 1]);
            }
            st16(rws, (r32 >> 4) * 131072 + e * 512 + hh * 256 + (r32 & 15) * 16, lw + WS_KV + (head0 * 128 + 64 + tile) * 1024, o);
        }
        lds_barrier();
        ST(4);
        poller = publish(ws, lw + WS_FL + PH_ATT * FL_PH + cb * 32 * FR, 32, 1, rb, tag, lds0 + LDS_CNT, lane);
    } else if constexpr (PH == PH_GLU) {
#pragma unroll
        for (int i = 0; i < TM; ++i) {
            const float r2 = rstd[i] * rstd[i];
#pragma unroll
            for (int e = 0; e < 2; ++e) {
                u32x4 o;
#pragma unroll
                for (int j = 0; j < 4; ++j) {
                    const int r = 8 * e + 2 * j;
                    o[j] = pack_bf16(fmaxf(r2 * acc[i][0][r], 0.f) * acc[i][1][r], fmaxf(r2 * acc[i][0][r + 1], 0.f) * acc[i][1][r + 1]);
                }
                st16(rws, lane * 16 + e * 1024, lw + WS_H + (rb * MT + i) * (64 * FF) + (cb * 16 + wn * 4) * 512, o);
            }
        }
        lds_barrier();
        ST(4);
        poller = publish(ws, lw + WS_FL + PH_DN * FL_PH + rb * 16 * FR, 16, 1, cb, tag, lds0 + LDS_CNT, lane);
    } else {
        const bool last = PH == PH_DN && layer == 1;
        const unsigned rbo = (rb * MT + wm) * 32768;
        const unsigned vo = last ? r32 * 1024 + hh * 16 + rbo + (cb * 8 + wn * 4) * 16 : lane * 16 + (PH == PH_WO ? lw + WS_XB : WS_LAYER + WS_XA) + rbo + (cb * 8 + wn * 4) * 512;
        const rsrc_t ro = last ? rio : rws;
#pragma unroll
        for (int e = 0; e < 2; ++e) {
            u32x4 o;
#pragma unroll
            for (int j = 0; j < 4; ++j) o[j] = pack_bf16(acc[0][0][8 * e + 2 * j], acc[0][0][8 * e + 2 * j + 1]);
            st16(ro, vo, e * (last ? 32 : 1024), o);
        }
        if (!last) {
            float sum[2] = {0.f, 0.f}, sq[2] = {0.f, 0.f};
#pragma unroll
            for (int r = 0; r < 16; ++r) { sum[r & 1] += acc[0][0][r]; sq[r & 1] = fmaf(acc[0][0][r], acc[0][0][r], sq[r & 1]); }
            const float a = half_sum(sum[0] + sum[1]), b = half_sum(sq[0] + sq[1]);
            if (hh == 0) __builtin_amdgcn_raw_buffer_store_b64(u32x2{__float_as_uint(a), __float_as_uint(b)}, rws,
                                                               r32 * 16 + (PH == PH_WO ? lw + WS_STB : WS_LAYER + WS_STA) + (rb * MT + wm) * 4096 + cb * 512 + wn * 8, 0, 16);
        }
        if constexpr (!KSP) lds_barrier();
        ST(4);
        if (!last) {
            if constexpr (PH == PH_WO) poller = publish(ws, lw + WS_FL + PH_GLU * FL_PH + (rb >> 1) * 14 * FR, 14, 1, (rb & 1) * 8 + cb, tag, lds0 + LDS_CNT, lane);
            else poller = publish(ws, WS_LAYER + WS_FL + PH_QKV * FL_PH + rb * 8 * FR, 8, 1, cb, tag, lds0 + LDS_CNT, lane);
        }
    }
    ST(5); ST_OUT(layer * 5 + PH, 1);
}

#define AT_E(C, i) "v_exp_f32 v[" #C "+" #i "], v[" #C "+" #i "]\n\t"
#define AT_C(P, C, i) "v_cvt_pk_bf16_f32 v[" #P "], v[" #C "+" #i "], v[" #C "+" #i "+1]\n\t"
#define AT_HALF(C, O, V0, PRE, MID, POST)                                                                         \
    AT_E(C, 0) AT_E(C, 1) PRE AT_E(C, 2) AT_E(C, 3) AT_E(C, 4) AT_E(C, 5) MID                                     \
    AT_E(C, 6) AT_E(C, 7) AT_C(108, C, 0) AT_C(109, C, 2) AT_C(110, C, 4) AT_E(C, 8) AT_C(111, C, 6) AT_E(C, 9) AT_E(C, 10) \
    "v_mfma_f32_32x32x16_bf16 " O ", v[" #V0 ":" #V0 "+3], v[108:111], " O "\n\t" POST                            \
    AT_E(C, 11) AT_E(C, 12) AT_E(C, 13) AT_E(C, 14) AT_E(C, 15)                                                   \
    AT_C(112, C, 8) AT_C(113, C, 10) AT_C(114, C, 12) AT_C(115, C, 14)
#define AT_TILE(U, V0C, V0N, SYNC)                                                                                \
    AT_HALF(64, "%[o0]", V0C,                                                                                     \
            "v_mfma_f32_32x32x16_bf16 %[o1], v[104:107], v[112:115], %[o1]\n\t"                                   \
            "ds_read_b128 v[104:107], %[va] offset:" #U "*1024+512\n\t",                                          \
            "v_mfma_f32_32x32x16_bf16 v[80:95], v[96:99], %[q1], %[nm1]\n\t" SYNC                                 \
            "ds_read_b128 v[96:99], %[ka] offset:" #U "*1024+1024\n\t"                                            \
            "ds_read_b128 v[" #V0N ":" #V0N "+3], %[va] offset:" #U "*1024+1024\n\t",                             \
            "s_waitcnt lgkmcnt(2)\n\t")                                                                           \
    AT_HALF(80, "%[o1]", V0C,                                                                                     \
            "v_mfma_f32_32x32x16_bf16 %[o0], v[104:107], v[112:115], %[o0]\n\t"                                   \
            "s_waitcnt lgkmcnt(0)\n\t",                                                                           \
            "v_mfma_f32_32x32x16_bf16 v[64:79], v[96:99], %[q0], %[nm0]\n\t", "")

DI f32x16 neg_tile_max(const bf16x8& k0, const bf16x8& q) {
    const f32x16 s0 = MFMA32(k0, q, f32x16{});
    float m = s0[0];
#pragma unroll
    for (int r = 1; r < 16; ++r) m = fmaxf(m, s0[r]);
    m = half_max(m);
    f32x16 n;
#pragma unroll
    for (int r = 0; r < 16; ++r) n[r] = -m;
    return n;
}
DI f32x16 attn_exact(const bf16x8& q, rsrc_t rws, unsigned kv, unsigned ones, int lane) {
    const bool vlane = (lane & 31) < 16;
    const bf16x8 pad = lds_b128(ones);
    float m = -INFINITY;
    f32x16 o = f32x16{};
#pragma unroll 1
    for (int t = 0; t < 64; ++t) {
        const bf16x8 kf = __builtin_bit_cast(bf16x8, ld16(rws, lane * 16, kv + t * 1024));
        const unsigned vo = (lane >> 5) * 256 + (lane & 15) * 16;
        bf16x8 v0 = __builtin_bit_cast(bf16x8, ld16(rws, vo, kv + 65536 + t * 1024)), v1 = __builtin_bit_cast(bf16x8, ld16(rws, vo + 512, kv + 65536 + t * 1024));
        if (!vlane) { v0 = pad; v1 = pad; }
        const f32x16 s = MFMA32(kf, q, f32x16{});
        float tm = s[0];
#pragma unroll
        for (int r = 1; r < 16; ++r) tm = fmaxf(tm, s[r]);
        const float mn = fmaxf(m, half_max(tm));
        const float alpha = __builtin_amdgcn_exp2f(m - mn);
        m = mn;
#pragma unroll
        for (int r = 0; r < 16; ++r) o[r] *= alpha;
        u32x4 p0, p1;
#pragma unroll
        for (int j = 0; j < 4; ++j) {
            p0[j] = pack_bf16(__builtin_amdgcn_exp2f(s[2 * j] - m), __builtin_amdgcn_exp2f(s[2 * j + 1] - m));
            p1[j] = pack_bf16(__builtin_amdgcn_exp2f(s[8 + 2 * j] - m), __builtin_amdgcn_exp2f(s[8 + 2 * j + 1] - m));
        }
        o = MFMA32(v0, __builtin_bit_cast(bf16x8, p0), o);
        o = MFMA32(v1, __builtin_bit_cast(bf16x8, p1), o);
    }
    return o;
}

DI void attn_load(const MkArgs& p, rsrc_t rws, int layer, unsigned lds0, int wid, int lane, u64 tag, bf16x8 (&wd)[40]) {
    ST_DECL ST(0);
    char* ws = p.ws;
    FRESH(ws, lds0, wid, lane, bid);
    const int x = bid & 7, idx = bid >> 3, head = x * 4 + (idx >> 3), qb = idx & 7;
    const unsigned lw = layer * WS_LAYER;
    const unsigned vo = lane * 16;
    const char* src = ws + lw + WS_KV + (size_t)head * 131072 + wid * 2048;
    auto chunk = [&](int c) {
        const char* s = src + ((qb + c) & 7) * 8192;
        glds_run<2>(s, vo, lds0 + AT_K + c * 8192 + wid * 2048);
        glds_run<2>(s + 65536, vo, lds0 + AT_V + c * 8192 + wid * 2048);
    };
    ST(1);
    asm volatile("s_barrier" ::: "memory");
    ST(2);
    glds_run<2>(ws + lw + WS_Q + (size_t)(head * 64 + qb * 8 + wid * 2) * 1024, vo, lds0 + AT_Q + wid * 2048);
    chunk(0);
    wait_vm_barrier<0>();
    ST(3);
    wait_tags(ws, lw + WS_FL + PH_ATT * FL_PH + (head * 8 + qb) * FR, 32, tag, lane);
    chunk(1);
    chunk(2); chunk(3); chunk(4); chunk(5); chunk(6); chunk(7);
    wait_vm_barrier<0>();
    ST(4);
    if (layer == 0) {
#pragma unroll 1
        for (int u = bid * 4 + wid; u < 1536; u += 1024) {
            const char* s; size_t dst; int ld2;
            if (u < 896) { const int mat = u >= 448, v = u - mat * 448, pan = v >> 4, kb = v & 15;
                s = (mat ? p.Wu : p.Wg) + (size_t)kb * 32 * (FF * 2) + pan * 128; ld2 = FF * 2; dst = Geo<PH_GLU>::packed_off(pan >> 1, mat * 2 + (pan & 1), kb); }
            else if (u < 1024) { const int v = u - 896, pan = v & 7, kb = v >> 3;
                s = p.Wo + (size_t)kb * 32 * 1024 + pan * 128; ld2 = 1024; dst = Geo<PH_WO>::packed_off(pan, 0, kb); }
            else if (u < 1152) { const int v = u - 1024, pan = v & 7, kb = v >> 3;
                s = p.Wd + (size_t)kb * 32 * 1024 + pan * 128; ld2 = 1024; dst = Geo<PH_DN>::packed_off(pan, 0, kb); }
            else { const int v = u - 1152, pan = v >> 4, kb = v & 15;
                s = p.Wqkv + (size_t)kb * 32 * 3072 + pan * 128; ld2 = 3072; dst = Geo<PH_QKV>::packed_off(pan & 7, pan >> 3, kb); }
            u32x4 v[4];
            load_unit(v, s, ld2, lane);
            store_unit_packed(v, rws, (unsigned)dst, lane);
            if (u < 896) unit_colsum(v, lds0 + LDS_ROWG + wid * 256, lane);
        }
        if (arrive(lds0 + LDS_CNT + 4, lane) == 3) {
            if (bid < 224) { store_colsum(lds0 + LDS_ROWG, rws, WS_CSG + bid * 256, lane); asm volatile("s_waitcnt vmcnt(0)" ::: "memory"); }
            if (lane == 0) __hip_atomic_store((GAS u64*)(ws + WS_FL + FL_PK + bid * 8), tag, __ATOMIC_RELAXED, __HIP_MEMORY_SCOPE_AGENT);
            __builtin_amdgcn_s_sleep(24);
#pragma unroll 1
            for (int i = 0; i < 4; ++i) wait_tags(ws, WS_FL + FL_PK + ((bid + i) & 3) * 512, 64, tag, lane);
        }
    }
    lds_barrier();
    const unsigned own = lds0 + Geo<PH_WO>::b_off(0) + (wid >> 1) * 65536 + (wid & 1) * 16384;
    if (layer == 0) {
        int rb, cb; Geo<PH_DN>::tile(bid, rb, cb);
        const char* sw = p.Wd + cb * 128 + (wid & 1) * 64 + (wid >> 1) * 32768;
        const unsigned vw = (lane >> 2) * 1024 + (lane & 3) * 16;
        const int pp = lane & 3;
        const unsigned ra = own + (8 * (lane >> 5) + ((lane & 15) >> 2)) * 64 + ((lane >> 4) & 1) * 32 + ((((pp & 1) << 1) | (pp >> 1)) << 3);
        typedef short v4i16_t __attribute__((ext_vector_type(4)));
#pragma unroll
        for (int r0 = 0; r0 < 40; r0 += 16) {
#pragma unroll
            for (int i = r0; i < r0 + 16 && i < 40; ++i) glds_in(sw + (size_t)((8 + (i >> 1)) * 64 + (i & 1) * 16) * 1024, vw, own + (i - r0) * 1024);
            asm volatile("s_waitcnt vmcnt(0)" ::: "memory");
#pragma unroll
            for (int i = r0; i < r0 + 16 && i < 40; ++i) {
                const v4i16_t lo = __builtin_amdgcn_ds_read_tr16_b64_v4i16((LAS v4i16_t*)(uintptr_t)(ra + (i - r0) * 1024));
                const v4i16_t hi = __builtin_amdgcn_ds_read_tr16_b64_v4i16((LAS v4i16_t*)(uintptr_t)(ra + (i - r0) * 1024 + 256));
                wd[i] = __builtin_shufflevector(lo, hi, 0, 1, 2, 3, 4, 5, 6, 7);
            }
#pragma unroll
            for (int i = r0; i < r0 + 16 && i < 40; ++i) asm volatile("" : "+v"(wd[i])::"memory");
        }
    }
    {
        int rb, cb; Geo<PH_WO>::tile(bid, rb, cb);
        static_assert(Geo<PH_WO>::b_off(2) == 16384 && Geo<PH_WO>::b_off(4) == 65536 && Geo<PH_WO>::b_off(6) == 81920, "");
#pragma unroll
        for (int i = 0; i < 4; ++i) glds_run<4>(ws + WP_O + cb * 65536 + wid * 16384 + i * 4096, vo, own + i * 4096);
    }
    lds_barrier();
    ST(5); ST_OUT(layer * 5 + PH_ATT, 0);
}

DI void attn_comp(char* ws, rsrc_t rws, int layer, unsigned lds0, int cw, int lane, u64 tag, bool& poller) {
    ST_DECL ST(0);
    FRESH(ws, lds0, cw, lane, bid);
    const int x = bid & 7, idx = bid >> 3, head = x * 4 + (idx >> 3), qb = idx & 7;
    const unsigned lw = layer * WS_LAYER;
    dep_barrier(poller, ws, lw + WS_FL + PH_ATT * FL_PH + (head * 8 + qb) * FR + qb * 4 * TS, 4, tag, lane);
    const int r32 = lane & 31, hh = lane >> 5;
    const unsigned ka0 = lds0 + AT_K + lane * 16;
    const bool vlane = r32 < 16;
    const unsigned va0 = lds0 + (vlane ? AT_V : AT_ONES) + hh * 256 + (r32 & 15) * 16;
    const unsigned vstep = vlane ? 8192u : 0u;

    ST(1);
    lds_barrier();
    ST(2);
    const bf16x8 q0 = lds_b128(lds0 + AT_Q + cw * 2048 + lane * 16), q1 = lds_b128(lds0 + AT_Q + cw * 2048 + 1024 + lane * 16);
    const bf16x8 k0 = lds_b128(ka0);
    const f32x16 nm0 = neg_tile_max(k0, q0), nm1 = neg_tile_max(k0, q1);
    f32x16 o0 = f32x16{}, o1 = f32x16{};
    STC(6);
    {
        unsigned ka = ka0, va = va0; int cnt, tmp;
        asm volatile(
            "ds_read_b128 v[96:99], %[ka]\n\t"
            "ds_read_b128 v[100:103], %[va]\n\t"
            "v_mov_b32 v104, 0\n\tv_mov_b32 v105, 0\n\tv_mov_b32 v106, 0\n\tv_mov_b32 v107, 0\n\t"
            "v_mov_b32 v112, 0\n\tv_mov_b32 v113, 0\n\tv_mov_b32 v114, 0\n\tv_mov_b32 v115, 0\n\t"
            "s_mov_b32 %[cnt], 0\n\t"
            "s_waitcnt lgkmcnt(0)\n\t"
            "v_mfma_f32_32x32x16_bf16 v[64:79], v[96:99], %[q0], %[nm0]\n\t"
            "s_nop 12\n\t"
            "1:\n\t"
            AT_TILE(0, 100, 116, "") AT_TILE(1, 116, 100, "") AT_TILE(2, 100, 116, "") AT_TILE(3, 116, 100, "")
            AT_TILE(4, 100, 116, "") AT_TILE(5, 116, 100, "") AT_TILE(6, 100, 116, "")
            AT_TILE(7, 116, 100, "s_and_b32 %[tmp], %[cnt], 3\n\ts_cbranch_scc1 2f\n\ts_barrier\n\t2:\n\t")
            "v_add_u32 %[ka], 0x2000, %[ka]\n\t"
            "v_add_u32 %[va], %[vstep], %[va]\n\t"
            "s_add_u32 %[cnt], %[cnt], 1\n\t"
            "s_cmp_lt_u32 %[cnt], 8\n\t"
            "s_cbranch_scc1 1b\n\t"
            "v_mfma_f32_32x32x16_bf16 %[o1], v[104:107], v[112:115], %[o1]\n\t"
            "s_nop 15\n\t"
            : [o0] "+v"(o0), [o1] "+v"(o1), [ka] "+v"(ka), [va] "+v"(va), [cnt] "=&s"(cnt), [tmp] "=&s"(tmp)
            : [q0] "v"(q0), [q1] "v"(q1), [nm0] "v"(nm0), [nm1] "v"(nm1), [vstep] "v"(vstep)
            : "memory", "scc", "v64", "v65", "v66", "v67", "v68", "v69", "v70", "v71", "v72", "v73", "v74", "v75", "v76", "v77", "v78", "v79",
              "v80", "v81", "v82", "v83", "v84", "v85", "v86", "v87", "v88", "v89", "v90", "v91", "v92", "v93", "v94", "v95",
              "v96", "v97", "v98", "v99", "v100", "v101", "v102", "v103", "v104", "v105", "v106", "v107",
              "v108", "v109", "v110", "v111", "v112", "v113", "v114", "v115", "v116", "v117", "v118", "v119");
    }
    STC(7); ST(3);
    if (__builtin_expect(__any(!(o0[8] < LMAX) || !(o1[8] < LMAX)), 0)) {
        o0 = attn_exact(q0, rws, lw + WS_KV + head * 131072, lds0 + AT_ONES + (r32 & 15) * 16, lane);
        o1 = attn_exact(q1, rws, lw + WS_KV + head * 131072, lds0 + AT_ONES + (r32 & 15) * 16, lane);
    }
    const float i0 = 1.0f / o0[8], i1 = 1.0f / o1[8];
    u32x4 w0, w1;
#pragma unroll
    for (int j = 0; j < 4; ++j) { w0[j] = pack_bf16(o0[2 * j] * i0, o0[2 * j + 1] * i0); w1[j] = pack_bf16(o1[2 * j] * i1, o1[2 * j + 1] * i1); }
    const int qtile = qb * 8 + cw * 2;
    st16(rws, lane * 16, lw + WS_O + qtile * 32768 + head * 1024, w0);
    st16(rws, lane * 16, lw + WS_O + (qtile + 1) * 32768 + head * 1024, w1);
    lds_barrier();
    ST(4);
    poller = publish(ws, lw + WS_FL + PH_WO * FL_PH + qb * 32 * FR, 32, 1, head, tag, lds0 + LDS_CNT, lane);
    ST(5); ST_OUT(layer * 5 + PH_ATT, 1);
}

__global__ __launch_bounds__(512, 2) void mk_kernel(MkArgs p) {
    extern __shared__ __attribute__((aligned(16))) char smem[];
    const unsigned lds0 = (unsigned)(uintptr_t)(LAS char*)smem;
    const int tid = threadIdx.x, lane = tid & 63, wid = __builtin_amdgcn_readfirstlane(tid >> 6);
    const rsrc_t rws = make_rsrc(p.ws, WS_END);
    const bool glu = (blockIdx.x >> 3) < 28;
    const char* code;
    asm volatile("s_getpc_b64 %0" : "=s"(code));
    u64 epoch;
    asm volatile("s_load_dwordx2 %0, %1, 0x0" : "=s"(epoch) : "s"(p.ws + WS_FL + FL_EPOCH) : "memory");
    if (wid < 4) {
        first_load(p.x, p.Wqkv, lds0, wid, lane);
        asm volatile("s_waitcnt lgkmcnt(0)" : "+s"(epoch));
        const u64 tag = epoch + 1;
        const rsrc_t rout = make_rsrc(p.out, SEQ * DM * 2);
        bf16x8 wd[40];
        bool poller = false;
#pragma unroll 1
        for (int layer = 0; layer < 2; ++layer) {
            if (layer) gemm_load<PH_QKV>(p.ws, layer, lds0, wid, lane, false, tag, poller);
            attn_load(p, rws, layer, lds0, wid, lane, tag, wd);
            gemm_load<PH_WO>(p.ws, layer, lds0, wid, lane, !glu, tag, false);
            if (glu) gemm_load<PH_GLU>(p.ws, layer, lds0, wid, lane, false, tag, false);
            else prefetch_b<PH_DN>(p.ws, lds0, wid, lane);
            gemm_comp<PH_DN>(p.ws, rws, rout, layer, lds0, wid, lane, tag, poller, wd);
        }
    } else {
        const rsrc_t rx = make_rsrc(p.x, SEQ * DM * 2);
        bf16x8 none[40];
        __builtin_amdgcn_s_setprio(3);
        unsigned ct = 0;
        if (wid == 4 && blockIdx.x < 64 && lane < 58) ct = *(const GAS unsigned*)(code + (blockIdx.x >> 3) * (58 * 128) + lane * 128);
        if (wid == 5 && blockIdx.x < TLBN) ct = __builtin_amdgcn_raw_buffer_load_b32(rws, ((blockIdx.x >> 3) * 64 + lane) * TLBS, 0, 16);
        u32x4 sv[4];
        startup_load(p, wid - 4, lane, sv);
        {
            const unsigned ov = ((tid & 15) == 0 || (tid & 15) == 4) ? 0x3f803f80u : 0u;
            *(LAS u32x4*)(uintptr_t)(lds0 + AT_ONES + (tid - 256) * 16) = u32x4{ov, ov, ov, ov};
            *(LAS u32x4*)(uintptr_t)(lds0 + AT_ONES + tid * 16) = u32x4{ov, ov, ov, ov};
            if (tid < 320) *(LAS u32x4*)(uintptr_t)(lds0 + AT_ONES + (tid + 256) * 16) = u32x4{ov, ov, ov, ov};
            if (tid == 256) *(LAS u32x2*)(uintptr_t)(lds0 + LDS_CNT) = u32x2{0, 0};
        }
        lds_barrier();
        asm volatile("" : "+v"(ct));
        asm volatile("" : "+v"(sv[0]), "+v"(sv[1]), "+v"(sv[2]), "+v"(sv[3]));
        bool poller = startup_job(rws, lds0, wid - 4, lane, sv);
        asm volatile("s_waitcnt lgkmcnt(0)" : "+s"(epoch));
        const u64 tag = epoch + 1;
        __builtin_amdgcn_s_setprio(0);
#pragma unroll 1
        for (int layer = 0; layer < 2; ++layer) {
            gemm_comp<PH_QKV>(p.ws, rws, rx, layer, lds0, wid - 4, lane, tag, poller, none);
            attn_comp(p.ws, rws, layer, lds0, wid - 4, lane, tag, poller);
            gemm_comp<PH_WO>(p.ws, rws, rx, layer, lds0, wid - 4, lane, tag, poller, none);
            if (glu) gemm_comp<PH_GLU>(p.ws, rws, rx, layer, lds0, wid - 4, lane, tag, poller, none);
            gemm_load<PH_DN>(p.ws, layer, lds0, wid - 4, lane, layer == 1, tag, poller);
        }
        if (blockIdx.x == 0 && tid == 256) __hip_atomic_store((GAS u64*)(p.ws + WS_FL + FL_EPOCH), tag, __ATOMIC_RELAXED, __HIP_MEMORY_SCOPE_AGENT);
    }
}

extern "C" void kernel_launch(void* const* d_in, const int* in_sizes, int n_in, void* d_out, int out_size, void* d_ws, size_t ws_size,
                              hipStream_t stream) {
    (void)hipFuncSetAttribute((const void*)mk_kernel, hipFuncAttributeMaxDynamicSharedMemorySize, LDS_BYTES);
    mk_kernel<<<256, 512, LDS_BYTES, stream>>>(MkArgs{(const char*)d_in[0], (const char*)d_in[1], (const char*)d_in[2], (const char*)d_in[4], (const char*)d_in[5],
                                                      (const char*)d_in[6], (char*)d_ws, (char*)d_out});
}
```

```cpp
#include <hip/hip_runtime.h>
#include <hip/hip_bf16.h>
#include <stdint.h>
#include <utility>

typedef short bf16x8 __attribute__((ext_vector_type(8)));
typedef float f32x16 __attribute__((ext_vector_type(16)));
typedef float f32x4 __attribute__((ext_vector_type(4)));
typedef float f32x2 __attribute__((ext_vector_type(2)));
typedef unsigned u32x4 __attribute__((ext_vector_type(4)));
typedef unsigned u32x2 __attribute__((ext_vector_type(2)));
typedef __bf16 bf16x2_t __attribute__((ext_vector_type(2)));
typedef __amdgpu_buffer_rsrc_t rsrc_t;
#define LAS __attribute__((address_space(3)))
#define GAS __attribute__((address_space(1)))
#define DI __device__ __forceinline__
#define MFMA32(a, b, c) __builtin_amdgcn_mfma_f32_32x32x16_bf16((a), (b), (c), 0, 0, 0)
#define SBAR() __builtin_amdgcn_sched_barrier(0)

constexpr int SEQ = 2048, DM = 512, FF = 1792;
constexpr float LN_EPS = 1e-5f;
constexpr float QSCALE = 0.25f * 1.4426950408889634f;

enum { PH_QKV = 0, PH_ATT = 1, PH_WO = 2, PH_GLU = 3, PH_DN = 4 };
#define LMAX 1.0e18f
constexpr unsigned MB = 1u << 20;
constexpr unsigned WS_LAYER = 20 * MB;
constexpr unsigned WS_Q = 0, WS_KV = 2 * MB, WS_O = 6 * MB, WS_XA = 8 * MB, WS_XB = 10 * MB, WS_H = 12 * MB, WS_STA = 19 * MB, WS_STB = WS_STA + MB / 4, WS_FL = WS_STB + MB / 4;
constexpr unsigned TS = 8, FR = 32 * TS;
constexpr unsigned FL_PH = 256 * FR, FL_PK = 5 * FL_PH, FL_EPOCH = FL_PK + 2048;
constexpr unsigned WS_WP = 2 * WS_LAYER;
constexpr unsigned WP_QKV = WS_WP, WP_O = WP_QKV + DM * 3 * DM * 2, WP_GU = WP_O + DM * DM * 2, WP_D = WP_GU + DM * FF * 4;
constexpr unsigned WS_CSQ = WP_D + FF * DM * 2, WS_CSG = WS_CSQ + 3 * DM * 16, WS_END = WS_CSG + 2 * FF * 16;
typedef unsigned long long u64;

template <int... I, class Fn> DI void static_for_impl(std::integer_sequence<int, I...>, Fn&& f) { (f(std::integral_constant<int, I>{}), ...); }
template <int N, class Fn> DI void static_for(Fn&& f) { static_for_impl(std::make_integer_sequence<int, N>{}, f); }

DI unsigned pack_bf16(float lo, float hi) { bf16x2_t p; p.x = (__bf16)lo; p.y = (__bf16)hi; return __builtin_bit_cast(unsigned, p); }
DI float bf_lo(unsigned u) { return __uint_as_float(u << 16); }
DI float bf_hi(unsigned u) { return __uint_as_float(u & 0xffff0000u); }
template <int CTRL> DI float dpp(float v) { return __uint_as_float(__builtin_amdgcn_update_dpp(0u, __float_as_uint(v), CTRL, 0xf, 0xf, true)); }
DI float half_sum(float v) {
    auto r = __builtin_amdgcn_permlane32_swap(__float_as_uint(v), __float_as_uint(v), false, false);
    return __uint_as_float(r[0]) + __uint_as_float(r[1]);
}
DI float half_max(float v) {
    auto r = __builtin_amdgcn_permlane32_swap(__float_as_uint(v), __float_as_uint(v), false, false);
    return fmaxf(__uint_as_float(r[0]), __uint_as_float(r[1]));
}
DI void st16(rsrc_t r, unsigned voff, unsigned soff, u32x4 v) { __builtin_amdgcn_raw_buffer_store_b128(v, r, voff + soff, 0, 16); }
DI u32x4 ld16(rsrc_t r, unsigned voff, unsigned soff) { return __builtin_amdgcn_raw_buffer_load_b128(r, voff, soff, 16); }
template <int N> DI void wait_vm_barrier() { asm volatile("s_waitcnt vmcnt(%0) lgkmcnt(0)\n\ts_barrier" ::"i"(N) : "memory"); }
DI void lds_barrier() { asm volatile("s_waitcnt lgkmcnt(0)\n\ts_barrier" ::: "memory"); }
DI bf16x8 lds_b128(unsigned addr) { return *(const LAS bf16x8*)(uintptr_t)addr; }
DI bf16x8 lds_tr_frag(unsigned addr) {
    typedef short v4i16_t __attribute__((ext_vector_type(4)));
    v4i16_t lo = __builtin_amdgcn_ds_read_tr16_b64_v4i16((LAS v4i16_t*)(uintptr_t)addr);
    v4i16_t hi = __builtin_amdgcn_ds_read_tr16_b64_v4i16((LAS v4i16_t*)(uintptr_t)(addr + 512));
    return __builtin_shufflevector(lo, hi, 0, 1, 2, 3, 4, 5, 6, 7);
}
template <int N> DI void glds_run(const char* sbase, unsigned voff, unsigned lds_dst) {
    static_assert(N >= 1 && N <= 4, "");
#define GL(o) "\n\tglobal_load_lds_dwordx4 %0, %1 offset:" #o " sc1"
    if constexpr (N == 1) asm volatile("s_mov_b32 m0, %2\n\ts_nop 3" GL(0)::"v"(voff), "s"(sbase), "s"(lds_dst) : "memory");
    if constexpr (N == 2) asm volatile("s_mov_b32 m0, %2\n\ts_nop 3" GL(0) GL(1024)::"v"(voff), "s"(sbase), "s"(lds_dst) : "memory");
    if constexpr (N == 3) asm volatile("s_mov_b32 m0, %2\n\ts_nop 3" GL(0) GL(1024) GL(2048)::"v"(voff), "s"(sbase), "s"(lds_dst) : "memory");
    if constexpr (N == 4) asm volatile("s_mov_b32 m0, %2\n\ts_nop 3" GL(0) GL(1024) GL(2048) GL(3072)::"v"(voff), "s"(sbase), "s"(lds_dst) : "memory");
#undef GL
}
DI void glds_in(const char* sbase, unsigned voff, unsigned lds_dst) {
    asm volatile("s_mov_b32 m0, %2\n\ts_nop 3\n\tglobal_load_lds_dwordx4 %0, %1" ::"v"(voff), "s"(sbase), "s"(lds_dst) : "memory");
}
DI int arrive(unsigned cnt_addr, int lane) {
    asm volatile("s_waitcnt vmcnt(0) lgkmcnt(0)" ::: "memory");
    unsigned old = 0;
    if (lane == 0) old = __hip_atomic_fetch_add((LAS unsigned*)(uintptr_t)cnt_addr, 1u, __ATOMIC_RELAXED, __HIP_MEMORY_SCOPE_WORKGROUP);
    asm volatile("" ::: "memory");
    return __builtin_amdgcn_readfirstlane(old) & 3;
}
DI void signal(char* ws, unsigned first_line_off, int n, int lstride, int me, u64 tag, int lane) {
    if (lane < n) __hip_atomic_store((GAS u64*)(ws + first_line_off + lane * lstride * FR + me * TS), tag, __ATOMIC_RELAXED, __HIP_MEMORY_SCOPE_AGENT);
}
DI bool publish(char* ws, unsigned first_line_off, int n, int lstride, int me, u64 tag, unsigned cnt_addr, int lane) {
    const int rank = arrive(cnt_addr, lane);
    if (rank == 3) signal(ws, first_line_off, n, lstride, me, tag, lane);
    return rank == 3;
}
DI void wait_tags(const char* ws, unsigned off, int n, u64 tag, int lane) {
    const GAS u64* f = (const GAS u64*)(ws + off + (lane < n ? lane : 0) * TS);
#pragma unroll 1
    for (int spin = 0; spin < (1 << 16); ++spin) {
        const u64 v = __hip_atomic_load(f, __ATOMIC_RELAXED, __HIP_MEMORY_SCOPE_AGENT);
        if (__all(v == tag)) break;
        __builtin_amdgcn_s_sleep(1);
    }
    asm volatile("s_waitcnt vmcnt(0)" ::: "memory");
}
DI void dep_barrier(bool poller, const char* ws, unsigned off, int n, u64 tag, int lane) {
    if (__builtin_amdgcn_readfirstlane((int)poller)) wait_tags(ws, off, n, tag, lane);
    lds_barrier();
}

template <int PH> struct Cfg;
template <> struct Cfg<PH_QKV> { static constexpr int KTOT = 512, MT = 2, NP = 3, WM = 2, WN = 2, WK = 1, BK = 32, NS = 8, PA = 8, KG = 2, NCB = 8, XC = 2; };
template <> struct Cfg<PH_WO> { static constexpr int KTOT = 512, MT = 2, NP = 1, WM = 2, WN = 2, WK = 1, BK = 64, NS = 8, PA = 4, KG = 4, NCB = 8, XC = 2; };
template <> struct Cfg<PH_GLU> { static constexpr int KTOT = 512, MT = 4, NP = 4, WM = 1, WN = 4, WK = 1, BK = 32, NS = 5, PA = 3, KG = 1, NCB = 14, XC = 2; };
template <> struct Cfg<PH_DN> { static constexpr int KTOT = FF, MT = 2, NP = 1, WM = 1, WN = 2, WK = 2, BK = 64, NS = 8, PA = 4, KG = 2, NCB = 8, XC = 2; };

constexpr int AT_K = 0, AT_V = 65536, AT_ONES = 131072, AT_Q = AT_ONES + 9216, LDS_ROWQ = AT_Q + 8192, LDS_ROWG = LDS_ROWQ + 1024, LDS_CNT = LDS_ROWG + 1024, LDS_X = LDS_CNT + 16, LDS_BYTES = LDS_X + 8192;

template <int PH> struct Geo : Cfg<PH> {
    using C = Cfg<PH>;
    static constexpr int TM = C::MT / C::WM, TN = C::NP * 2 / C::WN, NT = C::KTOT / C::BK, KS = C::BK / 16, UPS = KS / C::WK / C::KG, NU = NT * UPS;
    static constexpr int ABYTES = C::MT * C::BK * 64, PBYTES = C::BK * 128, BBYTES = C::NP * PBYTES, SBYTES = ABYTES + BBYTES;
    static constexpr int GA = ABYTES / 4096, GB = BBYTES / 4096, G = GA + GB;
    static constexpr bool LN = PH == PH_QKV || PH == PH_GLU;
    static constexpr int ROWF = PH == PH_QKV ? LDS_ROWQ : LDS_ROWG;
    static constexpr int NRB = SEQ / (32 * C::MT), AUX = PH == PH_GLU ? 8 : 0;
    static constexpr unsigned WP = PH == PH_QKV ? WP_QKV : PH == PH_WO ? WP_O : PH == PH_GLU ? WP_GU : WP_D;
    static constexpr unsigned A = PH == PH_QKV ? WS_XA : PH == PH_WO ? WS_O : PH == PH_GLU ? WS_XB : WS_H;
    static_assert(C::WM * C::WN * C::WK == 4 && GA * 4096 == ABYTES && GB * 4096 == BBYTES && GA <= KS && KS % GA == 0 && GB <= 4, "pieces must divide over the waves");
    static_assert(C::NS * SBYTES <= 131072 && C::NS <= NT, "LDS budget");
    static constexpr int a_off(int k) {
        if (PH == PH_WO) return (k < 4 ? 32768 : 65536) + k * 8192;
        if (PH == PH_DN && k >= 8) return (((k - 8) / 2 + 1) % 8) * SBYTES + ((k - 8) & 1) * ABYTES;
        return ((k + (PH == PH_QKV ? 6 : PH == PH_DN ? 1 : 0)) % C::NS) * SBYTES;
    }
    static constexpr int b_off(int k) { return PH == PH_WO ? (k < 4 ? 0 : 32768) + k * 8192 : a_off(k) + ABYTES; }
    static constexpr bool held(int k) { return PH == PH_DN && k >= 8; }
    static constexpr bool uses(int k, int lo, int hi) { return (lo < a_off(k) + ABYTES && a_off(k) < hi) || (!held(k) && lo < b_off(k) + BBYTES && b_off(k) < hi); }
    static constexpr bool SWAPIN = PH == PH_DN || PH == PH_QKV;
    static DI void tile(int b, int& rb, int& cb) {
        const int x = b & 7, i = b >> 3;
        constexpr int CH = C::NCB / C::XC, RH = NRB * C::XC / 8;
        rb = (x / C::XC) * RH + i / CH; cb = (x % C::XC) * CH + i % CH;
    }
    static DI size_t packed_off(int cb, int pan, int kb) {
        return (size_t)WP + (size_t)cb * (NT * BBYTES) + (size_t)((kb * 32) / C::BK) * BBYTES + pan * PBYTES + ((kb * 32) % C::BK) * 128;
    }
};

DI unsigned tr_lane_off(int lane, int j) {
    const int hh = lane >> 5, blk = (lane >> 4) & 1, q = (lane & 15) >> 2, pp = lane & 3, pi = ((pp & 1) << 1) | (pp >> 1);
    const int ch = (4 * j + 2 * blk + (pi >> 1)) ^ (((q >> 1) & 1) << 2);
    return (8 * hh + q) * 128 + ch * 16 + 8 * (pi & 1);
}
DI int slot_col(int r32) { const int pp = (r32 >> 2) & 3; return (r32 & 16) + 4 * (((pp & 1) << 1) | (pp >> 1)) + (r32 & 3); }
DI bf16x8 corr_frag(float v, bool second_lo, int hh) {
    const unsigned h = pack_bf16(v, 0.f) & 0xffffu, l = pack_bf16(v - bf_lo(h), 0.f) & 0xffffu;
    u32x4 u; u[0] = second_lo ? (h | (l << 16)) : (h | (h << 16)); u[1] = second_lo ? h : l; u[2] = 0; u[3] = 0;
    if (hh) { u[0] = 0; u[1] = 0; }
    return __builtin_bit_cast(bf16x8, u);
}

DI f32x2 row_stat(const f32x2 (&p)[8]) {
    const f32x2 t = ((p[0] + p[1]) + (p[2] + p[3])) + ((p[4] + p[5]) + (p[6] + p[7]));
    const float mean = half_sum(t.x) * (1.0f / DM), var = fmaxf(half_sum(t.y) * (1.0f / DM) - mean * mean, 0.f);
    return f32x2{mean, __builtin_amdgcn_rsqf(var + LN_EPS)};
}

#define FRESH(ws, lds0, w, lane, b) int b = blockIdx.x; asm volatile("" : "+s"(ws), "+s"(lds0), "+s"(w), "+s"(b)); asm volatile("" : "+v"(lane))

enum { OP_A, OP_F, OP_X, OP_W, OP_N, OP_E, OP_R };
struct Sched { int n; int op[128]; int arg[128]; int pend[128]; int nn[128]; int xw; };
template <int PH, int NX> constexpr Sched make_sched() {
    using G_ = Geo<PH>; using N_ = Geo<NX>;
    Sched sc{}; int cnt = 0, ncnt = 0, lastop[64] = {}, xpos = 0; bool ndone[8] = {};
    int next = 0;
    auto push = [&](int op, int arg) { sc.op[sc.n] = op; sc.arg[sc.n] = arg; sc.pend[sc.n] = 0; ++sc.n; };
    auto item = [&](int s) -> int {
        if (next < G_::NS) { push(OP_A, next); cnt += G_::GA; lastop[next] = cnt; ++next; return G_::GA; }
        if (next == G_::NS) { ++next; if (G_::AUX) { push(OP_X, 0); cnt += G_::AUX; xpos = cnt; return G_::AUX; } }
        const int k = next - 1;
        bool ok = k < G_::NT && k <= s + 8;
        for (int j = s > 0 ? s - 1 : 0; j < k && ok; ++j) ok = !G_::uses(j, G_::a_off(k), G_::a_off(k) + G_::ABYTES);
        if (ok) { const int g = G_::held(k) ? G_::GA : G_::G; push(G_::held(k) ? OP_A : OP_F, k); cnt += g; lastop[k] = cnt; ++next; return g; }
        return 0;
    };
    auto nextb = [&](int s) {
        if (PH == PH_QKV || (PH == PH_DN && s < G_::NT && (s & 1))) return;
        for (int j = 0; j < N_::NS; ++j) {
            if (ndone[j]) continue;
            bool ok = next > G_::NT;
            for (int k = s; k < G_::NT && ok; ++k) ok = !G_::uses(k, N_::b_off(j), N_::b_off(j) + N_::BBYTES);
            if (ok) { push(OP_N, j); cnt += N_::GB; ncnt += N_::GB; ndone[j] = true; if (PH == PH_DN && s < G_::NT) return; }
        }
    };
    for (int k = 0; k < G_::PA; ++k) item(0);
    sc.xw = -1;
    for (int s = 0; s < G_::NT; ++s) {
        push(OP_W, s); sc.pend[sc.n - 1] = cnt - lastop[s]; sc.nn[sc.n - 1] = ncnt;
        if (G_::AUX && sc.xw < 0 && xpos && lastop[s] > xpos) sc.xw = s;
        for (int q = 0; q < G_::G;) { const int r = item(s); if (!r) break; q += r; }
        if (G_::WK > 1 && s == G_::NT - 1) { push(OP_R, 0); nextb(G_::NT); }
        else nextb(s > 0 ? s - 1 : 0);
    }
    push(OP_E, 0);
    nextb(G_::NT);
    return sc;
}
template <int PH> struct NextPh { static constexpr int v = PH == PH_WO ? PH_GLU : PH == PH_GLU ? PH_DN : PH_QKV; };
template <int PH> struct SchedOf { static constexpr Sched v = make_sched<PH, NextPh<PH>::v>(); };

template <int PH> DI void issue_b(const char* ws, int k, int cb, unsigned lds0, int wid, unsigned vo) {
    using G_ = Geo<PH>;
    glds_run<G_::GB>(ws + G_::WP + (size_t)cb * (G_::NT * G_::BBYTES) + k * G_::BBYTES + wid * G_::GB * 1024, vo, lds0 + G_::b_off(k) + wid * G_::GB * 1024);
}
template <int PH> DI void prefetch_b(char* ws, unsigned lds0, int wid, int lane) {
    FRESH(ws, lds0, wid, lane, bid);
    int rb, cb; Geo<PH>::tile(bid, rb, cb);
#pragma unroll
    for (int k = 0; k < Geo<PH>::NS; ++k) issue_b<PH>(ws, k, cb, lds0, wid, lane * 16);
}

DI void first_load(const char* x0, const char* wqkv, unsigned lds0, int wid, int lane) {
    using G_ = Geo<PH_QKV>;
    int rb, cb; G_::tile(blockIdx.x, rb, cb);
    const int rot = 2 * ((rb + cb) & 3);
    const char* sA = x0 + (size_t)(rb * 64 + wid * 16) * 1024;
    const unsigned vA0 = (lane >> 3) * 1024 + (((lane & 7) ^ (lane >> 4)) << 4), vA1 = 8192 + (vA0 ^ 64);
    const unsigned vB = (lane >> 3) * 3072 + (((lane & 7) ^ (((lane >> 4) & 1) << 2)) << 4);
    const char* sB[3];
#pragma unroll
    for (int g = 0; g < 3; ++g) { const int b = wid * 3 + g; sB[g] = wqkv + (size_t)(b & 3) * (8 * 3072) + (b >> 2) * 1024 + cb * 128; }
    auto issue = [&](int j) {
        const int jj = (j + rot) & 7;
        const unsigned da = lds0 + G_::a_off(2 * j) + (wid >> 1) * G_::SBYTES + (wid & 1) * 2048;
        glds_in(sA + jj * 128, vA0, da); glds_in(sA + jj * 128, vA1, da + 1024);
#pragma unroll
        for (int h = 0; h < 2; ++h)
#pragma unroll
            for (int g = 0; g < 3; ++g) glds_in(sB[g] + (2 * jj + h) * (32 * 3072), vB, lds0 + G_::b_off(2 * j + h) + (wid * 3 + g) * 1024);
    };
    struct S { static constexpr int issued(int s) { int a = 2 + s, b = s < 2 ? 4 : (s + G_::NS - 2) / 2; a = a < b ? a : b; return a < 8 ? a : 8; } };
    issue(0);
    asm volatile("s_barrier" ::: "memory");
    issue(1);
    static_for<G_::NT>([&](auto sc) {
        constexpr int s = decltype(sc)::value;
        wait_vm_barrier<8 * (S::issued(s) - 1 - s / 2) + (s & 1 ? 0 : 3)>();
#pragma unroll
        for (int j = S::issued(s); j < S::issued(s + 1); ++j) issue(j);
    });
}

template <int PH> DI void gemm_load(char* ws, int layer, unsigned lds0, int wid, int lane, bool no_next, u64 tag, bool poller) {
    using G_ = Geo<PH>;
    constexpr int NX = NextPh<PH>::v;
    constexpr int MT = G_::MT, NT = G_::NT, KS = G_::KS, GA = G_::GA;
    FRESH(ws, lds0, wid, lane, bid);
    int rb, cb, nrb, ncb; G_::tile(bid, rb, cb); Geo<NX>::tile(bid, nrb, ncb);
    const unsigned lw = layer * WS_LAYER;
    const unsigned vo = lane * 16;
    const char* sA = ws + lw + G_::A + (size_t)(rb * MT + (wid * GA) / KS) * (64 * G_::KTOT) + ((wid * GA) % KS) * 1024;
    if constexpr (G_::SWAPIN) dep_barrier(poller, ws, lw + WS_FL + PH * FL_PH + (rb * G_::NCB + cb) * FR, PH == PH_QKV ? 8 : 14, tag, lane);
    else asm volatile("s_barrier" ::: "memory");
    f32x2 stp[8];
    static_for<SchedOf<PH>::v.n>([&](auto ic) {
        constexpr int i = decltype(ic)::value, op = SchedOf<PH>::v.op[i], k = SchedOf<PH>::v.arg[i];
        if constexpr (op == OP_A || op == OP_F) glds_run<GA>(sA + k * (KS * 1024), vo, lds0 + G_::a_off(k) + wid * GA * 1024);
        if constexpr (op == OP_F) issue_b<PH>(ws, k, cb, lds0, wid, vo);
        if constexpr (op == OP_N) { if (!no_next) issue_b<NX>(ws, k, ncb, lds0, wid, vo); }
        if constexpr (op == OP_X) {
            const char* sp = ws + lw + WS_STB + (size_t)(rb * MT + wid % MT) * 4096;
            const unsigned so = (lane >> 5) * 2048 + (lane & 31) * 8;
#define SL(i, o) "\n\tglobal_load_dwordx2 %" #i ", %8, %9 offset:" #o " sc1"
            asm volatile("s_nop 4" SL(0, 0) SL(1, 256) SL(2, 512) SL(3, 768) SL(4, 1024) SL(5, 1280) SL(6, 1536) SL(7, 1792)
                         : "=&v"(stp[0]), "=&v"(stp[1]), "=&v"(stp[2]), "=&v"(stp[3]), "=&v"(stp[4]), "=&v"(stp[5]), "=&v"(stp[6]), "=&v"(stp[7]) : "v"(so), "s"(sp) : "memory");
#undef SL
        }
        if constexpr (op == OP_W) {
            if constexpr (G_::AUX && k == SchedOf<PH>::v.xw + 1) {
                asm volatile("" : "+v"(stp[0]), "+v"(stp[1]), "+v"(stp[2]), "+v"(stp[3]), "+v"(stp[4]), "+v"(stp[5]), "+v"(stp[6]), "+v"(stp[7]));
                const f32x2 st = row_stat(stp);
                if (lane < 32) ((LAS f32x2*)(uintptr_t)(lds0 + G_::ROWF))[(wid % MT) * 32 + lane] = st;
            }
            static_assert(SchedOf<PH>::v.pend[i] <= 63, "vmcnt");
            if (SchedOf<PH>::v.nn[i] && no_next) wait_vm_barrier<SchedOf<PH>::v.pend[i] - SchedOf<PH>::v.nn[i]>();
            else wait_vm_barrier<SchedOf<PH>::v.pend[i]>();
        }
        if constexpr (op == OP_R) asm volatile("s_barrier" ::: "memory");
        if constexpr (op == OP_E && (PH == PH_WO || PH == PH_GLU)) lds_barrier();
    });
}

DI void load_unit(u32x4 (&v)[4], const char* src, int ld2, int lane) {
    const GAS char* a = (const GAS char*)src + (size_t)(lane >> 3) * ld2 + (lane & 7) * 16;
#pragma unroll
    for (int i = 0; i < 4; ++i) v[i] = *(const GAS u32x4*)(a + (size_t)i * 8 * ld2);
}
DI void store_unit_packed(const u32x4 (&v)[4], rsrc_t rws, unsigned dst, int lane) {
#pragma unroll
    for (int i = 0; i < 4; ++i) st16(rws, ((lane ^ (((lane >> 4) & 1) << 2)) << 4) + i * 1024, dst, v[i]);
}
DI void unit_colsum(const u32x4 (&v)[4], unsigned lds_dst, int lane) {
    float c[8];
#pragma unroll
    for (int e = 0; e < 4; ++e) {
        c[2 * e] = (bf_lo(v[0][e]) + bf_lo(v[1][e])) + (bf_lo(v[2][e]) + bf_lo(v[3][e]));
        c[2 * e + 1] = (bf_hi(v[0][e]) + bf_hi(v[1][e])) + (bf_hi(v[2][e]) + bf_hi(v[3][e]));
    }
#pragma unroll
    for (int e = 0; e < 4; ++e) { auto r = __builtin_amdgcn_permlane32_swap(__float_as_uint(c[e]), __float_as_uint(c[e + 4]), false, false); c[e] = __uint_as_float(r[0]) + __uint_as_float(r[1]); }
#pragma unroll
    for (int e = 0; e < 2; ++e) { auto r = __builtin_amdgcn_permlane16_swap(__float_as_uint(c[e]), __float_as_uint(c[e + 2]), false, false); c[e] = __uint_as_float(r[0]) + __uint_as_float(r[1]); }
    const bool b3 = lane & 8;
    const float t = (b3 ? c[1] : c[0]) + dpp<0x128>(b3 ? c[0] : c[1]);
    *(LAS float*)(uintptr_t)(lds_dst + ((lane & 7) * 8 + (lane >> 3)) * 4) = t;
}
DI void store_colsum(unsigned lds_src, rsrc_t rws, unsigned dst, int lane) {
    const LAS float* r = (const LAS float*)(uintptr_t)lds_src + lane;
    __builtin_amdgcn_raw_buffer_store_b32(__float_as_uint((r[0] + r[64]) + (r[128] + r[192])), rws, dst + lane * 4, 0, 16);
}
DI rsrc_t make_rsrc(const void* p, unsigned bytes) { return __builtin_amdgcn_make_buffer_rsrc((void*)p, 0, bytes, 0x00020000); }

struct MkArgs { const char* x; const char* Wqkv; const char* Wo; const char* Wg; const char* Wu; const char* Wd; char* ws; char* out; };

DI void startup_load(const MkArgs& p, int cw, int lane, u32x4 (&v)[4]) {
    const int bid = blockIdx.x < 224 ? blockIdx.x : 0;
    const char* src = bid < 128 ? p.x + (size_t)(bid >> 1) * 32768 + ((bid & 1) * 4 + cw) * 128 : p.Wqkv + (size_t)((bid & 3) * 4 + cw) * 32 * 3072 + ((bid - 128) >> 2) * 128;
    load_unit(v, src, bid < 128 ? 1024 : 3072, lane);
}
DI bool startup_job(rsrc_t rws, unsigned lds0, int cw, int lane, const u32x4 (&v)[4]) {
    const int bid = blockIdx.x;
    if (bid >= 224) return false;
    if (bid < 128) {
#pragma unroll
        for (int i = 0; i < 4; ++i) {
            float sum = 0.f, sq = 0.f;
#pragma unroll
            for (int j = 0; j < 4; ++j) { const float lo = bf_lo(v[i][j]), hi = bf_hi(v[i][j]); sum += lo + hi; sq = fmaf(lo, lo, fmaf(hi, hi, sq)); }
            sum += dpp<0xB1>(sum); sq += dpp<0xB1>(sq);
            sum += dpp<0x4E>(sum); sq += dpp<0x4E>(sq);
            if ((lane & 3) == 0) *(LAS f32x2*)(uintptr_t)(lds0 + AT_Q + cw * 512 + (i * 8 + (lane >> 3)) * 8 + ((lane >> 2) & 1) * 256) = f32x2{sum, sq};
        }
    } else unit_colsum(v, lds0 + AT_Q + cw * 256, lane);
    if (arrive(lds0 + LDS_CNT, lane) != 3) return false;
    if (bid < 128) {
#pragma unroll
        for (int e = 0; e < 2; ++e) st16(rws, lane * 16 + e * 1024, WS_STA + bid * 2048, *(LAS u32x4*)(uintptr_t)(lds0 + AT_Q + e * 1024 + lane * 16));
    } else store_colsum(lds0 + AT_Q, rws, WS_CSQ + (bid - 128) * 256, lane);
    return true;
}
DI void startup_signal(char* ws, int bid, int lane, u64 tag) {
    asm volatile("s_waitcnt vmcnt(0)" ::: "memory");
    if (bid < 128) signal(ws, WS_FL + PH_QKV * FL_PH + (bid >> 2) * 8 * FR, 8, 1, bid & 3, tag, lane);
    else { const int pan = (bid - 128) >> 2; signal(ws, WS_FL + PH_QKV * FL_PH + (pan & 7) * FR, 32, 8, 4 + (pan >> 3) * 4 + (bid & 3), tag, lane); }
}

template <int PH> DI void gemm_comp(char* ws, rsrc_t rws, rsrc_t rio, int layer, unsigned lds0, int cw, int lane, u64 tag, bool& poller, const bf16x8 (&wd)[40]) {
    using G_ = Geo<PH>;
    constexpr int MT = G_::MT, WN = G_::WN, NS = G_::NS, NT = G_::NT, KS = G_::KS, KG = G_::KG, UPS = G_::UPS, NU = G_::NU, TM = G_::TM, TN = G_::TN;
    constexpr int PBYTES = G_::PBYTES;
    constexpr bool LN = G_::LN;
    constexpr int CORR_U = (PH == PH_QKV ? 15 : 10) * UPS, SIG_U = 3, POLL_U = 9, STAT_U = 12;
    static_assert(!G_::AUX || (SchedOf<PH>::v.xw >= 0 && CORR_U >= (SchedOf<PH>::v.xw + 1) * UPS), "row table must be ready");
    FRESH(ws, lds0, cw, lane, bid);
    int rb, cb; G_::tile(bid, rb, cb);
    const unsigned lw = layer * WS_LAYER;
    const int r32 = lane & 31, hh = lane >> 5, wm = cw / WN, wn = cw % WN;
    u32x4 resid[2];
    f32x2 stp[8];
    float cpart[TN][2];
    auto load_cs = [&]() {
        constexpr int NPAN = PH == PH_QKV ? 8 : 28;
#pragma unroll
        for (int t = 0; t < TN; ++t)
#pragma unroll
            for (int e = 0; e < 2; ++e)
                cpart[t][e] = __uint_as_float(__builtin_amdgcn_raw_buffer_load_b32(rws, slot_col(r32) * 4 + hh * 512 + e * 256,
                                                                                   (PH == PH_QKV ? WS_CSQ : WS_CSG) + (t * NPAN + cb * (WN / 2) + (wn >> 1)) * 1024 + (wn & 1) * 128, 16));
    };
    bf16x8 wf[TN];
    auto make_wf = [&]() {
#pragma unroll
        for (int t = 0; t < TN; ++t) wf[t] = corr_frag(half_sum(cpart[t][0] + cpart[t][1]), false, hh);
    };
    auto load_res = [&]() {
        static_assert(Cfg<PH_WO>::XC == Cfg<PH_DN>::XC, "the residual tile stays with its workgroup");
        const bool raw = PH == PH_WO && layer == 0;
        const unsigned vo = (rb * MT + wm) * 32768 + (raw ? r32 * 1024 + hh * 16 + (cb * 8 + wn * 4) * 16 : lane * 16 + lw + (PH == PH_WO ? WS_XA : WS_XB) + (cb * 8 + wn * 4) * 512);
#pragma unroll
        for (int e = 0; e < 2; ++e) resid[e] = ld16(raw ? rio : rws, vo + e * (raw ? 32 : 1024), 0);
    };
    if constexpr (PH == PH_WO) load_res();
    auto load_st = [&]() {
#pragma unroll
        for (int k = 0; k < 8; ++k) stp[k] = __builtin_bit_cast(f32x2, __builtin_amdgcn_raw_buffer_load_b64(rws, hh * 2048 + r32 * 8 + k * 256, lw + WS_STA + (rb * MT + wm) * 4096, 16));
    };
    if (PH == PH_GLU || (PH == PH_QKV && layer)) load_cs();
    if constexpr (G_::SWAPIN) {
        if (PH != PH_QKV || layer) wait_vm_barrier<0>();
    } else dep_barrier(poller, ws, lw + WS_FL + PH * FL_PH + (rb * G_::NCB + cb) * FR, PH == PH_WO ? 32 : 16, tag, lane);
    if constexpr (PH == PH_DN) load_res();
    if (PH == PH_QKV && layer) load_st();
    LAS f32x2* rowf = (LAS f32x2*)(uintptr_t)(lds0 + G_::ROWF);
    constexpr bool KSP = G_::WK > 1;
    unsigned aoff = lds0 + (KSP ? wm * (KS * 1024 + 2048) : wm * TM * KS * 1024) + lane * 16;
    unsigned boff = lds0 + (wn >> 1) * PBYTES + tr_lane_off(lane, wn & 1) + (KSP ? wm * 4096 : 0);
    unsigned aoth = aoff + (1 - 2 * wm) * (KS * 1024), aoth2 = aoth + 65536, aown2 = aoff + 65536;
    if constexpr (KSP) asm volatile("" : "+v"(aoth), "+v"(aoth2), "+v"(aown2));
    unsigned aoff2 = aoff + 49152, boff2 = boff + 49152, aoff3 = aoff + 98304, boff3 = boff + 98304;
    asm volatile("" : "+v"(aoff2), "+v"(boff2), "+v"(aoff3), "+v"(boff3));
    unsigned aq[2][4];
    if constexpr (PH == PH_QKV) {
#pragma unroll
        for (int v = 0; v < 4; ++v) {
            aq[0][v] = layer ? aoff + (v >> 1) * G_::SBYTES + (v & 1) * 1024 : lds0 + wm * G_::SBYTES + r32 * 128 + (((2 * v + hh) ^ ((r32 >> 1) & 7)) << 4);
            aq[1][v] = aq[0][v] + 65536;
            asm volatile("" : "+v"(aq[0][v]), "+v"(aq[1][v]));
        }
    }
    constexpr int TSTR = (WN / 2) * PBYTES;

    f32x16 acc[TM][TN];
#pragma unroll
    for (int i = 0; i < TM; ++i)
#pragma unroll
        for (int t = 0; t < TN; ++t) acc[i][t] = f32x16{};
    if constexpr (!LN) {
#pragma unroll
        for (int r = 0; r < 16; r += 2) { acc[0][0][r] = bf_lo(resid[r >> 3][(r >> 1) & 3]); acc[0][0][r + 1] = bf_hi(resid[r >> 3][(r >> 1) & 3]); }
        asm volatile("" : "+v"(acc[0][0]));
    }
    if constexpr (PH == PH_GLU) { make_wf(); asm volatile("" : "+v"(wf[0]), "+v"(wf[1])); }
    float rstd[TM];
    u64 seen = 0;
    constexpr int NF = KG * (TM + TN), NM = KG * TM * TN;
    bf16x8 fr[2][NF];
    auto rd = [&](auto uc, auto fc) {
        constexpr int u = decltype(uc)::value, f = decltype(fc)::value;
        constexpr int ks = (u % UPS) * KG + f / (TM + TN), e = f % (TM + TN);
        constexpr int so = e < TN ? G_::b_off(u / UPS) : G_::a_off(u / UPS), hi = so / 49152, lo = so % 49152;
        if constexpr (e < TN && G_::held(u / UPS)) {}
        else if constexpr (e < TN) fr[u & 1][f] = lds_tr_frag((hi == 0 ? boff : hi == 1 ? boff2 : boff3) + lo + e * TSTR + ks * 2048);
        else if constexpr (KSP) fr[u & 1][f] = lds_b128((e == TN ? (so < 65536 ? aoff : aown2) : (so < 65536 ? aoth : aoth2)) + (so & 65535) + ks * 1024);
        else if constexpr (PH == PH_QKV) fr[u & 1][f] = lds_b128(aq[G_::a_off(u & ~1) >> 16][(u & 1) * 2 + ks] + (G_::a_off(u & ~1) & 65535));
        else fr[u & 1][f] = lds_b128((hi == 0 ? aoff : hi == 1 ? aoff2 : aoff3) + lo + ((e - TN) * KS + ks) * 1024);
    };
    auto mma = [&](auto tc, f32x16& c, const bf16x8& af, const bf16x8& bf) {
        if constexpr (PH == PH_QKV && decltype(tc)::value == 2) c = MFMA32(af, bf, c);
        else c = MFMA32(bf, af, c);
    };
    lds_barrier();
    static_for<NF>([&](auto fc) { rd(std::integral_constant<int, 0>{}, fc); });
    SBAR();
    static_for<NU>([&](auto uc) {
        constexpr int u = decltype(uc)::value;
        if constexpr (PH == PH_QKV && u == SIG_U) { if (layer == 0 && __builtin_amdgcn_readfirstlane((int)poller)) { __builtin_amdgcn_s_setprio(3); startup_signal(ws, bid, lane, tag); __builtin_amdgcn_s_setprio(0); } SBAR(); }
        if constexpr (PH == PH_QKV && u == POLL_U) {
            if (layer == 0) {
                __builtin_amdgcn_s_setprio(3);
                seen = __hip_atomic_load((const GAS u64*)(ws + WS_FL + PH * FL_PH + (rb * G_::NCB + cb) * FR + (lane & 15) * TS), __ATOMIC_RELAXED, __HIP_MEMORY_SCOPE_AGENT);
                __builtin_amdgcn_s_setprio(0);
            }
            SBAR();
        }
        if constexpr (PH == PH_QKV && u == STAT_U) {
            if (layer == 0) {
                __builtin_amdgcn_s_setprio(3);
                if (!__all(seen == tag)) wait_tags(ws, WS_FL + PH * FL_PH + (rb * G_::NCB + cb) * FR, 16, tag, lane);
                asm volatile("" ::: "memory");
                load_st(); load_cs();
                __builtin_amdgcn_s_setprio(0);
            }
            SBAR();
        }
        if constexpr (LN && u == CORR_U) {
            if constexpr (PH == PH_QKV) make_wf();
#pragma unroll
            for (int i = 0; i < TM; ++i) {
                f32x2 st;
                if constexpr (PH == PH_QKV) {
                    st = row_stat(stp);
                    if (hh == 0) rowf[cw * 32 + r32] = st;
                } else st = rowf[(wm * TM + i) * 32 + r32];
                rstd[i] = st.y;
                const bf16x8 xf = corr_frag(-st.x, true, hh);
                static_for<TN>([&](auto tc) { mma(tc, acc[i][decltype(tc)::value], xf, wf[decltype(tc)::value]); });
            }
            SBAR();
        }
        if constexpr (u + 1 < NU && (u + 1) % UPS == 0) { asm volatile("s_barrier" ::: "memory"); SBAR(); }
        static_for<NM>([&](auto mc) {
            constexpr int m = decltype(mc)::value, g = m / (TM * TN), i = (m % (TM * TN)) / TN, t = m % TN;
            if constexpr (G_::held(u / UPS)) mma(std::integral_constant<int, t>{}, acc[i][t], fr[u & 1][g * (TM + TN) + TN + i], wd[(u - 8) * 2 + g]);
            else mma(std::integral_constant<int, t>{}, acc[i][t], fr[u & 1][g * (TM + TN) + TN + i], fr[u & 1][g * (TM + TN) + t]);
            if constexpr (u + 1 < NU) {
                SBAR();
                constexpr int PER = PH == PH_GLU ? 1 : 4;
                static_for<PER>([&](auto kc) {
                    constexpr int f = m * PER + decltype(kc)::value;
                    if constexpr (f < NF) rd(std::integral_constant<int, u + 1>{}, std::integral_constant<int, f>{});
                });
                SBAR();
            }
        });
    });

    if constexpr (KSP) {
        auto xbuf = [&](int w) { return lds0 + (w < 2 ? AT_Q : LDS_X) + (w & 1) * 4096 + lane * 16; };
#pragma unroll
        for (int g = 0; g < 4; ++g) *(LAS f32x4*)(uintptr_t)(xbuf(cw) + g * 1024) = f32x4{acc[1][0][4 * g], acc[1][0][4 * g + 1], acc[1][0][4 * g + 2], acc[1][0][4 * g + 3]};
        lds_barrier();
#pragma unroll
        for (int g = 0; g < 4; ++g) {
            const f32x4 o = *(LAS f32x4*)(uintptr_t)(xbuf(cw ^ 2) + g * 1024);
#pragma unroll
            for (int j = 0; j < 4; ++j) acc[0][0][4 * g + j] += o[j];
        }
    }
    if constexpr (PH == PH_QKV) {
        const int tile = rb * MT + wm;
        const int head0 = cb * 4 + wn * 2;
        const float rq = rstd[0] * QSCALE, rk = rstd[0];
#pragma unroll
        for (int e = 0; e < 2; ++e) {
            u32x4 q, k;
#pragma unroll
            for (int j = 0; j < 4; ++j) {
                q[j] = pack_bf16(rq * acc[0][0][8 * e + 2 * j], rq * acc[0][0][8 * e + 2 * j + 1]);
                k[j] = pack_bf16(rk * acc[0][1][8 * e + 2 * j], rk * acc[0][1][8 * e + 2 * j + 1]);
            }
            st16(rws, lane * 16, lw + WS_KV + ((head0 + e) * 128 + tile) * 1024, k);
            st16(rws, lane * 16, lw + WS_Q + ((head0 + e) * 64 + tile) * 1024, q);
        }
#pragma unroll
        for (int e = 0; e < 2; ++e) {
            u32x4 o;
#pragma unroll
            for (int j = 0; j < 4; ++j) {
                const f32x4 st = *(LAS f32x4*)&rowf[cw * 32 + 16 * e + 8 * (j >> 1) + 4 * hh + 2 * (j & 1)];
                o[j] = pack_bf16(st[1] * acc[0][2][8 * e + 2 * j], st[3] * acc[0][2][8 * e + 2 * j + 1]);
            }
            st16(rws, (r32 >> 4) * 131072 + e * 512 + hh * 256 + (r32 & 15) * 16, lw + WS_KV + (head0 * 128 + 64 + tile) * 1024, o);
        }
        poller = publish(ws, lw + WS_FL + PH_ATT * FL_PH + cb * 32 * FR, 32, 1, rb, tag, lds0 + LDS_CNT, lane);
    } else if constexpr (PH == PH_GLU) {
#pragma unroll
        for (int i = 0; i < TM; ++i) {
            const float r2 = rstd[i] * rstd[i];
#pragma unroll
            for (int e = 0; e < 2; ++e) {
                u32x4 o;
#pragma unroll
                for (int j = 0; j < 4; ++j) {
                    const int r = 8 * e + 2 * j;
                    o[j] = pack_bf16(fmaxf(r2 * acc[i][0][r], 0.f) * acc[i][1][r], fmaxf(r2 * acc[i][0][r + 1], 0.f) * acc[i][1][r + 1]);
                }
                st16(rws, lane * 16 + e * 1024, lw + WS_H + (rb * MT + i) * (64 * FF) + (cb * 16 + wn * 4) * 512, o);
            }
        }
        lds_barrier();
        poller = publish(ws, lw + WS_FL + PH_DN * FL_PH + rb * 16 * FR, 16, 1, cb, tag, lds0 + LDS_CNT, lane);
    } else {
        const bool last = PH == PH_DN && layer == 1;
        const unsigned rbo = (rb * MT + wm) * 32768;
        u32x4 o[2];
#pragma unroll
        for (int e = 0; e < 2; ++e)
#pragma unroll
            for (int j = 0; j < 4; ++j) o[e][j] = pack_bf16(acc[0][0][8 * e + 2 * j], acc[0][0][8 * e + 2 * j + 1]);
        if (last) {
#pragma unroll
            for (int e = 0; e < 2; ++e) __builtin_amdgcn_raw_buffer_store_b128(o[e], rio, r32 * 1024 + hh * 16 + rbo + (cb * 8 + wn * 4) * 16 + e * 32, 0, 0);
        } else {
#pragma unroll
            for (int e = 0; e < 2; ++e) st16(rws, lane * 16 + e * 1024, (PH == PH_WO ? lw + WS_XB : WS_LAYER + WS_XA) + rbo + (cb * 8 + wn * 4) * 512, o[e]);
        }
        if (!last) {
            float sum[2] = {0.f, 0.f}, sq[2] = {0.f, 0.f};
#pragma unroll
            for (int r = 0; r < 16; ++r) { sum[r & 1] += acc[0][0][r]; sq[r & 1] = fmaf(acc[0][0][r], acc[0][0][r], sq[r & 1]); }
            const float a = half_sum(sum[0] + sum[1]), b = half_sum(sq[0] + sq[1]);
            if (hh == 0) __builtin_amdgcn_raw_buffer_store_b64(u32x2{__float_as_uint(a), __float_as_uint(b)}, rws,
                                                               r32 * 8 + (PH == PH_WO ? lw + WS_STB : WS_LAYER + WS_STA) + (rb * MT + wm) * 4096 + cb * 512 + wn * 256, 0, 16);
        }
        if constexpr (!KSP) lds_barrier();
        if (!last) {
            if constexpr (PH == PH_WO) poller = publish(ws, lw + WS_FL + PH_GLU * FL_PH + (rb >> 1) * 14 * FR, 14, 1, (rb & 1) * 8 + cb, tag, lds0 + LDS_CNT, lane);
            else poller = publish(ws, WS_LAYER + WS_FL + PH_QKV * FL_PH + rb * 8 * FR, 8, 1, cb, tag, lds0 + LDS_CNT, lane);
        }
    }
}

#define AT_E(C, i) "v_exp_f32 v[" #C "+" #i "], v[" #C "+" #i "]\n\t"
#define AT_C(P, C, i) "v_cvt_pk_bf16_f32 v[" #P "], v[" #C "+" #i "], v[" #C "+" #i "+1]\n\t"
#define AT_HALF(C, O, V0, PRE, MID, POST)                                                                         \
    AT_E(C, 0) AT_E(C, 1) PRE AT_E(C, 2) AT_E(C, 3) AT_E(C, 4) AT_E(C, 5) MID                                     \
    AT_E(C, 6) AT_E(C, 7) AT_C(108, C, 0) AT_C(109, C, 2) AT_C(110, C, 4) AT_E(C, 8) AT_C(111, C, 6) AT_E(C, 9) AT_E(C, 10) \
    "v_mfma_f32_32x32x16_bf16 " O ", v[" #V0 ":" #V0 "+3], v[108:111], " O "\n\t" POST                            \
    AT_E(C, 11) AT_E(C, 12) AT_E(C, 13) AT_E(C, 14) AT_E(C, 15)                                                   \
    AT_C(112, C, 8) AT_C(113, C, 10) AT_C(114, C, 12) AT_C(115, C, 14)
#define AT_TILE(U, V0C, V0N, SYNC)                                                                                \
    AT_HALF(64, "%[o0]", V0C,                                                                                     \
            "v_mfma_f32_32x32x16_bf16 %[o1], v[104:107], v[112:115], %[o1]\n\t"                                   \
            "ds_read_b128 v[104:107], %[va] offset:" #U "*1024+512\n\t",                                          \
            "v_mfma_f32_32x32x16_bf16 v[80:95], v[96:99], %[q1], %[nm1]\n\t" SYNC                                 \
            "ds_read_b128 v[96:99], %[ka] offset:" #U "*1024+1024\n\t"                                            \
            "ds_read_b128 v[" #V0N ":" #V0N "+3], %[va] offset:" #U "*1024+1024\n\t",                             \
            "s_waitcnt lgkmcnt(2)\n\t")                                                                           \
    AT_HALF(80, "%[o1]", V0C,                                                                                     \
            "v_mfma_f32_32x32x16_bf16 %[o0], v[104:107], v[112:115], %[o0]\n\t"                                   \
            "s_waitcnt lgkmcnt(0)\n\t",                                                                           \
            "v_mfma_f32_32x32x16_bf16 v[64:79], v[96:99], %[q0], %[nm0]\n\t", "")

DI f32x16 neg_tile_max(const bf16x8& k0, const bf16x8& q) {
    const f32x16 s0 = MFMA32(k0, q, f32x16{});
    float m = s0[0];
#pragma unroll
    for (int r = 1; r < 16; ++r) m = fmaxf(m, s0[r]);
    m = half_max(m);
    f32x16 n;
#pragma unroll
    for (int r = 0; r < 16; ++r) n[r] = -m;
    return n;
}
DI f32x16 attn_exact(const bf16x8& q, rsrc_t rws, unsigned kv, unsigned ones, int lane) {
    const bool vlane = (lane & 31) < 16;
    const bf16x8 pad = lds_b128(ones);
    float m = -INFINITY;
    f32x16 o = f32x16{};
#pragma unroll 1
    for (int t = 0; t < 64; ++t) {
        const bf16x8 kf = __builtin_bit_cast(bf16x8, ld16(rws, lane * 16, kv + t * 1024));
        const unsigned vo = (lane >> 5) * 256 + (lane & 15) * 16;
        bf16x8 v0 = __builtin_bit_cast(bf16x8, ld16(rws, vo, kv + 65536 + t * 1024)), v1 = __builtin_bit_cast(bf16x8, ld16(rws, vo + 512, kv + 65536 + t * 1024));
        if (!vlane) { v0 = pad; v1 = pad; }
        const f32x16 s = MFMA32(kf, q, f32x16{});
        float tm = s[0];
#pragma unroll
        for (int r = 1; r < 16; ++r) tm = fmaxf(tm, s[r]);
        const float mn = fmaxf(m, half_max(tm));
        const float alpha = __builtin_amdgcn_exp2f(m - mn);
        m = mn;
#pragma unroll
        for (int r = 0; r < 16; ++r) o[r] *= alpha;
        u32x4 p0, p1;
#pragma unroll
        for (int j = 0; j < 4; ++j) {
            p0[j] = pack_bf16(__builtin_amdgcn_exp2f(s[2 * j] - m), __builtin_amdgcn_exp2f(s[2 * j + 1] - m));
            p1[j] = pack_bf16(__builtin_amdgcn_exp2f(s[8 + 2 * j] - m), __builtin_amdgcn_exp2f(s[8 + 2 * j + 1] - m));
        }
        o = MFMA32(v0, __builtin_bit_cast(bf16x8, p0), o);
        o = MFMA32(v1, __builtin_bit_cast(bf16x8, p1), o);
    }
    return o;
}

DI void attn_load(const MkArgs& p, rsrc_t rws, int layer, unsigned lds0, int wid, int lane, u64 tag, bf16x8 (&wd)[40]) {
    char* ws = p.ws;
    FRESH(ws, lds0, wid, lane, bid);
    const int x = bid & 7, idx = bid >> 3, head = x * 4 + (idx >> 3), qb = idx & 7;
    const unsigned lw = layer * WS_LAYER;
    const unsigned vo = lane * 16;
    const char* src = ws + lw + WS_KV + (size_t)head * 131072 + wid * 2048;
    auto chunk = [&](int c) {
        const char* s = src + ((qb + c) & 7) * 8192;
        glds_run<2>(s, vo, lds0 + AT_K + c * 8192 + wid * 2048);
        glds_run<2>(s + 65536, vo, lds0 + AT_V + c * 8192 + wid * 2048);
    };
    asm volatile("s_barrier" ::: "memory");
    glds_run<2>(ws + lw + WS_Q + (size_t)(head * 64 + qb * 8 + wid * 2) * 1024, vo, lds0 + AT_Q + wid * 2048);
    chunk(0);
    wait_vm_barrier<0>();
    wait_tags(ws, lw + WS_FL + PH_ATT * FL_PH + (head * 8 + qb) * FR, 32, tag, lane);
    chunk(1);
    chunk(2); chunk(3); chunk(4); chunk(5); chunk(6); chunk(7);
    wait_vm_barrier<0>();
    if (layer == 0) {
#pragma unroll 1
        for (int u = bid * 4 + wid; u < 1536; u += 1024) {
            const char* s; size_t dst; int ld2;
            if (u < 896) { const int mat = u >= 448, v = u - mat * 448, pan = v >> 4, kb = v & 15;
                s = (mat ? p.Wu : p.Wg) + (size_t)kb * 32 * (FF * 2) + pan * 128; ld2 = FF * 2; dst = Geo<PH_GLU>::packed_off(pan >> 1, mat * 2 + (pan & 1), kb); }
            else if (u < 1024) { const int v = u - 896, pan = v & 7, kb = v >> 3;
                s = p.Wo + (size_t)kb * 32 * 1024 + pan * 128; ld2 = 1024; dst = Geo<PH_WO>::packed_off(pan, 0, kb); }
            else if (u < 1152) { const int v = u - 1024, pan = v & 7, kb = v >> 3;
                s = p.Wd + (size_t)kb * 32 * 1024 + pan * 128; ld2 = 1024; dst = Geo<PH_DN>::packed_off(pan, 0, kb); }
            else { const int v = u - 1152, pan = v >> 4, kb = v & 15;
                s = p.Wqkv + (size_t)kb * 32 * 3072 + pan * 128; ld2 = 3072; dst = Geo<PH_QKV>::packed_off(pan & 7, pan >> 3, kb); }
            u32x4 v[4];
            load_unit(v, s, ld2, lane);
            store_unit_packed(v, rws, (unsigned)dst, lane);
            if (u < 896) unit_colsum(v, lds0 + LDS_ROWG + wid * 256, lane);
        }
        if (arrive(lds0 + LDS_CNT + 4, lane) == 3) {
            if (bid < 224) { store_colsum(lds0 + LDS_ROWG, rws, WS_CSG + bid * 256, lane); asm volatile("s_waitcnt vmcnt(0)" ::: "memory"); }
            if (lane == 0) __hip_atomic_store((GAS u64*)(ws + WS_FL + FL_PK + bid * 8), tag, __ATOMIC_RELAXED, __HIP_MEMORY_SCOPE_AGENT);
            __builtin_amdgcn_s_sleep(24);
#pragma unroll 1
            for (int i = 0; i < 4; ++i) wait_tags(ws, WS_FL + FL_PK + ((bid + i) & 3) * 512, 64, tag, lane);
        }
    }
    lds_barrier();
    const unsigned own = lds0 + Geo<PH_WO>::b_off(0) + (wid >> 1) * 65536 + (wid & 1) * 16384;
    if (layer == 0) {
        int rb, cb; Geo<PH_DN>::tile(bid, rb, cb);
        const char* sw = p.Wd + cb * 128 + (wid & 1) * 64 + (wid >> 1) * 32768;
        const unsigned vw = (lane >> 2) * 1024 + (lane & 3) * 16;
        const int pp = lane & 3;
        const unsigned ra = own + (8 * (lane >> 5) + ((lane & 15) >> 2)) * 64 + ((lane >> 4) & 1) * 32 + ((((pp & 1) << 1) | (pp >> 1)) << 3);
        typedef short v4i16_t __attribute__((ext_vector_type(4)));
#pragma unroll
        for (int r0 = 0; r0 < 40; r0 += 16) {
#pragma unroll
            for (int i = r0; i < r0 + 16 && i < 40; ++i) glds_in(sw + (size_t)((8 + (i >> 1)) * 64 + (i & 1) * 16) * 1024, vw, own + (i - r0) * 1024);
            asm volatile("s_waitcnt vmcnt(0)" ::: "memory");
#pragma unroll
            for (int i = r0; i < r0 + 16 && i < 40; ++i) {
                const v4i16_t lo = __builtin_amdgcn_ds_read_tr16_b64_v4i16((LAS v4i16_t*)(uintptr_t)(ra + (i - r0) * 1024));
                const v4i16_t hi = __builtin_amdgcn_ds_read_tr16_b64_v4i16((LAS v4i16_t*)(uintptr_t)(ra + (i - r0) * 1024 + 256));
                wd[i] = __builtin_shufflevector(lo, hi, 0, 1, 2, 3, 4, 5, 6, 7);
            }
#pragma unroll
            for (int i = r0; i < r0 + 16 && i < 40; ++i) asm volatile("" : "+v"(wd[i])::"memory");
        }
    }
    {
        int rb, cb; Geo<PH_WO>::tile(bid, rb, cb);
        static_assert(Geo<PH_WO>::b_off(2) == 16384 && Geo<PH_WO>::b_off(4) == 65536 && Geo<PH_WO>::b_off(6) == 81920, "");
#pragma unroll
        for (int i = 0; i < 4; ++i) glds_run<4>(ws + WP_O + cb * 65536 + wid * 16384 + i * 4096, vo, own + i * 4096);
    }
}

DI void attn_comp(char* ws, rsrc_t rws, int layer, unsigned lds0, int cw, int lane, u64 tag, bool& poller) {
    FRESH(ws, lds0, cw, lane, bid);
    const int x = bid & 7, idx = bid >> 3, head = x * 4 + (idx >> 3), qb = idx & 7;
    const unsigned lw = layer * WS_LAYER;
    dep_barrier(poller, ws, lw + WS_FL + PH_ATT * FL_PH + (head * 8 + qb) * FR + qb * 4 * TS, 4, tag, lane);
    const int r32 = lane & 31, hh = lane >> 5;
    const unsigned ka0 = lds0 + AT_K + lane * 16;
    const bool vlane = r32 < 16;
    const unsigned va0 = lds0 + (vlane ? AT_V : AT_ONES) + hh * 256 + (r32 & 15) * 16;
    const unsigned vstep = vlane ? 8192u : 0u;

    lds_barrier();
    const bf16x8 q0 = lds_b128(lds0 + AT_Q + cw * 2048 + lane * 16), q1 = lds_b128(lds0 + AT_Q + cw * 2048 + 1024 + lane * 16);
    const bf16x8 k0 = lds_b128(ka0);
    const f32x16 nm0 = neg_tile_max(k0, q0), nm1 = neg_tile_max(k0, q1);
    f32x16 o0 = f32x16{}, o1 = f32x16{};
    {
        unsigned ka = ka0, va = va0; int cnt, tmp;
        asm volatile(
            "ds_read_b128 v[96:99], %[ka]\n\t"
            "ds_read_b128 v[100:103], %[va]\n\t"
            "v_mov_b32 v104, 0\n\tv_mov_b32 v105, 0\n\tv_mov_b32 v106, 0\n\tv_mov_b32 v107, 0\n\t"
            "v_mov_b32 v112, 0\n\tv_mov_b32 v113, 0\n\tv_mov_b32 v114, 0\n\tv_mov_b32 v115, 0\n\t"
            "s_mov_b32 %[cnt], 0\n\t"
            "s_waitcnt lgkmcnt(0)\n\t"
            "v_mfma_f32_32x32x16_bf16 v[64:79], v[96:99], %[q0], %[nm0]\n\t"
            "s_nop 12\n\t"
            "1:\n\t"
            AT_TILE(0, 100, 116, "") AT_TILE(1, 116, 100, "") AT_TILE(2, 100, 116, "") AT_TILE(3, 116, 100, "")
            AT_TILE(4, 100, 116, "") AT_TILE(5, 116, 100, "") AT_TILE(6, 100, 116, "")
            AT_TILE(7, 116, 100, "s_and_b32 %[tmp], %[cnt], 3\n\ts_cbranch_scc1 2f\n\ts_barrier\n\t2:\n\t")
            "v_add_u32 %[ka], 0x2000, %[ka]\n\t"
            "v_add_u32 %[va], %[vstep], %[va]\n\t"
            "s_add_u32 %[cnt], %[cnt], 1\n\t"
            "s_cmp_lt_u32 %[cnt], 8\n\t"
            "s_cbranch_scc1 1b\n\t"
            "v_mfma_f32_32x32x16_bf16 %[o1], v[104:107], v[112:115], %[o1]\n\t"
            "s_nop 15\n\t"
            : [o0] "+v"(o0), [o1] "+v"(o1), [ka] "+v"(ka), [va] "+v"(va), [cnt] "=&s"(cnt), [tmp] "=&s"(tmp)
            : [q0] "v"(q0), [q1] "v"(q1), [nm0] "v"(nm0), [nm1] "v"(nm1), [vstep] "v"(vstep)
            : "memory", "scc", "v64", "v65", "v66", "v67", "v68", "v69", "v70", "v71", "v72", "v73", "v74", "v75", "v76", "v77", "v78", "v79",
              "v80", "v81", "v82", "v83", "v84", "v85", "v86", "v87", "v88", "v89", "v90", "v91", "v92", "v93", "v94", "v95",
              "v96", "v97", "v98", "v99", "v100", "v101", "v102", "v103", "v104", "v105", "v106", "v107",
              "v108", "v109", "v110", "v111", "v112", "v113", "v114", "v115", "v116", "v117", "v118", "v119");
    }
    if (__builtin_expect(__any(!(o0[8] < LMAX) || !(o1[8] < LMAX)), 0)) {
        o0 = attn_exact(q0, rws, lw + WS_KV + head * 131072, lds0 + AT_ONES + (r32 & 15) * 16, lane);
        o1 = attn_exact(q1, rws, lw + WS_KV + head * 131072, lds0 + AT_ONES + (r32 & 15) * 16, lane);
    }
    const float i0 = __builtin_amdgcn_rcpf(o0[8]), i1 = __builtin_amdgcn_rcpf(o1[8]);
    u32x4 w0, w1;
#pragma unroll
    for (int j = 0; j < 4; ++j) { w0[j] = pack_bf16(o0[2 * j] * i0, o0[2 * j + 1] * i0); w1[j] = pack_bf16(o1[2 * j] * i1, o1[2 * j + 1] * i1); }
    const int qtile = qb * 8 + cw * 2;
    st16(rws, lane * 16, lw + WS_O + qtile * 32768 + head * 1024, w0);
    st16(rws, lane * 16, lw + WS_O + (qtile + 1) * 32768 + head * 1024, w1);
    poller = publish(ws, lw + WS_FL + PH_WO * FL_PH + qb * 32 * FR, 32, 1, head, tag, lds0 + LDS_CNT, lane);
}

__global__ __launch_bounds__(512, 2) void mk_kernel(MkArgs p) {
    extern __shared__ __attribute__((aligned(16))) char smem[];
    const unsigned lds0 = (unsigned)(uintptr_t)(LAS char*)smem;
    const int tid = threadIdx.x, lane = tid & 63, wid = __builtin_amdgcn_readfirstlane(tid >> 6);
    const rsrc_t rws = make_rsrc(p.ws, WS_END);
    const bool glu = (blockIdx.x >> 3) < 28;
    const char* code;
    asm volatile("s_getpc_b64 %0" : "=s"(code));
    u64 epoch;
    asm volatile("s_load_dwordx2 %0, %1, 0x0" : "=s"(epoch) : "s"(p.ws + WS_FL + FL_EPOCH) : "memory");
    if (wid < 4) {
        first_load(p.x, p.Wqkv, lds0, wid, lane);
        asm volatile("s_waitcnt lgkmcnt(0)" : "+s"(epoch));
        const u64 tag = epoch + 1;
        const rsrc_t rout = make_rsrc(p.out, SEQ * DM * 2);
        bf16x8 wd[40];
        bool poller = false;
#pragma unroll 1
        for (int layer = 0; layer < 2; ++layer) {
            if (layer) gemm_load<PH_QKV>(p.ws, layer, lds0, wid, lane, false, tag, poller);
            attn_load(p, rws, layer, lds0, wid, lane, tag, wd);
            gemm_load<PH_WO>(p.ws, layer, lds0, wid, lane, !glu, tag, false);
            if (glu) gemm_load<PH_GLU>(p.ws, layer, lds0, wid, lane, false, tag, false);
            else prefetch_b<PH_DN>(p.ws, lds0, wid, lane);
            gemm_comp<PH_DN>(p.ws, rws, rout, layer, lds0, wid, lane, tag, poller, wd);
        }
    } else {
        const rsrc_t rx = make_rsrc(p.x, SEQ * DM * 2);
        bf16x8 none[40];
        __builtin_amdgcn_s_setprio(3);
        unsigned ct = 0;
        if (wid == 4 && blockIdx.x < 64 && lane < 58) ct = *(const GAS unsigned*)(code + (blockIdx.x >> 3) * (58 * 128) + lane * 128);
        u32x4 sv[4];
        startup_load(p, wid - 4, lane, sv);
        {
            const unsigned ov = ((tid & 15) == 0 || (tid & 15) == 4) ? 0x3f803f80u : 0u;
            *(LAS u32x4*)(uintptr_t)(lds0 + AT_ONES + (tid - 256) * 16) = u32x4{ov, ov, ov, ov};
            *(LAS u32x4*)(uintptr_t)(lds0 + AT_ONES + tid * 16) = u32x4{ov, ov, ov, ov};
            if (tid < 320) *(LAS u32x4*)(uintptr_t)(lds0 + AT_ONES + (tid + 256) * 16) = u32x4{ov, ov, ov, ov};
            if (tid == 256) *(LAS u32x2*)(uintptr_t)(lds0 + LDS_CNT) = u32x2{0, 0};
        }
        lds_barrier();
        asm volatile("" : "+v"(ct));
        asm volatile("" : "+v"(sv[0]), "+v"(sv[1]), "+v"(sv[2]), "+v"(sv[3]));
        bool poller = startup_job(rws, lds0, wid - 4, lane, sv);
        asm volatile("s_waitcnt lgkmcnt(0)" : "+s"(epoch));
        const u64 tag = epoch + 1;
        __builtin_amdgcn_s_setprio(0);
#pragma unroll 1
        for (int layer = 0; layer < 2; ++layer) {
            gemm_comp<PH_QKV>(p.ws, rws, rx, layer, lds0, wid - 4, lane, tag, poller, none);
            attn_comp(p.ws, rws, layer, lds0, wid - 4, lane, tag, poller);
            gemm_comp<PH_WO>(p.ws, rws, rx, layer, lds0, wid - 4, lane, tag, poller, none);
            if (glu) gemm_comp<PH_GLU>(p.ws, rws, rx, layer, lds0, wid - 4, lane, tag, poller, none);
            gemm_load<PH_DN>(p.ws, layer, lds0, wid - 4, lane, layer == 1, tag, poller);
        }
        if (blockIdx.x == 0 && tid == 256) __hip_atomic_store((GAS u64*)(p.ws + WS_FL + FL_EPOCH), tag, __ATOMIC_RELAXED, __HIP_MEMORY_SCOPE_AGENT);
    }
}

extern "C" void kernel_launch(void* const* d_in, const int* in_sizes, int n_in, void* d_out, int out_size, void* d_ws, size_t ws_size,
                              hipStream_t stream) {
    (void)hipFuncSetAttribute((const void*)mk_kernel, hipFuncAttributeMaxDynamicSharedMemorySize, LDS_BYTES);
    mk_kernel<<<256, 512, LDS_BYTES, stream>>>(MkArgs{(const char*)d_in[0], (const char*)d_in[1], (const char*)d_in[2], (const char*)d_in[4], (const char*)d_in[5],
                                                      (const char*)d_in[6], (char*)d_ws, (char*)d_out});
}
```

```cpp
#include <hip/hip_runtime.h>
#include <hip/hip_bf16.h>
#include <stdint.h>
#include <utility>

typedef short bf16x8 __attribute__((ext_vector_type(8)));
typedef float f32x16 __attribute__((ext_vector_type(16)));
typedef float f32x4 __attribute__((ext_vector_type(4)));
typedef float f32x2 __attribute__((ext_vector_type(2)));
typedef unsigned u32x4 __attribute__((ext_vector_type(4)));
typedef unsigned u32x2 __attribute__((ext_vector_type(2)));
typedef __bf16 bf16x2_t __attribute__((ext_vector_type(2)));
typedef __amdgpu_buffer_rsrc_t rsrc_t;
#define LAS __attribute__((address_space(3)))
#define GAS __attribute__((address_space(1)))
#define DI __device__ __forceinline__
#define MFMA32(a, b, c) __builtin_amdgcn_mfma_f32_32x32x16_bf16((a), (b), (c), 0, 0, 0)
#define SBAR() __builtin_amdgcn_sched_barrier(0)

constexpr int SEQ = 2048, DM = 512, FF = 1792;
constexpr float LN_EPS = 1e-5f;
constexpr float QSCALE = 0.25f * 1.4426950408889634f;

enum { PH_QKV = 0, PH_ATT = 1, PH_WO = 2, PH_GLU = 3, PH_DN = 4 };
#define LMAX 1.0e18f
constexpr unsigned MB = 1u << 20;
constexpr unsigned WS_LAYER = 20 * MB;
constexpr unsigned WS_Q = 0, WS_KV = 2 * MB, WS_O = 6 * MB, WS_XA = 8 * MB, WS_XB = 10 * MB, WS_H = 12 * MB, WS_STA = 19 * MB, WS_STB = WS_STA + MB / 4, WS_FL = WS_STB + MB / 4;
constexpr unsigned TS = 8, FR = 32 * TS;
constexpr unsigned FL_PH = 256 * FR, FL_PK = 5 * FL_PH, FL_EPOCH = FL_PK + 2048;
constexpr unsigned WS_WP = 2 * WS_LAYER;
constexpr unsigned WP_QKV = WS_WP, WP_O = WP_QKV + DM * 3 * DM * 2, WP_GU = WP_O + DM * DM * 2, WP_D = WP_GU + DM * FF * 4;
constexpr unsigned WS_CSQ = WP_D + FF * DM * 2, WS_CSG = WS_CSQ + 3 * DM * 16, WS_END = WS_CSG + 2 * FF * 16;
typedef unsigned long long u64;

template <int... I, class Fn> DI void static_for_impl(std::integer_sequence<int, I...>, Fn&& f) { (f(std::integral_constant<int, I>{}), ...); }
template <int N, class Fn> DI void static_for(Fn&& f) { static_for_impl(std::make_integer_sequence<int, N>{}, f); }

DI unsigned pack_bf16(float lo, float hi) { bf16x2_t p; p.x = (__bf16)lo; p.y = (__bf16)hi; return __builtin_bit_cast(unsigned, p); }
DI float bf_lo(unsigned u) { return __uint_as_float(u << 16); }
DI float bf_hi(unsigned u) { return __uint_as_float(u & 0xffff0000u); }
template <int CTRL> DI float dpp(float v) { return __uint_as_float(__builtin_amdgcn_update_dpp(0u, __float_as_uint(v), CTRL, 0xf, 0xf, true)); }
DI float half_sum(float v) {
    auto r = __builtin_amdgcn_permlane32_swap(__float_as_uint(v), __float_as_uint(v), false, false);
    return __uint_as_float(r[0]) + __uint_as_float(r[1]);
}
DI float half_max(float v) {
    auto r = __builtin_amdgcn_permlane32_swap(__float_as_uint(v), __float_as_uint(v), false, false);
    return fmaxf(__uint_as_float(r[0]), __uint_as_float(r[1]));
}
DI void st16(rsrc_t r, unsigned voff, unsigned soff, u32x4 v) { __builtin_amdgcn_raw_buffer_store_b128(v, r, voff + soff, 0, 16); }
DI u32x4 ld16(rsrc_t r, unsigned voff, unsigned soff) { return __builtin_amdgcn_raw_buffer_load_b128(r, voff, soff, 16); }
template <int N> DI void wait_vm_barrier() { asm volatile("s_waitcnt vmcnt(%0) lgkmcnt(0)\n\ts_barrier" ::"i"(N) : "memory"); }
DI void lds_barrier() { asm volatile("s_waitcnt lgkmcnt(0)\n\ts_barrier" ::: "memory"); }
DI bf16x8 lds_b128(unsigned addr) { return *(const LAS bf16x8*)(uintptr_t)addr; }
DI bf16x8 lds_tr_frag(unsigned addr) {
    typedef short v4i16_t __attribute__((ext_vector_type(4)));
    v4i16_t lo = __builtin_amdgcn_ds_read_tr16_b64_v4i16((LAS v4i16_t*)(uintptr_t)addr);
    v4i16_t hi = __builtin_amdgcn_ds_read_tr16_b64_v4i16((LAS v4i16_t*)(uintptr_t)(addr + 512));
    return __builtin_shufflevector(lo, hi, 0, 1, 2, 3, 4, 5, 6, 7);
}
template <int N> DI void glds_run(const char* sbase, unsigned voff, unsigned lds_dst) {
    static_assert(N >= 1 && N <= 4, "");
#define GL(o) "\n\tglobal_load_lds_dwordx4 %0, %1 offset:" #o " sc1"
    if constexpr (N == 1) asm volatile("s_mov_b32 m0, %2\n\ts_nop 3" GL(0)::"v"(voff), "s"(sbase), "s"(lds_dst) : "memory");
    if constexpr (N == 2) asm volatile("s_mov_b32 m0, %2\n\ts_nop 3" GL(0) GL(1024)::"v"(voff), "s"(sbase), "s"(lds_dst) : "memory");
    if constexpr (N == 3) asm volatile("s_mov_b32 m0, %2\n\ts_nop 3" GL(0) GL(1024) GL(2048)::"v"(voff), "s"(sbase), "s"(lds_dst) : "memory");
    if constexpr (N == 4) asm volatile("s_mov_b32 m0, %2\n\ts_nop 3" GL(0) GL(1024) GL(2048) GL(3072)::"v"(voff), "s"(sbase), "s"(lds_dst) : "memory");
#undef GL
}
DI void glds_in(const char* sbase, unsigned voff, unsigned lds_dst) {
    asm volatile("s_mov_b32 m0, %2\n\ts_nop 3\n\tglobal_load_lds_dwordx4 %0, %1" ::"v"(voff), "s"(sbase), "s"(lds_dst) : "memory");
}
DI int arrive(unsigned cnt_addr, int lane) {
    asm volatile("s_waitcnt vmcnt(0) lgkmcnt(0)" ::: "memory");
    unsigned old = 0;
    if (lane == 0) old = __hip_atomic_fetch_add((LAS unsigned*)(uintptr_t)cnt_addr, 1u, __ATOMIC_RELAXED, __HIP_MEMORY_SCOPE_WORKGROUP);
    asm volatile("" ::: "memory");
    return __builtin_amdgcn_readfirstlane(old) & 3;
}
DI void signal(char* ws, unsigned first_line_off, int n, int lstride, int me, u64 tag, int lane) {
    if (lane < n) __hip_atomic_store((GAS u64*)(ws + first_line_off + lane * lstride * FR + me * TS), tag, __ATOMIC_RELAXED, __HIP_MEMORY_SCOPE_AGENT);
}
DI bool publish(char* ws, unsigned first_line_off, int n, int lstride, int me, u64 tag, unsigned cnt_addr, int lane) {
    const int rank = arrive(cnt_addr, lane);
    if (rank == 3) signal(ws, first_line_off, n, lstride, me, tag, lane);
    return rank == 3;
}
DI void wait_tags(const char* ws, unsigned off, int n, u64 tag, int lane) {
    const GAS u64* f = (const GAS u64*)(ws + off + (lane < n ? lane : 0) * TS);
#pragma unroll 1
    for (int spin = 0; spin < (1 << 16); ++spin) {
        const u64 v = __hip_atomic_load(f, __ATOMIC_RELAXED, __HIP_MEMORY_SCOPE_AGENT);
        if (__all(v == tag)) break;
        __builtin_amdgcn_s_sleep(1);
    }
    asm volatile("s_waitcnt vmcnt(0)" ::: "memory");
}
DI u64 read_tag(const char* ws) {
    u64 epoch;
    asm volatile("s_load_dwordx2 %0, %1, 0x0\n\ts_waitcnt lgkmcnt(0)" : "=&s"(epoch) : "s"(ws + WS_FL + FL_EPOCH) : "memory");
    return epoch + 1;
}
DI void dep_barrier(bool poller, const char* ws, unsigned off, int n, u64 tag, int lane) {
    if (__builtin_amdgcn_readfirstlane((int)poller)) wait_tags(ws, off, n, tag, lane);
    lds_barrier();
}

template <int PH> struct Cfg;
template <> struct Cfg<PH_QKV> { static constexpr int KTOT = 512, MT = 2, NP = 3, WM = 2, WN = 2, WK = 1, BK = 32, NS = 8, PA = 4, KG = 2, NCB = 8, XC = 2; };
template <> struct Cfg<PH_WO> { static constexpr int KTOT = 512, MT = 2, NP = 1, WM = 2, WN = 2, WK = 1, BK = 64, NS = 8, PA = 4, KG = 4, NCB = 8, XC = 2; };
template <> struct Cfg<PH_GLU> { static constexpr int KTOT = 512, MT = 4, NP = 4, WM = 1, WN = 4, WK = 1, BK = 32, NS = 5, PA = 2, KG = 1, NCB = 14, XC = 2; };
template <> struct Cfg<PH_DN> { static constexpr int KTOT = FF, MT = 2, NP = 1, WM = 1, WN = 2, WK = 2, BK = 64, NS = 8, PA = 4, KG = 2, NCB = 8, XC = 2; };

constexpr int AT_K = 0, AT_V = 65536, AT_ONES = 131072, AT_Q = AT_ONES + 9216, LDS_ROWQ = AT_Q + 8192, LDS_ROWG = LDS_ROWQ + 1024, LDS_CNT = LDS_ROWG + 1024, LDS_X = LDS_CNT + 16, LDS_BYTES = LDS_X + 8192;

template <int PH> struct Geo : Cfg<PH> {
    using C = Cfg<PH>;
    static constexpr int TM = C::MT / C::WM, TN = C::NP * 2 / C::WN, NT = C::KTOT / C::BK, KS = C::BK / 16, UPS = KS / C::WK / C::KG, NU = NT * UPS;
    static constexpr int ABYTES = C::MT * C::BK * 64, PBYTES = C::BK * 128, BBYTES = C::NP * PBYTES, SBYTES = ABYTES + BBYTES;
    static constexpr int GA = ABYTES / 4096, GB = BBYTES / 4096, G = GA + GB;
    static constexpr bool LN = PH == PH_QKV || PH == PH_GLU;
    static constexpr int ROWF = PH == PH_QKV ? LDS_ROWQ : LDS_ROWG;
    static constexpr int NRB = SEQ / (32 * C::MT), AUX = PH == PH_GLU ? 8 : 0;
    static constexpr unsigned WP = PH == PH_QKV ? WP_QKV : PH == PH_WO ? WP_O : PH == PH_GLU ? WP_GU : WP_D;
    static constexpr unsigned A = PH == PH_QKV ? WS_XA : PH == PH_WO ? WS_O : PH == PH_GLU ? WS_XB : WS_H;
    static_assert(C::WM * C::WN * C::WK == 4 && GA * 4096 == ABYTES && GB * 4096 == BBYTES && GA <= KS && KS % GA == 0 && GB <= 4, "pieces must divide over the waves");
    static_assert(C::NS * SBYTES <= 131072 && C::NS <= NT, "LDS budget");
    static constexpr int a_off(int k) {
        if (PH == PH_WO) return (k < 4 ? 32768 : 65536) + k * 8192;
        if (PH == PH_DN && k >= 8) return (((k - 8) / 2 + 1) % 8) * SBYTES + ((k - 8) & 1) * ABYTES;
        return ((k + (PH == PH_QKV ? 6 : PH == PH_DN ? 1 : 0)) % C::NS) * SBYTES;
    }
    static constexpr int b_off(int k) { return PH == PH_WO ? (k < 4 ? 0 : 32768) + k * 8192 : a_off(k) + ABYTES; }
    static constexpr bool held(int k) { return PH == PH_DN && k >= 8; }
    static constexpr bool uses(int k, int lo, int hi) { return (lo < a_off(k) + ABYTES && a_off(k) < hi) || (!held(k) && lo < b_off(k) + BBYTES && b_off(k) < hi); }
    static constexpr bool SWAPIN = PH == PH_DN || PH == PH_QKV;
    static DI void tile(int b, int& rb, int& cb) {
        const int x = b & 7, i = b >> 3;
        constexpr int CH = C::NCB / C::XC, RH = NRB * C::XC / 8;
        rb = (x / C::XC) * RH + i / CH; cb = (x % C::XC) * CH + i % CH;
    }
    static DI size_t packed_off(int cb, int pan, int kb) {
        return (size_t)WP + (size_t)cb * (NT * BBYTES) + (size_t)((kb * 32) / C::BK) * BBYTES + pan * PBYTES + ((kb * 32) % C::BK) * 128;
    }
};

DI unsigned tr_lane_off(int lane, int j) {
    const int hh = lane >> 5, blk = (lane >> 4) & 1, q = (lane & 15) >> 2, pp = lane & 3, pi = ((pp & 1) << 1) | (pp >> 1);
    const int ch = (4 * j + 2 * blk + (pi >> 1)) ^ (((q >> 1) & 1) << 2);
    return (8 * hh + q) * 128 + ch * 16 + 8 * (pi & 1);
}
DI int slot_col(int r32) { const int pp = (r32 >> 2) & 3; return (r32 & 16) + 4 * (((pp & 1) << 1) | (pp >> 1)) + (r32 & 3); }
DI bf16x8 corr_frag(float v, bool second_lo, int hh) {
    const unsigned h = pack_bf16(v, 0.f) & 0xffffu, l = pack_bf16(v - bf_lo(h), 0.f) & 0xffffu;
    u32x4 u; u[0] = second_lo ? (h | (l << 16)) : (h | (h << 16)); u[1] = second_lo ? h : l; u[2] = 0; u[3] = 0;
    if (hh) { u[0] = 0; u[1] = 0; }
    return __builtin_bit_cast(bf16x8, u);
}

DI f32x2 row_stat(const f32x2 (&p)[8]) {
    const f32x2 t = ((p[0] + p[1]) + (p[2] + p[3])) + ((p[4] + p[5]) + (p[6] + p[7]));
    const float mean = half_sum(t.x) * (1.0f / DM), var = fmaxf(half_sum(t.y) * (1.0f / DM) - mean * mean, 0.f);
    return f32x2{mean, __builtin_amdgcn_rsqf(var + LN_EPS)};
}

#define FRESH(ws, lds0, w, lane, b) int b = blockIdx.x; asm volatile("" : "+s"(ws), "+s"(lds0), "+s"(w), "+s"(b)); asm volatile("" : "+v"(lane))

enum { OP_A, OP_F, OP_X, OP_W, OP_N, OP_E, OP_R };
struct Sched { int n; int op[128]; int arg[128]; int pend[128]; int nn[128]; int xw; };
template <int PH, int NX> constexpr Sched make_sched() {
    using G_ = Geo<PH>; using N_ = Geo<NX>;
    Sched sc{}; int cnt = 0, ncnt = 0, lastop[64] = {}, xpos = 0; bool ndone[8] = {};
    int next = 0;
    auto push = [&](int op, int arg) { sc.op[sc.n] = op; sc.arg[sc.n] = arg; sc.pend[sc.n] = 0; ++sc.n; };
    auto item = [&](int s) -> int {
        if (next < G_::NS) { push(OP_A, next); cnt += G_::GA; lastop[next] = cnt; ++next; return G_::GA; }
        if (next == G_::NS) { ++next; if (G_::AUX) { push(OP_X, 0); cnt += G_::AUX; xpos = cnt; return G_::AUX; } }
        const int k = next - 1;
        bool ok = k < G_::NT && k <= s + 8;
        for (int j = s > 0 ? s - 1 : 0; j < k && ok; ++j) ok = !G_::uses(j, G_::a_off(k), G_::a_off(k) + G_::ABYTES);
        if (ok) { const int g = G_::held(k) ? G_::GA : G_::G; push(G_::held(k) ? OP_A : OP_F, k); cnt += g; lastop[k] = cnt; ++next; return g; }
        return 0;
    };
    auto nextb = [&](int s) {
        if (PH == PH_QKV || (PH == PH_DN && s < G_::NT && (s & 1))) return;
        for (int j = 0; j < N_::NS; ++j) {
            if (ndone[j]) continue;
            bool ok = next > G_::NT;
            for (int k = s; k < G_::NT && ok; ++k) ok = !G_::uses(k, N_::b_off(j), N_::b_off(j) + N_::BBYTES);
            if (ok) { push(OP_N, j); cnt += N_::GB; ncnt += N_::GB; ndone[j] = true; if (PH == PH_DN && s < G_::NT) return; }
        }
    };
    for (int k = 0; k < G_::PA; ++k) item(0);
    sc.xw = -1;
    for (int s = 0; s < G_::NT; ++s) {
        push(OP_W, s); sc.pend[sc.n - 1] = cnt - lastop[s]; sc.nn[sc.n - 1] = ncnt;
        if (G_::AUX && sc.xw < 0 && xpos && lastop[s] > xpos) sc.xw = s;
        for (int q = 0; q < G_::G;) { const int r = item(s); if (!r) break; q += r; }
        if (G_::WK > 1 && s == G_::NT - 1) { push(OP_R, 0); nextb(G_::NT); }
        else nextb(s > 0 ? s - 1 : 0);
    }
    push(OP_E, 0);
    nextb(G_::NT);
    return sc;
}
template <int PH> struct NextPh { static constexpr int v = PH == PH_WO ? PH_GLU : PH == PH_GLU ? PH_DN : PH_QKV; };
template <int PH> struct SchedOf { static constexpr Sched v = make_sched<PH, NextPh<PH>::v>(); };

template <int PH> DI void issue_b(const char* ws, int k, int cb, unsigned lds0, int wid, unsigned vo) {
    using G_ = Geo<PH>;
    glds_run<G_::GB>(ws + G_::WP + (size_t)cb * (G_::NT * G_::BBYTES) + k * G_::BBYTES + wid * G_::GB * 1024, vo, lds0 + G_::b_off(k) + wid * G_::GB * 1024);
}
template <int PH> DI void prefetch_b(char* ws, unsigned lds0, int wid, int lane) {
    FRESH(ws, lds0, wid, lane, bid);
    int rb, cb; Geo<PH>::tile(bid, rb, cb);
#pragma unroll
    for (int k = 0; k < Geo<PH>::NS; ++k) issue_b<PH>(ws, k, cb, lds0, wid, lane * 16);
}

DI void first_load(const char* x0, const char* wqkv, unsigned lds0, int wid, int lane) {
    using G_ = Geo<PH_QKV>;
    int rb, cb; G_::tile(blockIdx.x, rb, cb);
    const int rot = 2 * ((rb + cb) & 3);
    const char* sA = x0 + (size_t)(rb * 64 + wid * 16) * 1024;
    const unsigned vA0 = (lane >> 3) * 1024 + (((lane & 7) ^ (lane >> 4)) << 4), vA1 = 8192 + (vA0 ^ 64);
    const unsigned vB = (lane >> 3) * 3072 + (((lane & 7) ^ (((lane >> 4) & 1) << 2)) << 4);
    const char* sB[3];
#pragma unroll
    for (int g = 0; g < 3; ++g) { const int b = wid * 3 + g; sB[g] = wqkv + (size_t)(b & 3) * (8 * 3072) + (b >> 2) * 1024 + cb * 128; }
    auto issue = [&](int j) {
        const int jj = (j + rot) & 7;
        const unsigned da = lds0 + G_::a_off(2 * j) + (wid >> 1) * G_::SBYTES + (wid & 1) * 2048;
        glds_in(sA + jj * 128, vA0, da); glds_in(sA + jj * 128, vA1, da + 1024);
#pragma unroll
        for (int h = 0; h < 2; ++h)
#pragma unroll
            for (int g = 0; g < 3; ++g) glds_in(sB[g] + (2 * jj + h) * (32 * 3072), vB, lds0 + G_::b_off(2 * j + h) + (wid * 3 + g) * 1024);
    };
    struct S { static constexpr int issued(int s) { int a = 2 + s, b = s < 2 ? 4 : (s + G_::NS - 2) / 2; a = a < b ? a : b; return a < 8 ? a : 8; } };
    issue(0);
    asm volatile("s_barrier" ::: "memory");
    issue(1);
    static_for<G_::NT>([&](auto sc) {
        constexpr int s = decltype(sc)::value;
        wait_vm_barrier<8 * (S::issued(s) - 1 - s / 2) + (s & 1 ? 0 : 3)>();
#pragma unroll
        for (int j = S::issued(s); j < S::issued(s + 1); ++j) issue(j);
    });
}

template <int PH> DI void gemm_load(char* ws, int layer, unsigned lds0, int wid, int lane, bool no_next, u64 tag, bool poller) {
    using G_ = Geo<PH>;
    constexpr int NX = NextPh<PH>::v;
    constexpr int MT = G_::MT, NT = G_::NT, KS = G_::KS, GA = G_::GA;
    FRESH(ws, lds0, wid, lane, bid);
    int rb, cb, nrb, ncb; G_::tile(bid, rb, cb); Geo<NX>::tile(bid, nrb, ncb);
    const unsigned lw = layer * WS_LAYER;
    const unsigned vo = lane * 16;
    const char* sA = ws + lw + G_::A + (size_t)(rb * MT + (wid * GA) / KS) * (64 * G_::KTOT) + ((wid * GA) % KS) * 1024;
    if constexpr (G_::SWAPIN) dep_barrier(poller, ws, lw + WS_FL + PH * FL_PH + (rb * G_::NCB + cb) * FR, PH == PH_QKV ? 8 : 14, tag, lane);
    else asm volatile("s_barrier" ::: "memory");
    f32x2 stp[8];
    static_for<SchedOf<PH>::v.n>([&](auto ic) {
        constexpr int i = decltype(ic)::value, op = SchedOf<PH>::v.op[i], k = SchedOf<PH>::v.arg[i];
        if constexpr (op == OP_A || op == OP_F) glds_run<GA>(sA + k * (KS * 1024), vo, lds0 + G_::a_off(k) + wid * GA * 1024);
        if constexpr (op == OP_F) issue_b<PH>(ws, k, cb, lds0, wid, vo);
        if constexpr (op == OP_N) { if (!no_next) issue_b<NX>(ws, k, ncb, lds0, wid, vo); }
        if constexpr (op == OP_X) {
            const char* sp = ws + lw + WS_STB + (size_t)(rb * MT + wid % MT) * 4096;
            const unsigned so = (lane >> 5) * 2048 + (lane & 31) * 8;
#define SL(i, o) "\n\tglobal_load_dwordx2 %" #i ", %8, %9 offset:" #o " sc1"
            asm volatile("s_nop 4" SL(0, 0) SL(1, 256) SL(2, 512) SL(3, 768) SL(4, 1024) SL(5, 1280) SL(6, 1536) SL(7, 1792)
                         : "=&v"(stp[0]), "=&v"(stp[1]), "=&v"(stp[2]), "=&v"(stp[3]), "=&v"(stp[4]), "=&v"(stp[5]), "=&v"(stp[6]), "=&v"(stp[7]) : "v"(so), "s"(sp) : "memory");
#undef SL
        }
        if constexpr (op == OP_W) {
            if constexpr (G_::AUX && k == SchedOf<PH>::v.xw + 1) {
                asm volatile("" : "+v"(stp[0]), "+v"(stp[1]), "+v"(stp[2]), "+v"(stp[3]), "+v"(stp[4]), "+v"(stp[5]), "+v"(stp[6]), "+v"(stp[7]));
                const f32x2 st = row_stat(stp);
                if (lane < 32) ((LAS f32x2*)(uintptr_t)(lds0 + G_::ROWF))[(wid % MT) * 32 + lane] = st;
            }
            static_assert(SchedOf<PH>::v.pend[i] <= 63, "vmcnt");
            if (SchedOf<PH>::v.nn[i] && no_next) wait_vm_barrier<SchedOf<PH>::v.pend[i] - SchedOf<PH>::v.nn[i]>();
            else wait_vm_barrier<SchedOf<PH>::v.pend[i]>();
        }
        if constexpr (op == OP_R) asm volatile("s_barrier" ::: "memory");
        if constexpr (op == OP_E && (PH == PH_WO || PH == PH_GLU)) lds_barrier();
    });
}

DI void load_unit(u32x4 (&v)[4], const char* src, int ld2, int lane) {
    const GAS char* a = (const GAS char*)src + (size_t)(lane >> 3) * ld2 + (lane & 7) * 16;
#pragma unroll
    for (int i = 0; i < 4; ++i) v[i] = *(const GAS u32x4*)(a + (size_t)i * 8 * ld2);
}
DI void store_unit_packed(const u32x4 (&v)[4], rsrc_t rws, unsigned dst, int lane) {
#pragma unroll
    for (int i = 0; i < 4; ++i) st16(rws, ((lane ^ (((lane >> 4) & 1) << 2)) << 4) + i * 1024, dst, v[i]);
}
DI void unit_colsum(const u32x4 (&v)[4], unsigned lds_dst, int lane) {
    float c[8];
#pragma unroll
    for (int e = 0; e < 4; ++e) {
        c[2 * e] = (bf_lo(v[0][e]) + bf_lo(v[1][e])) + (bf_lo(v[2][e]) + bf_lo(v[3][e]));
        c[2 * e + 1] = (bf_hi(v[0][e]) + bf_hi(v[1][e])) + (bf_hi(v[2][e]) + bf_hi(v[3][e]));
    }
#pragma unroll
    for (int e = 0; e < 4; ++e) { auto r = __builtin_amdgcn_permlane32_swap(__float_as_uint(c[e]), __float_as_uint(c[e + 4]), false, false); c[e] = __uint_as_float(r[0]) + __uint_as_float(r[1]); }
#pragma unroll
    for (int e = 0; e < 2; ++e) { auto r = __builtin_amdgcn_permlane16_swap(__float_as_uint(c[e]), __float_as_uint(c[e + 2]), false, false); c[e] = __uint_as_float(r[0]) + __uint_as_float(r[1]); }
    const bool b3 = lane & 8;
    const float t = (b3 ? c[1] : c[0]) + dpp<0x128>(b3 ? c[0] : c[1]);
    *(LAS float*)(uintptr_t)(lds_dst + ((lane & 7) * 8 + (lane >> 3)) * 4) = t;
}
DI void store_colsum(unsigned lds_src, rsrc_t rws, unsigned dst, int lane) {
    const LAS float* r = (const LAS float*)(uintptr_t)lds_src + lane;
    __builtin_amdgcn_raw_buffer_store_b32(__float_as_uint((r[0] + r[64]) + (r[128] + r[192])), rws, dst + lane * 4, 0, 16);
}
DI rsrc_t make_rsrc(const void* p, unsigned bytes) { return __builtin_amdgcn_make_buffer_rsrc((void*)p, 0, bytes, 0x00020000); }

struct MkArgs { const char* x; const char* Wqkv; const char* Wo; const char* Wg; const char* Wu; const char* Wd; char* ws; char* out; };

DI void startup_load(const MkArgs& p, int cw, int lane, u32x4 (&v)[4]) {
    const int bid = blockIdx.x < 224 ? blockIdx.x : 0;
    const char* src = bid < 128 ? p.x + (size_t)(bid >> 1) * 32768 + ((bid & 1) * 4 + cw) * 128 : p.Wqkv + (size_t)((bid & 3) * 4 + cw) * 32 * 3072 + ((bid - 128) >> 2) * 128;
    load_unit(v, src, bid < 128 ? 1024 : 3072, lane);
}
DI bool startup_job(rsrc_t rws, unsigned lds0, int cw, int lane, const u32x4 (&v)[4]) {
    const int bid = blockIdx.x;
    if (bid >= 224) return false;
    if (bid < 128) {
#pragma unroll
        for (int i = 0; i < 4; ++i) {
            float sum = 0.f, sq = 0.f;
#pragma unroll
            for (int j = 0; j < 4; ++j) { const float lo = bf_lo(v[i][j]), hi = bf_hi(v[i][j]); sum += lo + hi; sq = fmaf(lo, lo, fmaf(hi, hi, sq)); }
            sum += dpp<0xB1>(sum); sq += dpp<0xB1>(sq);
            sum += dpp<0x4E>(sum); sq += dpp<0x4E>(sq);
            if ((lane & 3) == 0) *(LAS f32x2*)(uintptr_t)(lds0 + AT_Q + cw * 512 + (i * 8 + (lane >> 3)) * 8 + ((lane >> 2) & 1) * 256) = f32x2{sum, sq};
        }
    } else unit_colsum(v, lds0 + AT_Q + cw * 256, lane);
    if (arrive(lds0 + LDS_CNT, lane) != 3) return false;
    if (bid < 128) {
#pragma unroll
        for (int e = 0; e < 2; ++e) st16(rws, lane * 16 + e * 1024, WS_STA + bid * 2048, *(LAS u32x4*)(uintptr_t)(lds0 + AT_Q + e * 1024 + lane * 16));
    } else store_colsum(lds0 + AT_Q, rws, WS_CSQ + (bid - 128) * 256, lane);
    return true;
}
DI void startup_signal(char* ws, int bid, int lane, u64 tag) {
    asm volatile("s_waitcnt vmcnt(0)" ::: "memory");
    if (bid < 128) signal(ws, WS_FL + PH_QKV * FL_PH + (bid >> 2) * 8 * FR, 8, 1, bid & 3, tag, lane);
    else { const int pan = (bid - 128) >> 2; signal(ws, WS_FL + PH_QKV * FL_PH + (pan & 7) * FR, 32, 8, 4 + (pan >> 3) * 4 + (bid & 3), tag, lane); }
}

template <int PH> DI void gemm_comp(char* ws, rsrc_t rws, rsrc_t rio, int layer, unsigned lds0, int cw, int lane, u64 tag, bool& poller, const bf16x8 (&wd)[40], u32x4 (&sv)[4], unsigned& ct) {
    using G_ = Geo<PH>;
    constexpr int MT = G_::MT, WN = G_::WN, NS = G_::NS, NT = G_::NT, KS = G_::KS, KG = G_::KG, UPS = G_::UPS, NU = G_::NU, TM = G_::TM, TN = G_::TN;
    constexpr int PBYTES = G_::PBYTES;
    constexpr bool LN = G_::LN;
    constexpr int CORR_U = (PH == PH_QKV ? 15 : 10) * UPS, SIG_U = 3, POLL_U = 9, STAT_U = 12;
    static_assert(!G_::AUX || (SchedOf<PH>::v.xw >= 0 && CORR_U >= (SchedOf<PH>::v.xw + 1) * UPS), "row table must be ready");
    FRESH(ws, lds0, cw, lane, bid);
    int rb, cb; G_::tile(bid, rb, cb);
    const unsigned lw = layer * WS_LAYER;
    const int r32 = lane & 31, hh = lane >> 5, wm = cw / WN, wn = cw % WN;
    u32x4 resid[2];
    f32x2 stp[8];
    float cpart[TN][2];
    auto load_cs = [&]() {
        constexpr int NPAN = PH == PH_QKV ? 8 : 28;
#pragma unroll
        for (int t = 0; t < TN; ++t)
#pragma unroll
            for (int e = 0; e < 2; ++e)
                cpart[t][e] = __uint_as_float(__builtin_amdgcn_raw_buffer_load_b32(rws, slot_col(r32) * 4 + hh * 512 + e * 256,
                                                                                   (PH == PH_QKV ? WS_CSQ : WS_CSG) + (t * NPAN + cb * (WN / 2) + (wn >> 1)) * 1024 + (wn & 1) * 128, 16));
    };
    bf16x8 wf[TN];
    auto make_wf = [&]() {
#pragma unroll
        for (int t = 0; t < TN; ++t) wf[t] = corr_frag(half_sum(cpart[t][0] + cpart[t][1]), false, hh);
    };
    auto load_res = [&]() {
        static_assert(Cfg<PH_WO>::XC == Cfg<PH_DN>::XC, "the residual tile stays with its workgroup");
        const bool raw = PH == PH_WO && layer == 0;
        const unsigned vo = (rb * MT + wm) * 32768 + (raw ? r32 * 1024 + hh * 16 + (cb * 8 + wn * 4) * 16 : lane * 16 + lw + (PH == PH_WO ? WS_XA : WS_XB) + (cb * 8 + wn * 4) * 512);
#pragma unroll
        for (int e = 0; e < 2; ++e) resid[e] = ld16(raw ? rio : rws, vo + e * (raw ? 32 : 1024), 0);
    };
    if constexpr (PH == PH_WO) load_res();
    auto load_st = [&]() {
#pragma unroll
        for (int k = 0; k < 8; ++k) stp[k] = __builtin_bit_cast(f32x2, __builtin_amdgcn_raw_buffer_load_b64(rws, hh * 2048 + r32 * 8 + k * 256, lw + WS_STA + (rb * MT + wm) * 4096, 16));
    };
    if (PH == PH_GLU || (PH == PH_QKV && layer)) load_cs();
    LAS f32x2* rowf = (LAS f32x2*)(uintptr_t)(lds0 + G_::ROWF);
    constexpr bool KSP = G_::WK > 1;
    unsigned aoff = lds0 + (KSP ? wm * (KS * 1024 + 2048) : wm * TM * KS * 1024) + lane * 16;
    unsigned boff = lds0 + (wn >> 1) * PBYTES + tr_lane_off(lane, wn & 1) + (KSP ? wm * 4096 : 0);
    unsigned aoth = aoff + (1 - 2 * wm) * (KS * 1024), aoth2 = aoth + 65536, aown2 = aoff + 65536;
    if constexpr (KSP) asm volatile("" : "+v"(aoth), "+v"(aoth2), "+v"(aown2));
    unsigned aoff2 = aoff + 49152, boff2 = boff + 49152, aoff3 = aoff + 98304, boff3 = boff + 98304;
    asm volatile("" : "+v"(aoff2), "+v"(boff2), "+v"(aoff3), "+v"(boff3));
    unsigned aq[2][4];
    if constexpr (PH == PH_QKV) {
#pragma unroll
        for (int v = 0; v < 4; ++v) {
            aq[0][v] = layer ? aoff + (v >> 1) * G_::SBYTES + (v & 1) * 1024 : lds0 + wm * G_::SBYTES + r32 * 128 + (((2 * v + hh) ^ ((r32 >> 1) & 7)) << 4);
            aq[1][v] = aq[0][v] + 65536;
            asm volatile("" : "+v"(aq[0][v]), "+v"(aq[1][v]));
        }
    }
    constexpr int TSTR = (WN / 2) * PBYTES;

    f32x16 acc[TM][TN];
#pragma unroll
    for (int i = 0; i < TM; ++i)
#pragma unroll
        for (int t = 0; t < TN; ++t) acc[i][t] = f32x16{};
    asm volatile("" : "+v"(aoff), "+v"(boff));
    if constexpr (G_::SWAPIN) {
        if (PH != PH_QKV || layer) wait_vm_barrier<0>();
        else {
            asm volatile("" : "+v"(sv[0]), "+v"(sv[1]), "+v"(sv[2]), "+v"(sv[3]), "+v"(ct));
            poller = startup_job(rws, lds0, cw, lane, sv);
            __builtin_amdgcn_s_setprio(0);
        }
    } else dep_barrier(poller, ws, lw + WS_FL + PH * FL_PH + (rb * G_::NCB + cb) * FR, PH == PH_WO ? 32 : 16, tag, lane);
    if constexpr (PH == PH_DN) load_res();
    if (PH == PH_QKV && layer) load_st();
    if constexpr (!LN) {
#pragma unroll
        for (int r = 0; r < 16; r += 2) { acc[0][0][r] = bf_lo(resid[r >> 3][(r >> 1) & 3]); acc[0][0][r + 1] = bf_hi(resid[r >> 3][(r >> 1) & 3]); }
        asm volatile("" : "+v"(acc[0][0]));
    }
    if constexpr (PH == PH_GLU) { make_wf(); asm volatile("" : "+v"(wf[0]), "+v"(wf[1])); }
    float rstd[TM];
    u64 seen = 0;
    constexpr int NF = KG * (TM + TN), NM = KG * TM * TN;
    bf16x8 fr[2][NF];
    auto rd = [&](auto uc, auto fc) {
        constexpr int u = decltype(uc)::value, f = decltype(fc)::value;
        constexpr int ks = (u % UPS) * KG + f / (TM + TN), e = f % (TM + TN);
        constexpr int so = e < TN ? G_::b_off(u / UPS) : G_::a_off(u / UPS), hi = so / 49152, lo = so % 49152;
        if constexpr (e < TN && G_::held(u / UPS)) {}
        else if constexpr (e < TN) fr[u & 1][f] = lds_tr_frag((hi == 0 ? boff : hi == 1 ? boff2 : boff3) + lo + e * TSTR + ks * 2048);
        else if constexpr (KSP) fr[u & 1][f] = lds_b128((e == TN ? (so < 65536 ? aoff : aown2) : (so < 65536 ? aoth : aoth2)) + (so & 65535) + ks * 1024);
        else if constexpr (PH == PH_QKV) fr[u & 1][f] = lds_b128(aq[G_::a_off(u & ~1) >> 16][(u & 1) * 2 + ks] + (G_::a_off(u & ~1) & 65535));
        else fr[u & 1][f] = lds_b128((hi == 0 ? aoff : hi == 1 ? aoff2 : aoff3) + lo + ((e - TN) * KS + ks) * 1024);
    };
    auto mma = [&](auto tc, f32x16& c, const bf16x8& af, const bf16x8& bf) {
        if constexpr (PH == PH_QKV && decltype(tc)::value == 2) c = MFMA32(af, bf, c);
        else c = MFMA32(bf, af, c);
    };
    lds_barrier();
    static_for<NF>([&](auto fc) { rd(std::integral_constant<int, 0>{}, fc); });
    SBAR();
    static_for<NU>([&](auto uc) {
        constexpr int u = decltype(uc)::value;
        if constexpr (PH == PH_QKV && u == SIG_U) { if (layer == 0 && __builtin_amdgcn_readfirstlane((int)poller)) { __builtin_amdgcn_s_setprio(3); startup_signal(ws, bid, lane, tag); __builtin_amdgcn_s_setprio(0); } SBAR(); }
        if constexpr (PH == PH_QKV && u == POLL_U) {
            if (layer == 0) {
                __builtin_amdgcn_s_setprio(3);
                seen = __hip_atomic_load((const GAS u64*)(ws + WS_FL + PH * FL_PH + (rb * G_::NCB + cb) * FR + (lane & 15) * TS), __ATOMIC_RELAXED, __HIP_MEMORY_SCOPE_AGENT);
                __builtin_amdgcn_s_setprio(0);
            }
            SBAR();
        }
        if constexpr (PH == PH_QKV && u == STAT_U) {
            if (layer == 0) {
                __builtin_amdgcn_s_setprio(3);
                if (!__all(seen == tag)) wait_tags(ws, WS_FL + PH * FL_PH + (rb * G_::NCB + cb) * FR, 16, tag, lane);
                asm volatile("" ::: "memory");
                load_st(); load_cs();
                __builtin_amdgcn_s_setprio(0);
            }
            SBAR();
        }
        if constexpr (LN && u == CORR_U) {
            if constexpr (PH == PH_QKV) make_wf();
#pragma unroll
            for (int i = 0; i < TM; ++i) {
                f32x2 st;
                if constexpr (PH == PH_QKV) {
                    st = row_stat(stp);
                    if (hh == 0) rowf[cw * 32 + r32] = st;
                } else st = rowf[(wm * TM + i) * 32 + r32];
                rstd[i] = st.y;
                const bf16x8 xf = corr_frag(-st.x, true, hh);
                static_for<TN>([&](auto tc) { mma(tc, acc[i][decltype(tc)::value], xf, wf[decltype(tc)::value]); });
            }
            SBAR();
        }
        if constexpr (u + 1 < NU && (u + 1) % UPS == 0) { asm volatile("s_barrier" ::: "memory"); SBAR(); }
        static_for<NM>([&](auto mc) {
            constexpr int m = decltype(mc)::value, g = m / (TM * TN), i = (m % (TM * TN)) / TN, t = m % TN;
            if constexpr (G_::held(u / UPS)) mma(std::integral_constant<int, t>{}, acc[i][t], fr[u & 1][g * (TM + TN) + TN + i], wd[(u - 8) * 2 + g]);
            else mma(std::integral_constant<int, t>{}, acc[i][t], fr[u & 1][g * (TM + TN) + TN + i], fr[u & 1][g * (TM + TN) + t]);
            if constexpr (u + 1 < NU) {
                SBAR();
                constexpr int PER = PH == PH_GLU ? 1 : 4;
                static_for<PER>([&](auto kc) {
                    constexpr int f = m * PER + decltype(kc)::value;
                    if constexpr (f < NF) rd(std::integral_constant<int, u + 1>{}, std::integral_constant<int, f>{});
                });
                SBAR();
            }
        });
    });

    if constexpr (KSP) {
        auto xbuf = [&](int w) { return lds0 + (w < 2 ? AT_Q : LDS_X) + (w & 1) * 4096 + lane * 16; };
#pragma unroll
        for (int g = 0; g < 4; ++g) *(LAS f32x4*)(uintptr_t)(xbuf(cw) + g * 1024) = f32x4{acc[1][0][4 * g], acc[1][0][4 * g + 1], acc[1][0][4 * g + 2], acc[1][0][4 * g + 3]};
        lds_barrier();
#pragma unroll
        for (int g = 0; g < 4; ++g) {
            const f32x4 o = *(LAS f32x4*)(uintptr_t)(xbuf(cw ^ 2) + g * 1024);
#pragma unroll
            for (int j = 0; j < 4; ++j) acc[0][0][4 * g + j] += o[j];
        }
    }
    if constexpr (PH == PH_QKV) {
        const int tile = rb * MT + wm;
        const int head0 = cb * 4 + wn * 2;
        const float rq = rstd[0] * QSCALE, rk = rstd[0];
#pragma unroll
        for (int e = 0; e < 2; ++e) {
            u32x4 q, k;
#pragma unroll
            for (int j = 0; j < 4; ++j) {
                q[j] = pack_bf16(rq * acc[0][0][8 * e + 2 * j], rq * acc[0][0][8 * e + 2 * j + 1]);
                k[j] = pack_bf16(rk * acc[0][1][8 * e + 2 * j], rk * acc[0][1][8 * e + 2 * j + 1]);
            }
            st16(rws, lane * 16, lw + WS_KV + ((head0 + e) * 128 + tile) * 1024, k);
            st16(rws, lane * 16, lw + WS_Q + ((head0 + e) * 64 + tile) * 1024, q);
        }
#pragma unroll
        for (int e = 0; e < 2; ++e) {
            u32x4 o;
#pragma unroll
            for (int j = 0; j < 4; ++j) {
                const f32x4 st = *(LAS f32x4*)&rowf[cw * 32 + 16 * e + 8 * (j >> 1) + 4 * hh + 2 * (j & 1)];
                o[j] = pack_bf16(st[1] * acc[0][2][8 * e + 2 * j], st[3] * acc[0][2][8 * e + 2 * j + 1]);
            }
            st16(rws, (r32 >> 4) * 131072 + e * 512 + hh * 256 + (r32 & 15) * 16, lw + WS_KV + (head0 * 128 + 64 + tile) * 1024, o);
        }
        poller = publish(ws, lw + WS_FL + PH_ATT * FL_PH + cb * 32 * FR, 32, 1, rb, tag, lds0 + LDS_CNT, lane);
    } else if constexpr (PH == PH_GLU) {
#pragma unroll
        for (int i = 0; i < TM; ++i) {
            const float r2 = rstd[i] * rstd[i];
#pragma unroll
            for (int e = 0; e < 2; ++e) {
                u32x4 o;
#pragma unroll
                for (int j = 0; j < 4; ++j) {
                    const int r = 8 * e + 2 * j;
                    o[j] = pack_bf16(fmaxf(r2 * acc[i][0][r], 0.f) * acc[i][1][r], fmaxf(r2 * acc[i][0][r + 1], 0.f) * acc[i][1][r + 1]);
                }
                st16(rws, lane * 16 + e * 1024, lw + WS_H + (rb * MT + i) * (64 * FF) + (cb * 16 + wn * 4) * 512, o);
            }
        }
        lds_barrier();
        poller = publish(ws, lw + WS_FL + PH_DN * FL_PH + rb * 16 * FR, 16, 1, cb, tag, lds0 + LDS_CNT, lane);
    } else {
        const bool last = PH == PH_DN && layer == 1;
        const unsigned rbo = (rb * MT + wm) * 32768;
        u32x4 o[2];
#pragma unroll
        for (int e = 0; e < 2; ++e)
#pragma unroll
            for (int j = 0; j < 4; ++j) o[e][j] = pack_bf16(acc[0][0][8 * e + 2 * j], acc[0][0][8 * e + 2 * j + 1]);
        if (last) {
#pragma unroll
            for (int e = 0; e < 2; ++e) __builtin_amdgcn_raw_buffer_store_b128(o[e], rio, r32 * 1024 + hh * 16 + rbo + (cb * 8 + wn * 4) * 16 + e * 32, 0, 0);
        } else {
#pragma unroll
            for (int e = 0; e < 2; ++e) st16(rws, lane * 16 + e * 1024, (PH == PH_WO ? lw + WS_XB : WS_LAYER + WS_XA) + rbo + (cb * 8 + wn * 4) * 512, o[e]);
        }
        if (!last) {
            float sum[2] = {0.f, 0.f}, sq[2] = {0.f, 0.f};
#pragma unroll
            for (int r = 0; r < 16; ++r) { sum[r & 1] += acc[0][0][r]; sq[r & 1] = fmaf(acc[0][0][r], acc[0][0][r], sq[r & 1]); }
            const float a = half_sum(sum[0] + sum[1]), b = half_sum(sq[0] + sq[1]);
            if (hh == 0) __builtin_amdgcn_raw_buffer_store_b64(u32x2{__float_as_uint(a), __float_as_uint(b)}, rws,
                                                               r32 * 8 + (PH == PH_WO ? lw + WS_STB : WS_LAYER + WS_STA) + (rb * MT + wm) * 4096 + cb * 512 + wn * 256, 0, 16);
        }
        if constexpr (!KSP) lds_barrier();
        if (!last) {
            if constexpr (PH == PH_WO) poller = publish(ws, lw + WS_FL + PH_GLU * FL_PH + (rb >> 1) * 14 * FR, 14, 1, (rb & 1) * 8 + cb, tag, lds0 + LDS_CNT, lane);
            else poller = publish(ws, WS_LAYER + WS_FL + PH_QKV * FL_PH + rb * 8 * FR, 8, 1, cb, tag, lds0 + LDS_CNT, lane);
        }
    }
}

#define AT_E(C, i) "v_exp_f32 v[" #C "+" #i "], v[" #C "+" #i "]\n\t"
#define AT_C(P, C, i) "v_cvt_pk_bf16_f32 v[" #P "], v[" #C "+" #i "], v[" #C "+" #i "+1]\n\t"
#define AT_HALF(C, O, V0, PRE, MID, POST)                                                                         \
    AT_E(C, 0) AT_E(C, 1) PRE AT_E(C, 2) AT_E(C, 3) AT_E(C, 4) AT_E(C, 5) MID                                     \
    AT_E(C, 6) AT_E(C, 7) AT_C(108, C, 0) AT_C(109, C, 2) AT_C(110, C, 4) AT_E(C, 8) AT_C(111, C, 6) AT_E(C, 9) AT_E(C, 10) \
    "v_mfma_f32_32x32x16_bf16 " O ", v[" #V0 ":" #V0 "+3], v[108:111], " O "\n\t" POST                            \
    AT_E(C, 11) AT_E(C, 12) AT_E(C, 13) AT_E(C, 14) AT_E(C, 15)                                                   \
    AT_C(112, C, 8) AT_C(113, C, 10) AT_C(114, C, 12) AT_C(115, C, 14)
#define AT_TILE(U, V0C, V0N, SYNC)                                                                                \
    AT_HALF(64, "%[o0]", V0C,                                                                                     \
            "v_mfma_f32_32x32x16_bf16 %[o1], v[104:107], v[112:115], %[o1]\n\t"                                   \
            "ds_read_b128 v[104:107], %[va] offset:" #U "*1024+512\n\t",                                          \
            "v_mfma_f32_32x32x16_bf16 v[80:95], v[96:99], %[q1], %[nm1]\n\t" SYNC                                 \
            "ds_read_b128 v[96:99], %[ka] offset:" #U "*1024+1024\n\t"                                            \
            "ds_read_b128 v[" #V0N ":" #V0N "+3], %[va] offset:" #U "*1024+1024\n\t",                             \
            "s_waitcnt lgkmcnt(2)\n\t")                                                                           \
    AT_HALF(80, "%[o1]", V0C,                                                                                     \
            "v_mfma_f32_32x32x16_bf16 %[o0], v[104:107], v[112:115], %[o0]\n\t"                                   \
            "s_waitcnt lgkmcnt(0)\n\t",                                                                           \
            "v_mfma_f32_32x32x16_bf16 v[64:79], v[96:99], %[q0], %[nm0]\n\t", "")

DI f32x16 neg_tile_max(const bf16x8& k0, const bf16x8& q) {
    const f32x16 s0 = MFMA32(k0, q, f32x16{});
    float m = s0[0];
#pragma unroll
    for (int r = 1; r < 16; ++r) m = fmaxf(m, s0[r]);
    m = half_max(m);
    f32x16 n;
#pragma unroll
    for (int r = 0; r < 16; ++r) n[r] = -m;
    return n;
}
DI f32x16 attn_exact(const bf16x8& q, rsrc_t rws, unsigned kv, unsigned ones, int lane) {
    const bool vlane = (lane & 31) < 16;
    const bf16x8 pad = lds_b128(ones);
    float m = -INFINITY;
    f32x16 o = f32x16{};
#pragma unroll 1
    for (int t = 0; t < 64; ++t) {
        const bf16x8 kf = __builtin_bit_cast(bf16x8, ld16(rws, lane * 16, kv + t * 1024));
        const unsigned vo = (lane >> 5) * 256 + (lane & 15) * 16;
        bf16x8 v0 = __builtin_bit_cast(bf16x8, ld16(rws, vo, kv + 65536 + t * 1024)), v1 = __builtin_bit_cast(bf16x8, ld16(rws, vo + 512, kv + 65536 + t * 1024));
        if (!vlane) { v0 = pad; v1 = pad; }
        const f32x16 s = MFMA32(kf, q, f32x16{});
        float tm = s[0];
#pragma unroll
        for (int r = 1; r < 16; ++r) tm = fmaxf(tm, s[r]);
        const float mn = fmaxf(m, half_max(tm));
        const float alpha = __builtin_amdgcn_exp2f(m - mn);
        m = mn;
#pragma unroll
        for (int r = 0; r < 16; ++r) o[r] *= alpha;
        u32x4 p0, p1;
#pragma unroll
        for (int j = 0; j < 4; ++j) {
            p0[j] = pack_bf16(__builtin_amdgcn_exp2f(s[2 * j] - m), __builtin_amdgcn_exp2f(s[2 * j + 1] - m));
            p1[j] = pack_bf16(__builtin_amdgcn_exp2f(s[8 + 2 * j] - m), __builtin_amdgcn_exp2f(s[8 + 2 * j + 1] - m));
        }
        o = MFMA32(v0, __builtin_bit_cast(bf16x8, p0), o);
        o = MFMA32(v1, __builtin_bit_cast(bf16x8, p1), o);
    }
    return o;
}

DI void attn_load(const MkArgs& p, rsrc_t rws, int layer, unsigned lds0, int wid, int lane, u64 tag, bf16x8 (&wd)[40]) {
    char* ws = p.ws;
    FRESH(ws, lds0, wid, lane, bid);
    const int x = bid & 7, idx = bid >> 3, head = x * 4 + (idx >> 3), qb = idx & 7;
    const unsigned lw = layer * WS_LAYER;
    const unsigned vo = lane * 16;
    const char* src = ws + lw + WS_KV + (size_t)head * 131072 + wid * 2048;
    auto chunk = [&](int c) {
        const char* s = src + ((qb + c) & 7) * 8192;
        glds_run<2>(s, vo, lds0 + AT_K + c * 8192 + wid * 2048);
        glds_run<2>(s + 65536, vo, lds0 + AT_V + c * 8192 + wid * 2048);
    };
    asm volatile("s_barrier" ::: "memory");
    glds_run<2>(ws + lw + WS_Q + (size_t)(head * 64 + qb * 8 + wid * 2) * 1024, vo, lds0 + AT_Q + wid * 2048);
    chunk(0);
    wait_vm_barrier<0>();
    wait_tags(ws, lw + WS_FL + PH_ATT * FL_PH + (head * 8 + qb) * FR, 32, tag, lane);
    chunk(1);
    chunk(2); chunk(3); chunk(4); chunk(5); chunk(6); chunk(7);
    wait_vm_barrier<0>();
    if (layer == 0) {
#pragma unroll 1
        for (int u = bid * 4 + wid; u < 1536; u += 1024) {
            const char* s; size_t dst; int ld2;
            if (u < 896) { const int mat = u >= 448, v = u - mat * 448, pan = v >> 4, kb = v & 15;
                s = (mat ? p.Wu : p.Wg) + (size_t)kb * 32 * (FF * 2) + pan * 128; ld2 = FF * 2; dst = Geo<PH_GLU>::packed_off(pan >> 1, mat * 2 + (pan & 1), kb); }
            else if (u < 1024) { const int v = u - 896, pan = v & 7, kb = v >> 3;
                s = p.Wo + (size_t)kb * 32 * 1024 + pan * 128; ld2 = 1024; dst = Geo<PH_WO>::packed_off(pan, 0, kb); }
            else if (u < 1152) { const int v = u - 1024, pan = v & 7, kb = v >> 3;
                s = p.Wd + (size_t)kb * 32 * 1024 + pan * 128; ld2 = 1024; dst = Geo<PH_DN>::packed_off(pan, 0, kb); }
            else { const int v = u - 1152, pan = v >> 4, kb = v & 15;
                s = p.Wqkv + (size_t)kb * 32 * 3072 + pan * 128; ld2 = 3072; dst = Geo<PH_QKV>::packed_off(pan & 7, pan >> 3, kb); }
            u32x4 v[4];
            load_unit(v, s, ld2, lane);
            store_unit_packed(v, rws, (unsigned)dst, lane);
            if (u < 896) unit_colsum(v, lds0 + LDS_ROWG + wid * 256, lane);
        }
        if (arrive(lds0 + LDS_CNT + 4, lane) == 3) {
            if (bid < 224) { store_colsum(lds0 + LDS_ROWG, rws, WS_CSG + bid * 256, lane); asm volatile("s_waitcnt vmcnt(0)" ::: "memory"); }
            if (lane == 0) __hip_atomic_store((GAS u64*)(ws + WS_FL + FL_PK + bid * 8), tag, __ATOMIC_RELAXED, __HIP_MEMORY_SCOPE_AGENT);
            __builtin_amdgcn_s_sleep(24);
#pragma unroll 1
            for (int i = 0; i < 4; ++i) wait_tags(ws, WS_FL + FL_PK + ((bid + i) & 3) * 512, 64, tag, lane);
        }
    }
    lds_barrier();
    const unsigned own = lds0 + Geo<PH_WO>::b_off(0) + (wid >> 1) * 65536 + (wid & 1) * 16384;
    if (layer == 0) {
        int rb, cb; Geo<PH_DN>::tile(bid, rb, cb);
        const char* sw = p.Wd + cb * 128 + (wid & 1) * 64 + (wid >> 1) * 32768;
        const unsigned vw = (lane >> 2) * 1024 + (lane & 3) * 16;
        const int pp = lane & 3;
        const unsigned ra = own + (8 * (lane >> 5) + ((lane & 15) >> 2)) * 64 + ((lane >> 4) & 1) * 32 + ((((pp & 1) << 1) | (pp >> 1)) << 3);
        typedef short v4i16_t __attribute__((ext_vector_type(4)));
#pragma unroll
        for (int r0 = 0; r0 < 40; r0 += 16) {
#pragma unroll
            for (int i = r0; i < r0 + 16 && i < 40; ++i) glds_in(sw + (size_t)((8 + (i >> 1)) * 64 + (i & 1) * 16) * 1024, vw, own + (i - r0) * 1024);
            asm volatile("s_waitcnt vmcnt(0)" ::: "memory");
#pragma unroll
            for (int i = r0; i < r0 + 16 && i < 40; ++i) {
                const v4i16_t lo = __builtin_amdgcn_ds_read_tr16_b64_v4i16((LAS v4i16_t*)(uintptr_t)(ra + (i - r0) * 1024));
                const v4i16_t hi = __builtin_amdgcn_ds_read_tr16_b64_v4i16((LAS v4i16_t*)(uintptr_t)(ra + (i - r0) * 1024 + 256));
                wd[i] = __builtin_shufflevector(lo, hi, 0, 1, 2, 3, 4, 5, 6, 7);
            }
#pragma unroll
            for (int i = r0; i < r0 + 16 && i < 40; ++i) asm volatile("" : "+v"(wd[i])::"memory");
        }
    }
    {
        int rb, cb; Geo<PH_WO>::tile(bid, rb, cb);
        static_assert(Geo<PH_WO>::b_off(2) == 16384 && Geo<PH_WO>::b_off(4) == 65536 && Geo<PH_WO>::b_off(6) == 81920, "");
#pragma unroll
        for (int i = 0; i < 4; ++i) glds_run<4>(ws + WP_O + cb * 65536 + wid * 16384 + i * 4096, vo, own + i * 4096);
    }
}

DI void attn_comp(char* ws, rsrc_t rws, int layer, unsigned lds0, int cw, int lane, u64 tag, bool& poller) {
    FRESH(ws, lds0, cw, lane, bid);
    const int x = bid & 7, idx = bid >> 3, head = x * 4 + (idx >> 3), qb = idx & 7;
    const unsigned lw = layer * WS_LAYER;
    dep_barrier(poller, ws, lw + WS_FL + PH_ATT * FL_PH + (head * 8 + qb) * FR + qb * 4 * TS, 4, tag, lane);
    const int r32 = lane & 31, hh = lane >> 5;
    const unsigned ka0 = lds0 + AT_K + lane * 16;
    const bool vlane = r32 < 16;
    const unsigned va0 = lds0 + (vlane ? AT_V : AT_ONES) + hh * 256 + (r32 & 15) * 16;
    const unsigned vstep = vlane ? 8192u : 0u;

    lds_barrier();
    const bf16x8 q0 = lds_b128(lds0 + AT_Q + cw * 2048 + lane * 16), q1 = lds_b128(lds0 + AT_Q + cw * 2048 + 1024 + lane * 16);
    const bf16x8 k0 = lds_b128(ka0);
    const f32x16 nm0 = neg_tile_max(k0, q0), nm1 = neg_tile_max(k0, q1);
    f32x16 o0 = f32x16{}, o1 = f32x16{};
    {
        unsigned ka = ka0, va = va0; int cnt, tmp;
        asm volatile(
            "ds_read_b128 v[96:99], %[ka]\n\t"
            "ds_read_b128 v[100:103], %[va]\n\t"
            "v_mov_b32 v104, 0\n\tv_mov_b32 v105, 0\n\tv_mov_b32 v106, 0\n\tv_mov_b32 v107, 0\n\t"
            "v_mov_b32 v112, 0\n\tv_mov_b32 v113, 0\n\tv_mov_b32 v114, 0\n\tv_mov_b32 v115, 0\n\t"
            "s_mov_b32 %[cnt], 0\n\t"
            "s_waitcnt lgkmcnt(0)\n\t"
            "v_mfma_f32_32x32x16_bf16 v[64:79], v[96:99], %[q0], %[nm0]\n\t"
            "s_nop 12\n\t"
            "1:\n\t"
            AT_TILE(0, 100, 116, "") AT_TILE(1, 116, 100, "") AT_TILE(2, 100, 116, "") AT_TILE(3, 116, 100, "")
            AT_TILE(4, 100, 116, "") AT_TILE(5, 116, 100, "") AT_TILE(6, 100, 116, "")
            AT_TILE(7, 116, 100, "s_and_b32 %[tmp], %[cnt], 3\n\ts_cbranch_scc1 2f\n\ts_barrier\n\t2:\n\t")
            "v_add_u32 %[ka], 0x2000, %[ka]\n\t"
            "v_add_u32 %[va], %[vstep], %[va]\n\t"
            "s_add_u32 %[cnt], %[cnt], 1\n\t"
            "s_cmp_lt_u32 %[cnt], 8\n\t"
            "s_cbranch_scc1 1b\n\t"
            "v_mfma_f32_32x32x16_bf16 %[o1], v[104:107], v[112:115], %[o1]\n\t"
            "s_nop 15\n\t"
            : [o0] "+v"(o0), [o1] "+v"(o1), [ka] "+v"(ka), [va] "+v"(va), [cnt] "=&s"(cnt), [tmp] "=&s"(tmp)
            : [q0] "v"(q0), [q1] "v"(q1), [nm0] "v"(nm0), [nm1] "v"(nm1), [vstep] "v"(vstep)
            : "memory", "scc", "v64", "v65", "v66", "v67", "v68", "v69", "v70", "v71", "v72", "v73", "v74", "v75", "v76", "v77", "v78", "v79",
              "v80", "v81", "v82", "v83", "v84", "v85", "v86", "v87", "v88", "v89", "v90", "v91", "v92", "v93", "v94", "v95",
              "v96", "v97", "v98", "v99", "v100", "v101", "v102", "v103", "v104", "v105", "v106", "v107",
              "v108", "v109", "v110", "v111", "v112", "v113", "v114", "v115", "v116", "v117", "v118", "v119");
    }
    if (__builtin_expect(__any(!(o0[8] < LMAX) || !(o1[8] < LMAX)), 0)) {
        o0 = attn_exact(q0, rws, lw + WS_KV + head * 131072, lds0 + AT_ONES + (r32 & 15) * 16, lane);
        o1 = attn_exact(q1, rws, lw + WS_KV + head * 131072, lds0 + AT_ONES + (r32 & 15) * 16, lane);
    }
    const float i0 = __builtin_amdgcn_rcpf(o0[8]), i1 = __builtin_amdgcn_rcpf(o1[8]);
    u32x4 w0, w1;
#pragma unroll
    for (int j = 0; j < 4; ++j) { w0[j] = pack_bf16(o0[2 * j] * i0, o0[2 * j + 1] * i0); w1[j] = pack_bf16(o1[2 * j] * i1, o1[2 * j + 1] * i1); }
    const int qtile = qb * 8 + cw * 2;
    st16(rws, lane * 16, lw + WS_O + qtile * 32768 + head * 1024, w0);
    st16(rws, lane * 16, lw + WS_O + (qtile + 1) * 32768 + head * 1024, w1);
    poller = publish(ws, lw + WS_FL + PH_WO * FL_PH + qb * 32 * FR, 32, 1, head, tag, lds0 + LDS_CNT, lane);
}

__global__ __launch_bounds__(512, 2) void mk_kernel(MkArgs p) {
    extern __shared__ __attribute__((aligned(16))) char smem[];
    const unsigned lds0 = (unsigned)(uintptr_t)(LAS char*)smem;
    const int tid = threadIdx.x, lane = tid & 63, wid = __builtin_amdgcn_readfirstlane(tid >> 6);
    const rsrc_t rws = make_rsrc(p.ws, WS_END);
    const bool glu = (blockIdx.x >> 3) < 28;
    const char* code;
    asm volatile("s_getpc_b64 %0" : "=s"(code));
    if (wid < 4) {
        first_load(p.x, p.Wqkv, lds0, wid, lane);
        const u64 tag = read_tag(p.ws);
        const rsrc_t rout = make_rsrc(p.out, SEQ * DM * 2);
        bf16x8 wd[40];
        u32x4 nosv[4];
        unsigned noct;
        bool poller = false;
#pragma unroll 1
        for (int layer = 0; layer < 2; ++layer) {
            if (layer) gemm_load<PH_QKV>(p.ws, layer, lds0, wid, lane, false, tag, poller);
            attn_load(p, rws, layer, lds0, wid, lane, tag, wd);
            gemm_load<PH_WO>(p.ws, layer, lds0, wid, lane, !glu, tag, false);
            if (glu) gemm_load<PH_GLU>(p.ws, layer, lds0, wid, lane, false, tag, false);
            else prefetch_b<PH_DN>(p.ws, lds0, wid, lane);
            gemm_comp<PH_DN>(p.ws, rws, rout, layer, lds0, wid, lane, tag, poller, wd, nosv, noct);
        }
    } else {
        const rsrc_t rx = make_rsrc(p.x, SEQ * DM * 2);
        bf16x8 none[40];
        __builtin_amdgcn_s_setprio(3);
        unsigned ct = 0;
        if (wid == 4 && blockIdx.x < 64 && lane < 58) ct = *(const GAS unsigned*)(code + (blockIdx.x >> 3) * (58 * 128) + lane * 128);
        u32x4 sv[4];
        startup_load(p, wid - 4, lane, sv);
        {
            const unsigned ov = ((tid & 15) == 0 || (tid & 15) == 4) ? 0x3f803f80u : 0u;
            *(LAS u32x4*)(uintptr_t)(lds0 + AT_ONES + (tid - 256) * 16) = u32x4{ov, ov, ov, ov};
            *(LAS u32x4*)(uintptr_t)(lds0 + AT_ONES + tid * 16) = u32x4{ov, ov, ov, ov};
            if (tid < 320) *(LAS u32x4*)(uintptr_t)(lds0 + AT_ONES + (tid + 256) * 16) = u32x4{ov, ov, ov, ov};
            if (tid == 256) *(LAS u32x2*)(uintptr_t)(lds0 + LDS_CNT) = u32x2{0, 0};
        }
        lds_barrier();
        bool poller = false;
        const u64 tag = read_tag(p.ws);
#pragma unroll 1
        for (int layer = 0; layer < 2; ++layer) {
            gemm_comp<PH_QKV>(p.ws, rws, rx, layer, lds0, wid - 4, lane, tag, poller, none, sv, ct);
            attn_comp(p.ws, rws, layer, lds0, wid - 4, lane, tag, poller);
            gemm_comp<PH_WO>(p.ws, rws, rx, layer, lds0, wid - 4, lane, tag, poller, none, sv, ct);
            if (glu) gemm_comp<PH_GLU>(p.ws, rws, rx, layer, lds0, wid - 4, lane, tag, poller, none, sv, ct);
            gemm_load<PH_DN>(p.ws, layer, lds0, wid - 4, lane, layer == 1, tag, poller);
        }
        if (blockIdx.x == 0 && tid == 256) __hip_atomic_store((GAS u64*)(p.ws + WS_FL + FL_EPOCH), tag, __ATOMIC_RELAXED, __HIP_MEMORY_SCOPE_AGENT);
    }
}

extern "C" void kernel_launch(void* const* d_in, const int* in_sizes, int n_in, void* d_out, int out_size, void* d_ws, size_t ws_size,
                              hipStream_t stream) {
    (void)hipFuncSetAttribute((const void*)mk_kernel, hipFuncAttributeMaxDynamicSharedMemorySize, LDS_BYTES);
    mk_kernel<<<256, 512, LDS_BYTES, stream>>>(MkArgs{(const char*)d_in[0], (const char*)d_in[1], (const char*)d_in[2], (const char*)d_in[4], (const char*)d_in[5],
                                                      (const char*)d_in[6], (char*)d_ws, (char*)d_out});
}
```
